```python
import torch

def _softmax(x, axis=-1):
    return torch.softmax(x, dim=axis)
def _gelu(x):
    return torch.nn.functional.gelu(x, approximate="tanh")
def _silu(x):
    return torch.nn.functional.silu(x)
def _sigmoid(x):
    return torch.sigmoid(x)
def _layernorm(x, axis=-1, eps=1e-5):
    m = x.mean(dim=axis, keepdim=True)
    v = x.var(dim=axis, keepdim=True, unbiased=False)
    return (x - m) / torch.sqrt(v + eps)
def _scatter_add(tgt, idx, src, axis=0):
    return tgt.clone().index_add_(axis, idx, src)
def _segment_sum(src, seg, n):
    out = torch.zeros((n,) + src.shape[1:], dtype=src.dtype)
    return out.index_add_(0, seg, src)
def _pad(x, pw):
    flat = tuple(v for pair in reversed(pw) for v in pair)
    return torch.nn.functional.pad(x, flat)

kernel_name = 'sequential_preLN__sliding_block__geglu_ffn__prompt'

def setup_inputs():

    _w = (lambda a: (lambda t: t.to(torch.bfloat16) if t.is_floating_point() else t)(torch.as_tensor(a)))
    def _A_setup():
        B, S, D = 1, 2048, 512
        return {
            "x": _w((lambda *s: torch.randn(*s, dtype=torch.bfloat16) * 0.05)(B, S, D)),
            "wqkv": _w((lambda *s: torch.randn(*s, dtype=torch.bfloat16) * 0.05)(D, 3 * D)),
            "wo": _w((lambda *s: torch.randn(*s, dtype=torch.bfloat16) * 0.05)(D, D)),
        }
    _A_in = _A_setup()

    def _B_setup():
        B, S, D, F = 1, 2048, 512, 1376
        return {
            "x": _w((lambda *s: torch.randn(*s, dtype=torch.bfloat16) * 0.05)(B, S, D)),
            "w_gate": _w((lambda *s: torch.randn(*s, dtype=torch.bfloat16) * 0.05)(D, F)),
            "w_up": _w((lambda *s: torch.randn(*s, dtype=torch.bfloat16) * 0.05)(D, F)),
            "w_down": _w((lambda *s: torch.randn(*s, dtype=torch.bfloat16) * 0.05)(F, D)),
        }
    _B_in = _B_setup()

    return {
        "A_x": _A_in["x"],
        "A_wqkv": _A_in["wqkv"],
        "A_wo": _A_in["wo"],
        "B_x": _B_in["x"],
        "B_w_gate": _B_in["w_gate"],
        "B_w_up": _B_in["w_up"],
        "B_w_down": _B_in["w_down"],
    }

def reference(A_x, A_wqkv, A_wo, B_x, B_w_gate, B_w_up, B_w_down):
    def _A_ref(x, wqkv, wo):
        B, S, D = x.shape
        H = 32; Dh = D // H; W = 256
        qkv = (lambda x, *d: x.permute(*d))((x @ wqkv).reshape(B, S, 3, H, Dh), 2, 0, 3, 1, 4)
        q, k, v = qkv[0], qkv[1], qkv[2]
        scores = torch.einsum("bhqd,bhkd->bhqk", q, k) * (Dh ** -0.5)
        idx = torch.arange(S)
        mask = (idx[:, None] - idx[None, :] >= 0) & (idx[:, None] - idx[None, :] < W)
        scores = torch.where(mask[None, None], scores, -1e9)
        p = _softmax(scores, axis=-1)
        o = torch.einsum("bhqk,bhkd->bhqd", p, v)
        return (lambda x, *d: x.permute(*d))(o, 0, 2, 1, 3).reshape(B, S, D) @ wo

    def _B_ref(x, w_gate, w_up, w_down):
        return (_gelu(x @ w_gate) * (x @ w_up)) @ w_down

    x = A_x
    h = x + _A_ref(_layernorm(x, axis=-1), A_wqkv, A_wo)
    h2 = h + _B_ref(_layernorm(h, axis=-1), B_w_gate, B_w_up, B_w_down)
    return h2
```

```cpp
#include <hip/hip_runtime.h>
#include <hip/hip_bf16.h>
#include <stdint.h>
typedef unsigned short u16;
__device__ __forceinline__ float bf2f(u16 b){ return __uint_as_float(((unsigned)b)<<16); }
__device__ __forceinline__ u16 f2bf(float f){ unsigned u=__float_as_uint(f); u += 0x7FFFu + ((u>>16)&1u); return (u16)(u>>16); }
constexpr int S=2048, D=512, F=1376, H=32, DH=16, W=256;
__global__ void ln_k(const u16* x, u16* y){
  int r=blockIdx.x; int t=threadIdx.x;
  float v[8]; float s=0;
  for(int j=0;j<8;j++){ v[j]=bf2f(x[r*D+t*8+j]); s+=v[j]; }
  for(int m=32;m>=1;m>>=1) s+=__shfl_xor(s,m);
  float mean=s/D; float q=0;
  for(int j=0;j<8;j++){ float d=v[j]-mean; q+=d*d; }
  for(int m=32;m>=1;m>>=1) q+=__shfl_xor(q,m);
  float rs=1.0f/sqrtf(q/D+1e-5f);
  for(int j=0;j<8;j++) y[r*D+t*8+j]=f2bf((v[j]-mean)*rs);
}
__global__ void gemm_k(const u16* A,const u16* B,u16* C,const u16* res,int M,int N,int K){
  int n=blockIdx.x*64+threadIdx.x; int m0=blockIdx.y*8;
  if(n>=N) return;
  float acc[8]={0,0,0,0,0,0,0,0};
  for(int k=0;k<K;k++){ float b=bf2f(B[(size_t)k*N+n]);
    for(int i=0;i<8;i++) acc[i]+=bf2f(A[(size_t)(m0+i)*K+k])*b; }
  for(int i=0;i<8;i++){ float v=acc[i]; if(res){ v=bf2f(f2bf(v))+bf2f(res[(size_t)(m0+i)*N+n]); } C[(size_t)(m0+i)*N+n]=f2bf(v);}
}
__global__ void attn_k(const u16* qkv,u16* o){
  int idx=blockIdx.x*64+threadIdx.x; int q=idx%S; int h=idx/S;
  float qv[16]; for(int d=0;d<16;d++) qv[d]=bf2f(qkv[(size_t)q*1536+h*16+d]);
  float m=-1e30f,l=0,acc[16]; for(int d=0;d<16;d++) acc[d]=0;
  int k0=q-W+1; if(k0<0)k0=0;
  for(int k=k0;k<=q;k++){ float s=0; for(int d=0;d<16;d++) s+=qv[d]*bf2f(qkv[(size_t)k*1536+512+h*16+d]); s*=0.25f;
    float mn=fmaxf(m,s); float a=__expf(m-mn); float p=__expf(s-mn); l=l*a+p; for(int d=0;d<16;d++) acc[d]=acc[d]*a+p*bf2f(qkv[(size_t)k*1536+1024+h*16+d]); m=mn; }
  for(int d=0;d<16;d++) o[(size_t)q*D+h*16+d]=f2bf(acc[d]/l);
}
__global__ void geglu_k(const u16* g,const u16* u,u16* a,int n){
  int i=blockIdx.x*256+threadIdx.x; if(i>=n) return;
  float x=bf2f(g[i]); float t=0.5f*x*(1.f+tanhf(0.7978845608f*(x+0.044715f*x*x*x)));
  a[i]=f2bf(bf2f(f2bf(t))*bf2f(u[i]));
}
extern "C" void kernel_launch(void* const* d_in, const int* in_sizes, int n_in, void* d_out, int out_size, void* d_ws, size_t ws_size, hipStream_t stream){
  const u16* x=(const u16*)d_in[0]; const u16* wqkv=(const u16*)d_in[1]; const u16* wo=(const u16*)d_in[2];
  const u16* wg=(const u16*)d_in[4]; const u16* wu=(const u16*)d_in[5]; const u16* wd=(const u16*)d_in[6];
  u16* ws=(u16*)d_ws; u16* xn=ws; u16* qkv=xn+S*D; u16* o=qkv+S*1536; u16* h=o+S*D; u16* hn=h+S*D; u16* g=hn+S*D; u16* u=g+S*F; u16* a=u+S*F;
  ln_k<<<S,64,0,stream>>>(x,xn);
  gemm_k<<<dim3(1536/64,S/8),64,0,stream>>>(xn,wqkv,qkv,nullptr,S,1536,D);
  attn_k<<<S*H/64,64,0,stream>>>(qkv,o);
  gemm_k<<<dim3(D/64,S/8),64,0,stream>>>(o,wo,h,x,S,D,D);
  ln_k<<<S,64,0,stream>>>(h,hn);
  gemm_k<<<dim3((F+63)/64,S/8),64,0,stream>>>(hn,wg,g,nullptr,S,F,D);
  gemm_k<<<dim3((F+63)/64,S/8),64,0,stream>>>(hn,wu,u,nullptr,S,F,D);
  geglu_k<<<(S*F+255)/256,256,0,stream>>>(g,u,a,S*F);
  gemm_k<<<dim3(D/64,S/8),64,0,stream>>>(a,wd,(u16*)d_out,h,S,D,F);
}
```

```cpp
#include <hip/hip_runtime.h>
#include <stdint.h>

namespace {
typedef unsigned u32;
typedef unsigned long long u64;
typedef u32 u32x2 __attribute__((ext_vector_type(2)));
typedef u32 u32x4 __attribute__((ext_vector_type(4)));
typedef float f32x2 __attribute__((ext_vector_type(2)));
typedef float f32x4 __attribute__((ext_vector_type(4)));
typedef float f32x16 __attribute__((ext_vector_type(16)));
typedef short s16x4 __attribute__((ext_vector_type(4)));
#define DI __device__ __forceinline__
#define LAS __attribute__((address_space(3)))
#define SBAR() __builtin_amdgcn_sched_barrier(0)

constexpr int SEQ = 2048, DM = 512, FF = 1376;
constexpr u32 OFF_E = 0, OFF_FINIT = 1024, OFF_FKV = 4096, OFF_FO = OFF_FKV + 256 * 128, OFF_FH = OFF_FO + 512 * 128, OFF_FA = OFF_FH + 512 * 128;
constexpr u32 OFF_HS = 256 * 1024, OFF_KF = 512 * 1024, OFF_VF = OFF_KF + (2u << 20), OFF_OF = OFF_VF + (2u << 20), OFF_HF = OFF_OF + (2u << 20);
constexpr u32 AF_RT = 176 * 256, OFF_AF = OFF_HF + (2u << 20), OFF_WOT = OFF_AF + 128 * AF_RT, OFF_WDT = OFF_WOT + 512 * 1024, OFF_WGT = OFF_WDT + 8 * 44 * 4096;
constexpr u32 WS_BYTES = OFF_WGT + 180 * 16384;
constexpr u32 OOB = 0x80000000u;
constexpr u64 TAG_STEP = 0x9E3779B97F4A7C15ull;
constexpr u32 L_X = 0, L_W = 16384, W_SLOT = 24576, L_SCR = 139264  , L_CTL = 160768  ;
constexpr u32 L_IMG0 = 16384, L_IMG2 = 90112  , L_ONES = 65536, L_XCH = 132096  ;
constexpr u32 L_H = 0, L_ST = 156672  ;
constexpr u32 LDS_BYTES = 163840;
constexpr float QSCALE = 0.25f * 1.4426950408889634f;
constexpr int SPIN_CAP = 1 << 15;

DI u32x4 make_srd(const void* p, u32 bytes) { const u64 a = (u64)p; u32x4 r; r.x = (u32)a; r.y = (u32)(a >> 32) & 0xffffu; r.z = bytes; r.w = 0x00020000u; return r; }
template <int OFF, bool SC1> DI void ld16v(u32x4& d, u32 voff, const u32x4& srd, u32 soff) {
  if constexpr (SC1) asm volatile("buffer_load_dwordx4 %0, %1, %2, %3 offen offset:%4 sc1" : "=v"(d) : "v"(voff), "s"(srd), "s"(soff), "i"(OFF) : "memory");
  else asm volatile("buffer_load_dwordx4 %0, %1, %2, %3 offen offset:%4" : "=v"(d) : "v"(voff), "s"(srd), "s"(soff), "i"(OFF) : "memory");
}
template <int OFF> DI void ld16a(u32x4& d, u32 voff, const u32x4& srd, u32 soff) {
  asm volatile("buffer_load_dwordx4 %0, %1, %2, %3 offen offset:%4 sc1" : "=a"(d) : "v"(voff), "s"(srd), "s"(soff), "i"(OFF) : "memory");
}
template <int OFF> DI void ld8v(u32x2& d, u32 voff, const u32x4& srd, u32 soff) {
  asm volatile("buffer_load_dwordx2 %0, %1, %2, %3 offen offset:%4 sc1" : "=v"(d) : "v"(voff), "s"(srd), "s"(soff), "i"(OFF) : "memory");
}
DI void ld4v(u32& d, u32 voff, const u32x4& srd, u32 soff) { asm volatile("buffer_load_dword %0, %1, %2, %3 offen" : "=v"(d) : "v"(voff), "s"(srd), "s"(soff) : "memory"); }
template <int OFF, bool SC1> DI void st16(const u32x4& d, u32 voff, const u32x4& srd, u32 soff) {
  if constexpr (SC1) asm volatile("buffer_store_dwordx4 %0, %1, %2, %3 offen offset:%4 sc1\n\ts_nop 1" ::"v"(d), "v"(voff), "s"(srd), "s"(soff), "i"(OFF) : "memory");
  else asm volatile("buffer_store_dwordx4 %0, %1, %2, %3 offen offset:%4\n\ts_nop 1" ::"v"(d), "v"(voff), "s"(srd), "s"(soff), "i"(OFF) : "memory");
}
DI void st8(const u32x2& d, u32 voff, const u32x4& srd, u32 soff) { asm volatile("buffer_store_dwordx2 %0, %1, %2, %3 offen sc1" ::"v"(d), "v"(voff), "s"(srd), "s"(soff) : "memory"); }
template <bool SC1> DI void dma1(u32 ldsb, u32 voff, const u32x4& srd, u32 soff) {
  if constexpr (SC1) asm volatile("s_mov_b32 m0, %0\n\ts_nop 0\n\tbuffer_load_dwordx4 %1, %2, %3 offen sc1 lds" ::"s"(ldsb), "v"(voff), "s"(srd), "s"(soff) : "memory");
  else asm volatile("s_mov_b32 m0, %0\n\ts_nop 0\n\tbuffer_load_dwordx4 %1, %2, %3 offen lds" ::"s"(ldsb), "v"(voff), "s"(srd), "s"(soff) : "memory");
}
DI void dma4(u32 ldsb, u32 voff, const u32x4& srd, u32 soff) {
  asm volatile("s_mov_b32 m0, %0\n\ts_nop 0\n\tbuffer_load_dwordx4 %1, %2, %3 offen sc1 lds\n\tbuffer_load_dwordx4 %1, %2, %3 offen offset:1024 sc1 lds\n\t"
               "buffer_load_dwordx4 %1, %2, %3 offen offset:2048 sc1 lds\n\tbuffer_load_dwordx4 %1, %2, %3 offen offset:3072 sc1 lds" ::"s"(ldsb), "v"(voff), "s"(srd), "s"(soff) : "memory");
}
DI void dma2(u32 ldsb, u32 voff, const u32x4& srd, u32 soff) {
  asm volatile("s_mov_b32 m0, %0\n\ts_nop 0\n\tbuffer_load_dwordx4 %1, %2, %3 offen sc1 lds\n\tbuffer_load_dwordx4 %1, %2, %3 offen offset:1024 sc1 lds" ::"s"(ldsb), "v"(voff), "s"(srd), "s"(soff) : "memory");
}
template <int N> DI void vmwait() { asm volatile("s_waitcnt vmcnt(%0)" ::"i"(N) : "memory"); }
template <int N> DI void vmwait_bar() { asm volatile("s_waitcnt vmcnt(%0) lgkmcnt(0)\n\ts_barrier" ::"i"(N) : "memory"); }
DI void lgm_bar() { asm volatile("s_waitcnt lgkmcnt(0)\n\ts_barrier" ::: "memory"); }
template <class T> DI void pinv(T& x) { asm volatile("" : "+v"(x)); }
template <class T> DI void pina(T& x) { asm volatile("" : "+a"(x)); }
template <int N> DI void nops() { asm volatile("s_nop %0" ::"i"(N - 1)); }
DI void mm32(f32x16& c, const u32x4& a, const u32x4& b) { asm volatile("v_mfma_f32_32x32x16_bf16 %0, %1, %2, %0" : "+v"(c) : "v"(a), "v"(b)); }
DI void mm32z(f32x16& d, const u32x4& a, const u32x4& b) { asm volatile("v_mfma_f32_32x32x16_bf16 %0, %1, %2, 0" : "=&v"(d) : "v"(a), "v"(b)); }
DI void mm32c(f32x16& d, const u32x4& a, const u32x4& b, const f32x16& c) { asm volatile("v_mfma_f32_32x32x16_bf16 %0, %1, %2, %3" : "=&v"(d) : "v"(a), "v"(b), "v"(c)); }
DI void mm16a(f32x4& c, const u32x4& a, const u32x4& b) { asm volatile("v_mfma_f32_16x16x32_bf16 %0, %1, %2, %0" : "+v"(c) : "a"(a), "v"(b)); }
DI void mm16az(f32x4& d, const u32x4& a, const u32x4& b) { asm volatile("v_mfma_f32_16x16x32_bf16 %0, %1, %2, 0" : "=&v"(d) : "a"(a), "v"(b)); }
DI void mm16v(f32x4& c, const u32x4& a, const u32x4& b) { asm volatile("v_mfma_f32_16x16x32_bf16 %0, %1, %2, %0" : "+v"(c) : "v"(a), "v"(b)); }
DI void mm16vz(f32x4& d, const u32x4& a, const u32x4& b) { asm volatile("v_mfma_f32_16x16x32_bf16 %0, %1, %2, 0" : "=&v"(d) : "v"(a), "v"(b)); }
DI u32 cvtpk(float lo, float hi) { u32 r; asm("v_cvt_pk_bf16_f32 %0, %1, %2" : "=v"(r) : "v"(lo), "v"(hi)); return r; }
DI float max3(float a, float b, float c) { float r; asm("v_max3_f32 %0, %1, %2, %3" : "=v"(r) : "v"(a), "v"(b), "v"(c)); return r; }
DI float dot2(u32 a, u32 b, float c) { float r; asm("v_dot2_f32_bf16 %0, %1, %2, %3" : "=v"(r) : "v"(a), "v"(b), "v"(c)); return r; }
DI float bflo(u32 v) { return __uint_as_float(v << 16); }
DI float bfhi(u32 v) { return __uint_as_float(v & 0xffff0000u); }
template <int CTRL> DI float dpp_add(float v) { return v + __int_as_float(__builtin_amdgcn_update_dpp(0, __float_as_int(v), CTRL, 0xf, 0xf, true)); }
DI float xsum16_32(float v) {
  auto s = __builtin_amdgcn_permlane16_swap(__float_as_uint(v), __float_as_uint(v), false, false);
  v = __uint_as_float(s[0]) + __uint_as_float(s[1]);
  auto t = __builtin_amdgcn_permlane32_swap(__float_as_uint(v), __float_as_uint(v), false, false);
  return __uint_as_float(t[0]) + __uint_as_float(t[1]);
}
DI u32x4 lrd16(u32 a) { return *(const LAS u32x4*)(uintptr_t)a; }
DI void lwr16(u32 a, const u32x4& v) { *(LAS u32x4*)(uintptr_t)a = v; }
DI u32x4 lrdtr(u32 a0, u32 a1) {
  const s16x4 lo = __builtin_amdgcn_ds_read_tr16_b64_v4i16((LAS s16x4*)(uintptr_t)a0), hi = __builtin_amdgcn_ds_read_tr16_b64_v4i16((LAS s16x4*)(uintptr_t)a1);
  const u32x2 l = __builtin_bit_cast(u32x2, lo), h = __builtin_bit_cast(u32x2, hi);
  return u32x4{l.x, l.y, h.x, h.y};
}

struct Ctx {
  u32x4 ws;
  u32 tlo, thi;
  u32 lane;
};
DI void wait_flags(const Ctx& c, u32 voff, bool ok0) {
#pragma nounroll
  for (int it = 0; it < SPIN_CAP; ++it) {
    u32x2 v;
    asm volatile("buffer_load_dwordx2 %0, %1, %2, 0 offen sc1\n\ts_waitcnt vmcnt(0)" : "=&v"(v) : "v"(voff), "s"(c.ws) : "memory");
    const bool ok = ok0 || (v.x == c.tlo && v.y == c.thi);
    if (__builtin_amdgcn_ballot_w64(!ok) == 0ull) break;
    __builtin_amdgcn_s_sleep(1);
  }
}
DI void put_flag(const Ctx& c, u32 soff) {
  if (c.lane == 0) { const u32x2 t = {c.tlo, c.thi}; st8(t, 0u, c.ws, soff); }
}
DI void arrive_put(const Ctx& c, u32 ctr, u32 soff) {
  u32 old = 0;
  if (c.lane == 0) old = __hip_atomic_fetch_add((LAS u32*)(uintptr_t)ctr, 1u, __ATOMIC_RELAXED, __HIP_MEMORY_SCOPE_WORKGROUP);
  if (__builtin_amdgcn_readfirstlane(old) == 3u) put_flag(c, soff);
}

struct P1 {
  u32x4 xr[2][8];
  float rs[2], nb[2];
  u32 xw[2];
  u32 xa[4];
  u32 wqk, wv;
  u32x4 fx0[2], fx1[2], fqk[2], fv[2];
  f32x16 a0, a1, av;
};
DI void p1_norm(P1& p, int g, int c) {
  const u32x4 v = p.xr[g][c];
  u32x4 o;
#pragma unroll
  for (int i = 0; i < 4; ++i) {
    f32x2 t = {bflo(v[i]), bfhi(v[i])};
    t = t * f32x2{p.rs[g], p.rs[g]} + f32x2{p.nb[g], p.nb[g]};
    o[i] = cvtpk(t.x, t.y);
  }
  lwr16(p.xw[g] + (c & 1) * 8192, o);
}
template <int C, int S> DI void p1_read(P1& p) {
  constexpr int B = S & 1, slot = C % 5;
  p.fx0[B] = lrd16(p.xa[S] + (C & 1) * 8192);
  p.fx1[B] = lrd16((p.xa[S] ^ 4096) + (C & 1) * 8192);
  p.fqk[B] = lrdtr(p.wqk + slot * W_SLOT + S * 4096, p.wqk + slot * W_SLOT + S * 4096 + 1024);
  p.fv[B] = lrdtr(p.wv + slot * W_SLOT + S * 2048, p.wv + slot * W_SLOT + S * 2048 + 512);
}
template <int C> DI void p1_dma(u32 w, u32 vqk, u32 vv, const u32x4& srdw) {
  constexpr u32 slot = L_W + (C % 5) * W_SLOT;
#pragma unroll
  for (int m = 0; m < 4; ++m) dma1<false>(slot + (w + 4 * m) * 1024, vqk, srdw, (C * 64 + 16 * m + 4 * w) * 3072);
#pragma unroll
  for (int m = 0; m < 2; ++m) dma1<false>(slot + 16384 + (w + 4 * m) * 1024, vv, srdw, (C * 64 + 32 * m + 8 * w) * 3072);
}
template <int C, int S> DI void p1_mma(P1& p) {
  constexpr int B = S & 1;
  if constexpr (C == 0 && S == 0) { mm32z(p.a0, p.fqk[B], p.fx0[B]); mm32z(p.a1, p.fqk[B], p.fx1[B]); mm32z(p.av, p.fx0[B], p.fv[B]); }
  else { mm32(p.a0, p.fqk[B], p.fx0[B]); mm32(p.a1, p.fqk[B], p.fx1[B]); mm32(p.av, p.fx0[B], p.fv[B]); }
}

struct AttnMask { u32 pm[8]; };
DI float rowmax16(const f32x16& s, float m0, float m1) {
  m0 = max3(m0, s[0], s[1]); m1 = max3(m1, s[2], s[3]); m0 = max3(m0, s[4], s[5]); m1 = max3(m1, s[6], s[7]);
  m0 = max3(m0, s[8], s[9]); m1 = max3(m1, s[10], s[11]); m0 = max3(m0, s[12], s[13]); m1 = max3(m1, s[14], s[15]);
  return max3(m0, m1, m1);
}
DI void exp_pack(const f32x16& s, int r0, u32x4& p) {
#pragma unroll
  for (int i = 0; i < 4; ++i) p[i] = cvtpk(__builtin_amdgcn_exp2f(s[r0 + 2 * i]), __builtin_amdgcn_exp2f(s[r0 + 2 * i + 1]));
}
DI u32x4 attn_unit(const u32x4& q, u32 kimg, u32 va, u32 lane, const AttnMask& mk) {
  const u32 qq = lane & 31, h4 = (lane >> 5) * 4;
  u32x4 kf[9];
#pragma unroll
  for (int i = 0; i < 9; ++i) kf[i] = lrd16(kimg + (8 - i) * 1024);
  f32x16 sa, sb;
  mm32z(sa, kf[0], q); mm32z(sb, kf[8], q);
  nops<12>(); pinv(sa); pinv(sb);
  f32x16 sm;
#pragma unroll
  for (int r = 0; r < 16; ++r) sm[r] = ((r & 3) + 8 * (r >> 2) + h4 > qq) ? sb[r] : sa[r];
  SBAR();
  mm32z(sa, kf[1], q);
  SBAR();
  float m = rowmax16(sm, sm[0], sm[2]);
  pinv(m); nops<4>();
  SBAR();
#pragma unroll
  for (int i = 1; i <= 7; ++i) {
    if (i & 1) { if (i < 7) mm32z(sb, kf[i + 1], q); nops<2>(); pinv(sa); SBAR(); m = rowmax16(sa, m, m); }
    else       { mm32z(sa, kf[i + 1], q); nops<2>(); pinv(sb); SBAR(); m = rowmax16(sb, m, m); }
    pinv(m); SBAR();
  }
  { auto t = __builtin_amdgcn_permlane32_swap(__float_as_uint(m), __float_as_uint(m), false, false); m = max3(__uint_as_float(t[0]), __uint_as_float(t[1]), __uint_as_float(t[1])); }
  f32x16 cn;
#pragma unroll
  for (int r = 0; r < 16; ++r) cn[r] = -m;
  pinv(cn); nops<8>();
  u32x4 v0 = lrd16(va + 8192), v1 = lrd16(va + 8192 + 512), w0 = lrd16(va), w1 = lrd16(va + 512);
  SBAR();
  mm32c(sa, kf[1], q, cn);
  SBAR();
#pragma unroll
  for (int r = 0; r < 16; ++r) sm[r] += cn[r];
  u32x4 p0, p1, l0, l1;
  exp_pack(sm, 0, p0); exp_pack(sm, 8, p1);
#pragma unroll
  for (int i = 0; i < 4; ++i) { l0[i] = p0[i] & mk.pm[i]; l1[i] = p1[i] & mk.pm[4 + i]; p0[i] ^= l0[i]; p1[i] ^= l1[i]; }
  pinv(p0); pinv(p1); pinv(l0); pinv(l1); nops<2>();
  SBAR();
  f32x16 o;
  mm32z(o, w0, l0); mm32(o, w1, l1);
  SBAR();
#pragma unroll
  for (int i = 1; i <= 7; ++i) {
    u32x4 n0, n1;
    if (i & 1) {
      if (i < 7) mm32c(sb, kf[i + 1], q, cn);
      w0 = lrd16(va + (8 - i) * 1024); w1 = lrd16(va + (8 - i) * 1024 + 512); pinv(sa); SBAR();
      exp_pack(sa, 0, n0); pinv(n0); SBAR(); mm32(o, v0, p0); SBAR();
      exp_pack(sa, 8, n1); pinv(n1); SBAR(); mm32(o, v1, p1); SBAR();
      p0 = n0; p1 = n1; v0 = w0; v1 = w1;
    } else {
      mm32c(sa, kf[i + 1], q, cn);
      w0 = lrd16(va + (8 - i) * 1024); w1 = lrd16(va + (8 - i) * 1024 + 512); pinv(sb); SBAR();
      exp_pack(sb, 0, n0); pinv(n0); SBAR(); mm32(o, v0, p0); SBAR();
      exp_pack(sb, 8, n1); pinv(n1); SBAR(); mm32(o, v1, p1); SBAR();
      p0 = n0; p1 = n1; v0 = w0; v1 = w1;
    }
  }
  pinv(p0); pinv(p1); nops<2>();
  mm32(o, v0, p0); mm32(o, v1, p1);
  nops<12>(); pinv(o);
  const float il = __builtin_amdgcn_rcpf(o[8]);
  u32x4 r;
#pragma unroll
  for (int i = 0; i < 4; ++i) r[i] = cvtpk(o[2 * i] * il, o[2 * i + 1] * il);
  return r;
}

DI void xch_write(const f32x4 (&acc)[2][4], u32 w, u32 lane) {
#pragma unroll
  for (int ab = 1; ab < 4; ++ab) {
    const u32 dst = L_XCH + ((w ^ ab) * 3 + (ab - 1)) * 2048 + lane * 16;
    lwr16(dst, __builtin_bit_cast(u32x4, acc[ab >> 1][2 * (ab & 1)]));
    lwr16(dst + 1024, __builtin_bit_cast(u32x4, acc[ab >> 1][2 * (ab & 1) + 1]));
  }
}
DI void xch_read(f32x4& r0, f32x4& r1, u32 w, u32 lane) {
  const u32 src = L_XCH + w * 6144 + lane * 16;
#pragma unroll
  for (int s = 0; s < 3; ++s) { r0 += __builtin_bit_cast(f32x4, lrd16(src + s * 2048)); r1 += __builtin_bit_cast(f32x4, lrd16(src + s * 2048 + 1024)); }
}

struct GuEpi { f32x2 G, U, z, e; float e0, r0; };
struct GuStat { float nm, a0, a1, vv; };
template <int STEP> DI void gu_epi_step(const f32x4& acc, const f32x4& cs, const GuStat& st, GuEpi& t, u32& out) {
  if constexpr (STEP == 0) {
    const f32x2 nm = {st.nm, st.nm};
    t.G = f32x2{cs[0], cs[1]} * nm + f32x2{acc[0], acc[1]}; t.U = f32x2{cs[2], cs[3]} * nm + f32x2{acc[2], acc[3]};
    t.z = t.G * t.G; t.z = t.z * f32x2{st.a1, st.a1} + f32x2{st.a0, st.a0};
    pinv(t.G); pinv(t.U); pinv(t.z);
  } else if constexpr (STEP == 1) {
    t.z = t.z * t.G; t.e0 = __builtin_amdgcn_exp2f(t.z.x); t.U = t.U * t.G;
    pinv(t.z); pinv(t.e0); pinv(t.U);
  } else if constexpr (STEP == 2) {
    t.e = f32x2{t.e0, __builtin_amdgcn_exp2f(t.z.y)}; t.e = t.e * f32x2{st.vv, st.vv} + f32x2{st.vv, st.vv}; t.r0 = __builtin_amdgcn_rcpf(t.e.x);
    pinv(t.e); pinv(t.r0);
  } else {
    t.U = t.U * f32x2{t.r0, __builtin_amdgcn_rcpf(t.e.y)}; out = cvtpk(t.U.x, t.U.y);
    pinv(out);
  }
}
DI void gu_store(const u32 (&d)[3], u32 voff, const u32x4& ws, u32 soff) {
  auto s0 = __builtin_amdgcn_permlane16_swap(d[0], d[1], false, false);
  auto s1 = __builtin_amdgcn_permlane16_swap(d[2], d[2], false, false);
  auto s2 = __builtin_amdgcn_permlane32_swap(s0[1], s1[1], false, false);
  const u32x4 v = {d[0], d[1], d[2], s2[1]};
  st16<0, true>(v, voff, ws, soff);
}

struct Args { const void* x; const void* wqkv; const void* wo; const void* wg; const void* wu; const void* wd; void* out; void* ws; };

#define STAMP(n)

__global__ __launch_bounds__(256, 1) void fused_block(Args a) {
  extern __shared__ __attribute__((aligned(16))) char smem[];
  const u32 lane = threadIdx.x & 63, w = __builtin_amdgcn_readfirstlane(threadIdx.x >> 6), b = blockIdx.x;
  const u32 xl = b & 7, bi = b >> 3;
  const u32 rb1 = 8 * (xl >> 1) + (bi >> 2), hg = 4 * (xl & 1) + (bi & 3);
  const u32 rb = 4 * xl + (bi >> 3), cg = bi & 7;
  const u32 rb2 = 2 * xl + (bi >> 4), fg = bi & 15;
  Ctx c; c.ws = make_srd(a.ws, WS_BYTES); c.lane = lane;
  const u32x4 srdx = make_srd(a.x, SEQ * DM * 2), srdw = make_srd(a.wqkv, DM * 3 * DM * 2);
  STAMP(0);

  u32x2 e0; ld8v<0>(e0, 0u, c.ws, OFF_E);
  u32 pf;
  {
    u64 pc; asm volatile("s_getpc_b64 %0" : "=s"(pc));
    const u32x4 srdc = make_srd((const void*)(pc & ~127ull), 40960);
    ld4v(pf, w == 0 && lane < 10 ? (bi * 10 + lane) * 128 : OOB, srdc, 0u);
  }
  P1 p;
  const u32 r8 = lane >> 3, c8 = lane & 7;
  {
    const u32 vx = (rb1 * 64 + w * 16 + r8) * 1024 + c8 * 16;
    ld16v<0, false>(p.xr[0][0], vx, srdx, 0u);    ld16v<128, false>(p.xr[0][1], vx, srdx, 0u);  ld16v<256, false>(p.xr[0][2], vx, srdx, 0u);  ld16v<384, false>(p.xr[0][3], vx, srdx, 0u);
    ld16v<512, false>(p.xr[0][4], vx, srdx, 0u);  ld16v<640, false>(p.xr[0][5], vx, srdx, 0u);  ld16v<768, false>(p.xr[0][6], vx, srdx, 0u);  ld16v<896, false>(p.xr[0][7], vx, srdx, 0u);
    ld16v<0, false>(p.xr[1][0], vx, srdx, 8192u);   ld16v<128, false>(p.xr[1][1], vx, srdx, 8192u); ld16v<256, false>(p.xr[1][2], vx, srdx, 8192u); ld16v<384, false>(p.xr[1][3], vx, srdx, 8192u);
    ld16v<512, false>(p.xr[1][4], vx, srdx, 8192u); ld16v<640, false>(p.xr[1][5], vx, srdx, 8192u); ld16v<768, false>(p.xr[1][6], vx, srdx, 8192u); ld16v<896, false>(p.xr[1][7], vx, srdx, 8192u);
  }
  const u32 u = w * 256 + b;
  u32x4 raw[8];
  {
    const bool has = u < 592;
    const void* s1 = u < 352 ? a.wg : (u < 528 ? a.wd : a.wo);
    const void* s2 = u < 352 ? a.wu : s1;
    const u32 nb = !has ? 0u : (u < 528 ? FF * DM * 2 : DM * DM * 2);
    const u32x4 q1 = make_srd(s1, nb), q2 = make_srd(s2, nb);
    u32 off[8];
    if (u < 352) {
      const u32 G = u >> 4, ks = u & 15, f = 64 * G + 8 * c8;
#pragma unroll
      for (int n = 0; n < 8; ++n) off[n] = f < FF ? (32 * ks + 8 * (n & 3) + r8) * (FF * 2) + f * 2 : OOB;
    } else if (u < 528) {
      const u32 v = u - 352, g = v / 22, ks0 = 2 * (v % 22);
      const u32 lb = (r8 < 6 ? 8 * (r8 >> 1) : 6) + (r8 & 1), lm = r8 < 6 ? 2 : 8;
#pragma unroll
      for (int n = 0; n < 8; ++n) {
        const u32 ch = 4 * ks0 + n, pp = ch / 3, qq = ch - 3 * pp;
        const u32 f = 24 * pp + lb + qq * lm;
        off[n] = f < FF ? f * 1024 + g * 128 + c8 * 16 : OOB;
      }
    } else {
      const u32 v = u - 528, g = (v >> 3) & 7, ks0 = 2 * (v & 7);
#pragma unroll
      for (int n = 0; n < 8; ++n) {
        const u32 k = 32 * (ks0 + (n >> 2)) + 16 * ((n & 3) >> 1) + 8 * (r8 >> 2) + 4 * (n & 1) + (r8 & 3);
        off[n] = k * 1024 + g * 128 + c8 * 16;
      }
    }
#pragma unroll
    for (int n = 0; n < 4; ++n) ld16v<0, false>(raw[n], off[n], q1, 0u);
#pragma unroll
    for (int n = 4; n < 8; ++n) ld16v<0, false>(raw[n], off[n], q2, 0u);
  }
  const u32 vqk = (lane >> 4) * 3072 + ((lane >> 3) & 1) * 1024 + hg * 128 + (((c8 >> 1) ^ (lane >> 4)) << 5) + (c8 & 1) * 16;
  const u32 vv = r8 * 3072 + 2048 + hg * 128 + (((c8 >> 1) ^ (r8 & 3)) << 5) + (c8 & 1) * 16;
  p1_dma<0>(w, vqk, vv, srdw); p1_dma<1>(w, vqk, vv, srdw); p1_dma<2>(w, vqk, vv, srdw); p1_dma<3>(w, vqk, vv, srdw); p1_dma<4>(w, vqk, vv, srdw);
  u32 warm;
  {
    const u32 ln = (bi >> 2) * 72 + w * 18 + lane;
    ld4v(warm, lane < 18 ? (320 + ln / 3) * 3072 + (ln % 3) * 1024 + hg * 128 : OOB, srdw, 0u);
  }
  if (threadIdx.x < 16) *(LAS u32*)(uintptr_t)(L_CTL + threadIdx.x * 4) = 0u;

  vmwait<39>();
#pragma unroll
  for (int g = 0; g < 2; ++g)
#pragma unroll
    for (int j = 0; j < 8; ++j) pinv(p.xr[g][j]);
  STAMP(1);
#pragma unroll
  for (int g = 0; g < 2; ++g) {
    float s0 = 0.f, s1 = 0.f, q0 = 0.f, q1 = 0.f;
#pragma unroll
    for (int j = 0; j < 8; ++j) {
      const u32x4 v = p.xr[g][j];
      s0 = dot2(v.x, 0x3f803f80u, s0); q0 = dot2(v.x, v.x, q0); s1 = dot2(v.y, 0x3f803f80u, s1); q1 = dot2(v.y, v.y, q1);
      s0 = dot2(v.z, 0x3f803f80u, s0); q0 = dot2(v.z, v.z, q0); s1 = dot2(v.w, 0x3f803f80u, s1); q1 = dot2(v.w, v.w, q1);
    }
    asm volatile("s_nop 2" : "+v"(s0), "+v"(s1), "+v"(q0), "+v"(q1));
    float s = s0 + s1, q = q0 + q1;
    s = dpp_add<0xB1>(s); q = dpp_add<0xB1>(q); s = dpp_add<0x4E>(s); q = dpp_add<0x4E>(q); s = dpp_add<0x141>(s); q = dpp_add<0x141>(q);
    const float mean = s * (1.f / DM), var = fmaxf(q * (1.f / DM) - mean * mean, 0.f);
    p.rs[g] = __builtin_amdgcn_rsqf(var + 1e-5f); p.nb[g] = -mean * p.rs[g];
    const u32 tok = (w & 1) * 16 + g * 8 + r8;
    p.xw[g] = L_X + (w >> 1) * 4096 + (c8 >> 1) * 1024 + (c8 & 1) * 512 + ((tok ^ c8) << 4);
  }
  p1_norm(p, 0, 0); p1_norm(p, 1, 0);

  vmwait<31>();
#pragma unroll
  for (int n = 0; n < 8; ++n) pinv(raw[n]);
  {
    const u32 scr = w == 2 ? L_X + 8192 : L_SCR + (w & 1) * 8192, q4 = lane >> 4, qq = (lane & 15) >> 2, pp = lane & 3;
    u32x4 o[8]; u32 so; u32x4 wst = c.ws; u32 st1 = 1024;
    if (u >= 592) { wst.z = 0; so = 0;
#pragma unroll
      for (int n = 0; n < 8; ++n) o[n] = raw[n];
    } else if (u < 352) {
#pragma unroll
      for (int m = 0; m < 4; ++m) {
        const u32 ad = scr + (8 * m + r8) * 256 + c8 * 32;
        lwr16(ad, u32x4{raw[m].x, raw[m + 4].x, raw[m].y, raw[m + 4].y});
        lwr16(ad + 16, u32x4{raw[m].z, raw[m + 4].z, raw[m].w, raw[m + 4].w});
      }
      const u32 rd = scr + (8 * q4 + qq) * 256 + pp * 8;
#pragma unroll
      for (int n = 0; n < 8; ++n) o[n] = lrdtr(rd + n * 32, rd + n * 32 + 1024);
      so = OFF_WGT + (u >> 4) * 8 * 16384 + (u & 15) * 1024; st1 = 16384;
    } else {
#pragma unroll
      for (int n = 0; n < 8; ++n) lwr16(scr + (8 * n + r8) * 128 + c8 * 16, raw[n]);
      const u32 rd = scr + (8 * q4 + qq) * 128 + pp * 16;
#pragma unroll
      for (int n = 0; n < 8; ++n) { const u32 ad = rd + (n >> 2) * 4096 + ((n & 3) >> 1) * 64 + (n & 1) * 8; o[n] = lrdtr(ad, ad + 512); }
      const u32 v = u < 528 ? u - 352 : u - 528;
      so = (u < 528 ? OFF_WDT : OFF_WOT) + v * 8192;
    }
#pragma unroll
    for (int n = 0; n < 8; ++n) st16<0, true>(o[n], lane * 16, wst, so + n * st1);
  }

  {
    const u32 tk = lane & 31, hh = lane >> 5, g1 = (lane >> 4) & 1, qq = (lane & 15) >> 2, pp = lane & 3;
#pragma unroll
    for (int s = 0; s < 4; ++s) p.xa[s] = L_X + (w & 1) * 4096 + s * 1024 + hh * 512 + ((tk ^ (2 * s + hh)) << 4);
    p.wqk = L_W + (8 * hh + qq) * 256 + g1 * 128 + ((w ^ qq) << 5) + pp * 8;
    p.wv = L_W + 16384 + (8 * hh + qq) * 128 + ((((w & 2) + g1) ^ qq) << 5) + pp * 8;
  }
  const bool odd = w & 1;
  vmwait_bar<33>();
  STAMP(2);
  p1_read<0, 0>(p);
#define P1_CHUNK(C, NW)                                                                              \
  SBAR(); p1_read<C, 1>(p); SBAR(); p1_mma<C, 0>(p); SBAR();                                    \
  if (C < 7) p1_norm(p, 0, C + 1);                                                                   \
  SBAR(); p1_read<C, 2>(p); SBAR(); p1_mma<C, 1>(p); SBAR();                                    \
  if (C < 7) p1_norm(p, 1, C + 1);                                                                   \
  SBAR(); p1_read<C, 3>(p); SBAR(); p1_mma<C, 2>(p); SBAR();                                    \
  if constexpr (C < 7) {                                                                             \
    vmwait_bar<NW>();                                                                                \
    p1_read<(C + 1) & 7, 0>(p);                                                                      \
    if constexpr (C < 3) p1_dma<C + 5>(w, vqk, vv, srdw);                                            \
  }                                                                                                  \
  SBAR(); p1_mma<C, 3>(p); SBAR();
  P1_CHUNK(0, 27) P1_CHUNK(1, 27) P1_CHUNK(2, 27) P1_CHUNK(3, 27) P1_CHUNK(4, 12)
  pinv(e0); pinv(pf); pinv(warm);
  { const u64 t = (((u64)e0.y << 32) | e0.x) + TAG_STEP; c.tlo = (u32)t; c.thi = (u32)(t >> 32); }
  if (w == 3) put_flag(c, OFF_FINIT + b * 8);
  P1_CHUNK(5, 6) P1_CHUNK(6, 0) P1_CHUNK(7, 0)
  STAMP(3);
  nops<12>(); pinv(p.a0); pinv(p.a1); pinv(p.av);
  const u32 head = 4 * hg + w, img = (w < 2 ? L_IMG0 : L_IMG2) + (w & 1) * 20480;
  u32x4 qf0, qf1, kt0;
  {
    u32x4 k0, k1, va, vb;
#pragma unroll
    for (int i = 0; i < 4; ++i) {
      k0[i] = cvtpk(p.a0[8 + 2 * i], p.a0[9 + 2 * i]); k1[i] = cvtpk(p.a1[8 + 2 * i], p.a1[9 + 2 * i]);
      va[i] = cvtpk(p.av[2 * i], p.av[2 * i + 1]);     vb[i] = cvtpk(p.av[8 + 2 * i], p.av[9 + 2 * i]);
    }
    const u32 g1 = (lane >> 4) & 1, vl = (lane >> 5) * 256 + (lane & 15) * 16;
    const u32 sk = OFF_KF + (head * 64 + 2 * rb1) * 1024, sv = OFF_VF + ((4 * hg + (w & 2)) * 64 + 2 * rb1 + (w & 1)) * 1024;
    const u32 o0 = (w & 1) * 1024, o1 = o0 ^ 1024;
    st16<0, true>(k0, lane * 16, c.ws, sk + o0); st16<0, true>(k1, lane * 16, c.ws, sk + o1);
    st16<0, true>(va, g1 * 65536 + vl, c.ws, sv); st16<512, true>(vb, g1 * 65536 + vl, c.ws, sv);
    lwr16(img + 8192 + o0 + lane * 16, k0); lwr16(img + 8192 + o1 + lane * 16, k1);
#pragma unroll
    for (int i = 0; i < 4; ++i) kt0[i] = odd ? k1[i] : k0[i];
    const u32 vi = (w < 2 ? L_IMG0 : L_IMG2) + g1 * 20480 + 10240 + (8 + (w & 1)) * 1024 + vl;
    lwr16(vi, va); lwr16(vi + 512, vb);
#pragma unroll
    for (int i = 0; i < 4; ++i) {
      const u32 qa = cvtpk(p.a0[2 * i] * QSCALE, p.a0[2 * i + 1] * QSCALE), qb = cvtpk(p.a1[2 * i] * QSCALE, p.a1[2 * i + 1] * QSCALE);
      qf0[i] = odd ? qb : qa; qf1[i] = odd ? qa : qb;
    }
  }
  AttnMask mk;
#pragma unroll
  for (int i = 0; i < 8; ++i) {
    const u32 kk = ((2 * i) & 3) + 8 * (i >> 1) + (lane >> 5) * 4, qq = lane & 31;
    mk.pm[i] = (kk > qq ? 0xffffu : 0u) | (kk + 1 > qq ? 0xffff0000u : 0u);
  }
  vmwait_bar<0>();
  if (w == 0) put_flag(c, OFF_FKV + (rb1 * 8 + hg) * 128);
  STAMP(4);
  {
#pragma unroll
    for (int m = 0; m < 5; ++m) {
      const u32 t = 2 * m + (lane >> 5), one = (2 * rb1 + t >= 8) ? 0x3f803f80u : 0u;
      lwr16(L_ONES + t * 1024 + ((lane >> 4) & 1) * 512 + (lane & 15) * 16, u32x4{one, one, one, one});
    }
    if (rb1 == 0) {
      u32x4 kz;
#pragma unroll
      for (int i = 0; i < 4; ++i) kz[i] = (u32)__builtin_amdgcn_ds_bpermute((int)(lane & 32) * 4, (int)kt0[i]);
#pragma unroll
      for (int m = 0; m < 8; ++m) lwr16(img + m * 1024 + lane * 16, kz);
    }
  }
  { const int pb = (int)rb1 - 4 + (int)lane; const bool need = lane < 4 && pb >= 0;
    wait_flags(c, need ? OFF_FKV + (pb * 8 + hg) * 128 : OOB, !need); }
  STAMP(5);
#pragma unroll
  for (int m = 0; m < 4; ++m) {
    const int pb = (int)rb1 - 4 + m;
    const u32 so = (head * 64 + 2 * (pb >= 0 ? pb : 0)) * 1024;
    if (rb1 != 0) dma2(img + m * 2048, lane * 16 + OFF_KF, c.ws, so);
    dma2(img + 10240 + m * 2048, lane * 16 + OFF_VF, c.ws, pb >= 0 ? so : OOB);
  }
  u32x4 xres[2];
  const u32 vres = (rb * 64 + (w >> 1) * 16 + (lane & 15)) * 1024 + cg * 128 + (w & 1) * 64 + (lane >> 4) * 16;
  ld16v<0, false>(xres[0], vres, srdx, 0u); ld16v<0, false>(xres[1], vres, srdx, 32768u);
  wait_flags(c, OFF_FINIT + lane * 8, false); wait_flags(c, OFF_FINIT + 512 + lane * 8, false);
  wait_flags(c, OFF_FINIT + 1024 + lane * 8, false); wait_flags(c, OFF_FINIT + 1536 + lane * 8, false);
  pinv(xres[0]); pinv(xres[1]);
  if (b == 0 && w == 0) put_flag(c, OFF_E);
  STAMP(6);
  u32x4 wo[16], wg[48];
  const u32 pw = 4 * fg + w;
  {
    const u32 so = OFF_WOT + (cg * 16 + 4 * w) * 4096, v0 = lane * 16 + (w & 1) * 2048, v1 = lane * 16 + ((w & 1) ^ 1) * 2048;
#pragma unroll
    for (int k = 0; k < 4; ++k) { ld16a<0>(wo[4 * k], v0, c.ws, so + k * 4096); ld16a<1024>(wo[4 * k + 1], v0, c.ws, so + k * 4096); ld16a<0>(wo[4 * k + 2], v1, c.ws, so + k * 4096); ld16a<1024>(wo[4 * k + 3], v1, c.ws, so + k * 4096); }
  }
  const u32 sg = pw < 58 ? OFF_WGT + pw * 49152 : OOB;
#define LDG4(i) ld16a<0>(wg[i], lane * 16, c.ws, sg + (i) * 1024); ld16a<1024>(wg[(i) + 1], lane * 16, c.ws, sg + (i) * 1024); ld16a<2048>(wg[(i) + 2], lane * 16, c.ws, sg + (i) * 1024); ld16a<3072>(wg[(i) + 3], lane * 16, c.ws, sg + (i) * 1024);
  LDG4(0) LDG4(4) LDG4(8) LDG4(12) LDG4(16) LDG4(20)

  const u32 va0 = (lane & 16) ? L_ONES + (lane & 15) * 16 : img + 10240 + (lane >> 5) * 256 + (lane & 15) * 16;
  const u32 vo = ((lane >> 4) & 1) * 16384 + (lane >> 5) * 256 + (lane & 15) * 16, so_o = OFF_OF + rb1 * 65536 + (head >> 1) * 1024 + (head & 1) * 512;
  {
    const u32x4 o = attn_unit(qf0, img + lane * 16, va0, lane, mk);
    st16<0, true>(o, vo, c.ws, so_o);
    vmwait<0>();
    arrive_put(c, L_CTL + 0, OFF_FO + (rb1 * 8 + hg) * 128);
  }
  STAMP(7);
  LDG4(24) LDG4(28) LDG4(32) LDG4(36) LDG4(40) LDG4(44)
  {
    const u32x4 o = attn_unit(qf1, img + 1024 + lane * 16, va0 + 1024, lane, mk);
    st16<0, true>(o, vo, c.ws, so_o + 32768);
    vmwait<0>();
    arrive_put(c, L_CTL + 4, OFF_FO + (256 + rb1 * 8 + hg) * 128);
  }
  STAMP(8);
#pragma unroll
  for (int i = 0; i < 16; ++i) pina(wo[i]);
#pragma unroll
  for (int i = 0; i < 48; ++i) pina(wg[i]);

  u32x4 hres[2];
#pragma unroll
  for (int X = 0; X < 2; ++X) {
    wait_flags(c, lane < 2 ? OFF_FO + (X * 256 + rb * 8 + 2 * w + lane) * 128 : OOB, lane >= 2);
    u32x4 bf[2][4];
#pragma unroll
    for (int aa = 0; aa < 2; ++aa) {
      const u32 so = OFF_OF + (4 * rb + 2 * X + (aa ^ (w >> 1))) * 16384 + w * 4096;
      ld16v<0, true>(bf[aa][0], lane * 16, c.ws, so); ld16v<1024, true>(bf[aa][1], lane * 16, c.ws, so); ld16v<2048, true>(bf[aa][2], lane * 16, c.ws, so); ld16v<3072, true>(bf[aa][3], lane * 16, c.ws, so);
    }
    f32x4 acc[2][4];
    vmwait<4>();
#pragma unroll
    for (int k = 0; k < 4; ++k) pinv(bf[0][k]);
#pragma unroll
    for (int k = 0; k < 4; ++k)
#pragma unroll
      for (int t = 0; t < 4; ++t) { if (k == 0) mm16az(acc[0][t], wo[t], bf[0][0]); else mm16a(acc[0][t], wo[4 * k + t], bf[0][k]); }
    vmwait<0>();
#pragma unroll
    for (int k = 0; k < 4; ++k) pinv(bf[1][k]);
#pragma unroll
    for (int k = 0; k < 4; ++k)
#pragma unroll
      for (int t = 0; t < 4; ++t) { if (k == 0) mm16az(acc[1][t], wo[t], bf[1][0]); else mm16a(acc[1][t], wo[4 * k + t], bf[1][k]); }
    nops<8>();
#pragma unroll
    for (int t = 0; t < 4; ++t) { pinv(acc[0][t]); pinv(acc[1][t]); }
    xch_write(acc, w, lane);
    lgm_bar();
    f32x4 r0 = acc[0][0], r1 = acc[0][1];
    xch_read(r0, r1, w, lane);
    const u32x4 xv = xres[X];
    u32x4 hv;
    hv.x = cvtpk(r0[0] + bflo(xv.x), r0[1] + bfhi(xv.x)); hv.y = cvtpk(r0[2] + bflo(xv.y), r0[3] + bfhi(xv.y));
    hv.z = cvtpk(r1[0] + bflo(xv.z), r1[1] + bfhi(xv.z)); hv.w = cvtpk(r1[2] + bflo(xv.w), r1[3] + bfhi(xv.w));
    hres[X] = hv;
    const u32 rt = 4 * rb + 2 * X + (w >> 1), ks = 2 * cg + (w & 1);
    st16<0, true>(hv, lane * 16, c.ws, OFF_HF + rt * 16384 + ks * 1024);
    float s = dot2(hv.x, 0x3f803f80u, 0.f), q = dot2(hv.x, hv.x, 0.f);
    s = dot2(hv.y, 0x3f803f80u, s); q = dot2(hv.y, hv.y, q); s = dot2(hv.z, 0x3f803f80u, s); q = dot2(hv.z, hv.z, q); s = dot2(hv.w, 0x3f803f80u, s); q = dot2(hv.w, hv.w, q);
    asm volatile("s_nop 2" : "+v"(s), "+v"(q));
    s = xsum16_32(s); q = xsum16_32(q);
    st8(u32x2{__float_as_uint(s), __float_as_uint(q)}, lane < 16 ? lane * 8 : OOB, c.ws, OFF_HS + rt * 2048 + ks * 128);
    vmwait_bar<0>();
    if (w == 0) put_flag(c, OFF_FH + (X * 256 + rb * 8 + cg) * 128);
    STAMP(9 + X);
  }

  f32x4 cs[3];
  {
    u32x4 ones = {0x3f803f80u, 0x3f803f80u, 0x3f803f80u, 0x3f803f80u};
    pinv(ones); nops<2>();
#pragma unroll
    for (int k = 0; k < 16; ++k)
#pragma unroll
      for (int t = 0; t < 3; ++t) { if (k == 0) mm16az(cs[t], wg[16 * t], ones); else mm16a(cs[t], wg[16 * t + k], ones); }
    nops<8>(); pinv(cs[0]); pinv(cs[1]); pinv(cs[2]);
  }
  const u32 vact = lane < 48 && pw < 58 ? lane * 16 : OOB;
#pragma unroll
  for (int X = 0; X < 2; ++X) {
    wait_flags(c, lane < 4 ? OFF_FH + (X * 256 + (2 * rb2 + (lane >> 1)) * 8 + 2 * w + (lane & 1)) * 128 : OOB, lane >= 4);
    STAMP(11 + 3 * X);
    u32x2 ps[4];
    {
      const u32 q4 = lane >> 4, vs = (8 * rb2 + 4 * (q4 >> 1) + 2 * X + (q4 & 1)) * 2048 + (lane & 15) * 8;
      ld8v<0>(ps[0], vs, c.ws, OFF_HS + w * 512); ld8v<128>(ps[1], vs, c.ws, OFF_HS + w * 512); ld8v<256>(ps[2], vs, c.ws, OFF_HS + w * 512); ld8v<384>(ps[3], vs, c.ws, OFF_HS + w * 512);
    }
#pragma unroll
    for (int r = 0; r < 4; ++r) dma4(L_H + X * 65536 + r * 16384 + w * 4096, lane * 16, c.ws, OFF_HF + (8 * rb2 + 4 * (r >> 1) + 2 * X + (r & 1)) * 16384 + w * 4096);
    vmwait<16>();
#pragma unroll
    for (int i = 0; i < 4; ++i) pinv(ps[i]);
    {
      const f32x2 t = (__builtin_bit_cast(f32x2, ps[0]) + __builtin_bit_cast(f32x2, ps[1])) + (__builtin_bit_cast(f32x2, ps[2]) + __builtin_bit_cast(f32x2, ps[3]));
      *(LAS f32x2*)(uintptr_t)(L_ST + X * 2048 + lane * 32 + w * 8) = t;
    }
    vmwait_bar<0>();
    STAMP(12 + 3 * X);
    float s_nm, s_var;
    {
      const f32x4 t0 = __builtin_bit_cast(f32x4, lrd16(L_ST + X * 2048 + lane * 32)), t1 = __builtin_bit_cast(f32x4, lrd16(L_ST + X * 2048 + lane * 32 + 16));
      const float s = (t0[0] + t0[2]) + (t1[0] + t1[2]), q = (t0[1] + t0[3]) + (t1[1] + t1[3]);
      s_nm = -s * (1.f / DM); s_var = fmaxf(q * (1.f / DM) - s_nm * s_nm, 0.f) + 1e-5f;
    }
    const u32 hb = L_H + X * 65536 + lane * 16;
    f32x4 accA[3], accB[3];
    GuStat st; GuEpi ep; u32 d[3];
    const u32 sa0 = OFF_AF + (8 * rb2 + 2 * X) * AF_RT + pw * 768;
#define GU_STAT(R) { const int src = ((R) * 16 + (lane & 15)) * 4; st.nm = __int_as_float(__builtin_amdgcn_ds_bpermute(src, __float_as_int(s_nm))); st.vv = __int_as_float(__builtin_amdgcn_ds_bpermute(src, __float_as_int(s_var))); }
#define GU_STAT2() { const float ra = __builtin_amdgcn_rsqf(st.vv); st.a0 = ra * (-2.f * 1.4426950408889634f * 0.7978845608028654f); st.a1 = (ra * ra) * (st.a0 * 0.044715f); pinv(st.a0); pinv(st.a1); }
#define GU_EPI(PREV, K) \
    if constexpr ((K) >= 2 && (K) < 14) gu_epi_step<((K) - 2) & 3>(PREV[((K) - 2) >> 2], cs[((K) - 2) >> 2], st, ep, d[((K) - 2) >> 2]);
#define GU_K(CUR, PREV, R, K, EPI)                                                                    \
    { if constexpr ((K) < 14) bq[((K) + 2) & 3] = lrd16(hb + (R) * 16384 + ((K) + 2) * 1024);          \
      else if constexpr ((R) < 3) bq[((K) + 2) & 3] = lrd16(hb + ((R) + 1) * 16384 + ((K) - 14) * 1024); \
      SBAR();                                                                                         \
      if constexpr ((K) == 0) { mm16az(CUR[0], wg[0], bq[0]); mm16az(CUR[1], wg[16], bq[0]); mm16az(CUR[2], wg[32], bq[0]); } \
      else { mm16a(CUR[0], wg[K], bq[(K) & 3]); mm16a(CUR[1], wg[16 + (K)], bq[(K) & 3]); mm16a(CUR[2], wg[32 + (K)], bq[(K) & 3]); } \
      SBAR();                                                                                         \
      if constexpr (EPI) {                                                                            \
        if constexpr ((K) == 0) GU_STAT((R) - 1)                                                      \
        if constexpr ((K) == 1) { GU_STAT2() nops<2>(); pinv(PREV[0]); pinv(PREV[1]); pinv(PREV[2]); } \
        GU_EPI(PREV, K)                                                                               \
        if constexpr ((K) == 14) gu_store(d, vact, c.ws, sa0 + ((((R) - 1) >> 1) * 4 + (((R) - 1) & 1)) * AF_RT); \
        SBAR();                                                                                       \
      } }
#define GU_ROW(CUR, PREV, R, EPI) GU_K(CUR, PREV, R, 0, EPI) GU_K(CUR, PREV, R, 1, EPI) GU_K(CUR, PREV, R, 2, EPI) GU_K(CUR, PREV, R, 3, EPI) GU_K(CUR, PREV, R, 4, EPI) GU_K(CUR, PREV, R, 5, EPI) \
    GU_K(CUR, PREV, R, 6, EPI) GU_K(CUR, PREV, R, 7, EPI) GU_K(CUR, PREV, R, 8, EPI) GU_K(CUR, PREV, R, 9, EPI) GU_K(CUR, PREV, R, 10, EPI) GU_K(CUR, PREV, R, 11, EPI) \
    GU_K(CUR, PREV, R, 12, EPI) GU_K(CUR, PREV, R, 13, EPI) GU_K(CUR, PREV, R, 14, EPI) GU_K(CUR, PREV, R, 15, EPI)
    u32x4 bq[4];
    bq[0] = lrd16(hb); bq[1] = lrd16(hb + 1024);
    GU_ROW(accA, accB, 0, false) GU_ROW(accB, accA, 1, true) GU_ROW(accA, accB, 2, true) GU_ROW(accB, accA, 3, true)
    GU_STAT(3) GU_STAT2() nops<8>(); pinv(accB[0]); pinv(accB[1]); pinv(accB[2]);
#pragma unroll
    for (int t = 0; t < 3; ++t) { gu_epi_step<0>(accB[t], cs[t], st, ep, d[t]); gu_epi_step<1>(accB[t], cs[t], st, ep, d[t]); gu_epi_step<2>(accB[t], cs[t], st, ep, d[t]); gu_epi_step<3>(accB[t], cs[t], st, ep, d[t]); }
    gu_store(d, vact, c.ws, sa0 + 5 * AF_RT);
    vmwait<0>();
    arrive_put(c, L_CTL + 8 + 4 * X, OFF_FA + (X * 256 + rb2 * 16 + fg) * 128);
    STAMP(13 + 3 * X);
  }

  u32x4 wd[44];
  {
    const u32 so = OFF_WDT + (cg * 44 + 11 * w) * 4096, v0 = lane * 16 + (w & 1) * 2048, v1 = lane * 16 + ((w & 1) ^ 1) * 2048;
#pragma unroll
    for (int k = 0; k < 11; ++k) { ld16a<0>(wd[4 * k], v0, c.ws, so + k * 4096); ld16a<1024>(wd[4 * k + 1], v0, c.ws, so + k * 4096); ld16a<0>(wd[4 * k + 2], v1, c.ws, so + k * 4096); ld16a<1024>(wd[4 * k + 3], v1, c.ws, so + k * 4096); }
  }
  const u32x4 srdo = make_srd(a.out, SEQ * DM * 2);
  const u32 fg0 = (44 * w) / 12, fgn = (w == 3 ? 173 : 44 * w + 43) / 12 - fg0 + 1;
  const u32 vl = lane * 16, vlast = (w == 3 && lane >= 32) ? OOB : vl;
#pragma unroll
  for (int X = 0; X < 2; ++X) {
    wait_flags(c, lane < fgn ? OFF_FA + (X * 256 + (rb >> 1) * 16 + fg0 + lane) * 128 : OOB, lane >= fgn);
    STAMP(17 + 2 * X);
    if (X == 0) {
#pragma unroll
      for (int i = 0; i < 44; ++i) pina(wd[i]);
    }
    u32x4 bf[2][11];
#pragma unroll
    for (int aa = 0; aa < 2; ++aa) {
      const u32 so = OFF_AF + (4 * rb + 2 * X + (aa ^ (w >> 1))) * AF_RT + w * 11264;
      ld16v<0, true>(bf[aa][0], vl, c.ws, so); ld16v<1024, true>(bf[aa][1], vl, c.ws, so); ld16v<2048, true>(bf[aa][2], vl, c.ws, so); ld16v<3072, true>(bf[aa][3], vl, c.ws, so);
      ld16v<0, true>(bf[aa][4], vl, c.ws, so + 4096); ld16v<1024, true>(bf[aa][5], vl, c.ws, so + 4096); ld16v<2048, true>(bf[aa][6], vl, c.ws, so + 4096); ld16v<3072, true>(bf[aa][7], vl, c.ws, so + 4096);
      ld16v<0, true>(bf[aa][8], vl, c.ws, so + 8192); ld16v<1024, true>(bf[aa][9], vl, c.ws, so + 8192); ld16v<2048, true>(bf[aa][10], vlast, c.ws, so + 8192);
    }
    f32x4 acc[2][4];
#define DN_K(AA, K) { vmwait<21 - 11 * (AA) - (K)>(); pinv(bf[AA][K]);                                   \
      if constexpr ((K) == 0) { mm16az(acc[AA][0], wd[0], bf[AA][0]); mm16az(acc[AA][1], wd[1], bf[AA][0]); mm16az(acc[AA][2], wd[2], bf[AA][0]); mm16az(acc[AA][3], wd[3], bf[AA][0]); } \
      else { mm16a(acc[AA][0], wd[4 * (K)], bf[AA][K]); mm16a(acc[AA][1], wd[4 * (K) + 1], bf[AA][K]); mm16a(acc[AA][2], wd[4 * (K) + 2], bf[AA][K]); mm16a(acc[AA][3], wd[4 * (K) + 3], bf[AA][K]); } }
#define DN_ROW(AA) DN_K(AA, 0) DN_K(AA, 1) DN_K(AA, 2) DN_K(AA, 3) DN_K(AA, 4) DN_K(AA, 5) DN_K(AA, 6) DN_K(AA, 7) DN_K(AA, 8) DN_K(AA, 9) DN_K(AA, 10)
    DN_ROW(0) DN_ROW(1)
    nops<8>();
#pragma unroll
    for (int t = 0; t < 4; ++t) { pinv(acc[0][t]); pinv(acc[1][t]); }
    if (X == 1) lgm_bar();
    xch_write(acc, w, lane);
    lgm_bar();
    f32x4 r0 = acc[0][0], r1 = acc[0][1];
    xch_read(r0, r1, w, lane);
    const u32x4 hv = hres[X];
    u32x4 ov;
    ov.x = cvtpk(r0[0] + bflo(hv.x), r0[1] + bfhi(hv.x)); ov.y = cvtpk(r0[2] + bflo(hv.y), r0[3] + bfhi(hv.y));
    ov.z = cvtpk(r1[0] + bflo(hv.z), r1[1] + bfhi(hv.z)); ov.w = cvtpk(r1[2] + bflo(hv.w), r1[3] + bfhi(hv.w));
    st16<0, false>(ov, vres, srdo, X * 32768u);
    STAMP(18 + 2 * X);
  }
}
}

extern "C" void kernel_launch(void* const* d_in, const int* in_sizes, int n_in, void* d_out, int out_size, void* d_ws, size_t ws_size, hipStream_t stream) {
  Args a;
  a.x = d_in[0]; a.wqkv = d_in[1]; a.wo = d_in[2]; a.wg = d_in[4]; a.wu = d_in[5]; a.wd = d_in[6]; a.out = d_out; a.ws = d_ws;
  (void)hipFuncSetAttribute((const void*)fused_block, hipFuncAttributeMaxDynamicSharedMemorySize, LDS_BYTES);
  fused_block<<<256, 256, LDS_BYTES, stream>>>(a);
}
```

```cpp
#include <hip/hip_runtime.h>
#include <stdint.h>

namespace {
#define KERNEL_ASM \
  "s_load_dwordx16 s[72:87], s[0:1], 0x0\n\t" \
  "v_lshrrev_b32 v6, 6, v0\n\t" \
  "v_and_b32 v1, 63, v0\n\t" \
  "v_readfirstlane_b32 s24, v6\n\t" \
  "s_and_b32 s60, s2, 7\n\t" \
  "s_lshr_b32 s61, s2, 3\n\t" \
  "s_lshr_b32 s32, s60, 1\n\t" \
  "s_lshr_b32 s33, s61, 2\n\t" \
  "s_lshl3_add_u32 s25, s32, s33\n\t" \
  "v_lshrrev_b32 v2, 3, v1\n\t" \
  "v_and_b32 v3, 7, v1\n\t" \
  "v_lshlrev_b32 v4, 4, v3\n\t" \
  "v_lshl_or_b32 v14, v2, 10, v4\n\t" \
  "s_bfe_u32 s3, s61, 0x20002\n\t" \
  "s_lshl_b32 s3, s3, 1\n\t" \
  "v_lshl_or_b32 v54, s3, 7, v14\n\t" \
  "s_lshl2_add_u32 s88, s25, s24\n\t" \
  "s_lshl_b32 s88, s88, 14\n\t" \
  "s_add_u32 s89, s88, 8192\n\t" \
  "v_mov_b32 v7, 0\n\t" \
  "s_waitcnt lgkmcnt(0)\n\t" \
  "s_mov_b32 s8, s72\n\t" \
  "s_and_b32 s9, s73, 0xffff\n\t" \
  "s_mov_b32 s10, 2097152\n\t" \
  "s_mov_b32 s11, 0x20000\n\t" \
  "buffer_load_dwordx4 v[64:67], v54, s[8:11], s88 offen offset:0\n\t" \
  "v_xor_b32 v55, 256, v54\n\t" \
  "v_xor_b32 v56, 512, v54\n\t" \
  "v_xor_b32 v57, 768, v54\n\t" \
  "buffer_load_dwordx4 v[68:71], v54, s[8:11], s88 offen offset:128\n\t" \
  "s_mov_b32 s4, s86\n\t" \
  "s_and_b32 s5, s87, 0xffff\n\t" \
  "s_mov_b32 s6, 0x1370000\n\t" \
  "s_mov_b32 s7, 0x20000\n\t" \
  "buffer_load_dwordx2 v[26:27], v7, s[4:7], 0 offen offset:0 sc1\n\t" \
  "v_lshlrev_b32 v5, 7, v1\n\t" \
  "v_bfrev_b32 v8, 1\n\t" \
  "s_mov_b64 s[32:33], 1023\n\t" \
  "v_cndmask_b32 v5, v8, v5, s[32:33]\n\t" \
  "s_getpc_b64 s[20:21]\n\tLpc_%=:\n\t" \
  "buffer_load_dwordx4 v[72:75], v55, s[8:11], s88 offen offset:0\n\t" \
  "s_and_b32 s20, s20, 0xffffff80\n\t" \
  "s_and_b32 s21, s21, 0xffff\n\t" \
  "s_mov_b32 s22, Lend_%=-Lpc_%=\n\t" \
  "s_mov_b32 s23, 0x20000\n\t" \
  "s_mul_i32 s32, s61, 1280\n\t" \
  "s_cmp_eq_u32 s24, 0\n\t" \
  "s_cselect_b32 s32, s32, 0x80000000\n\t" \
  "buffer_load_dword v30, v5, s[20:23], s32 offen offset:0\n\t" \
  "s_mov_b32 s12, s74\n\t" \
  "s_and_b32 s13, s75, 0xffff\n\t" \
  "buffer_load_dwordx4 v[76:79], v55, s[8:11], s88 offen offset:128\n\t" \
  "s_mov_b32 s14, 1572864\n\t" \
  "s_mov_b32 s15, 0x20000\n\t" \
  "s_mov_b64 s[98:99], s[84:85]\n\t" \
  "s_and_b32 s32, s60, 1\n\t" \
  "s_and_b32 s35, s61, 3\n\t" \
  "s_lshl2_add_u32 s97, s32, s35\n\t" \
  "s_lshl2_add_u32 s26, s97, s24\n\t" \
  "v_lshrrev_b32 v13, 4, v1\n\t" \
  "v_lshrrev_b32 v9, 5, v1\n\t" \
  "v_and_b32 v12, 1, v13\n\t" \
  "buffer_load_dwordx4 v[80:83], v56, s[8:11], s88 offen offset:0\n\t" \
  "v_bfe_u32 v10, v1, 2, 2\n\t" \
  "v_and_b32 v11, 3, v1\n\t" \
  "v_lshrrev_b32 v6, 1, v3\n\t" \
  "v_and_b32 v7, 16, v4\n\t" \
  "s_movk_i32 s32, 3072\n\t" \
  "s_lshl_b32 s34, s97, 7\n\t" \
  "v_and_b32 v8, 3, v2\n\t" \
  "v_xor_b32 v8, v6, v8\n\t" \
  "v_lshl_or_b32 v8, v8, 5, v7\n\t" \
  "v_mad_u32_u24 v8, v2, s32, v8\n\t" \
  "buffer_load_dwordx4 v[84:87], v56, s[8:11], s88 offen offset:128\n\t" \
  "s_mul_i32 s33, s24, 24576\n\t" \
  "s_add_u32 s33, s33, s34\n\t" \
  "v_add_u32 v48, s33, v8\n\t" \
  "v_add_u32 v49, 98304, v48\n\t" \
  "s_lshl_b32 s84, s24, 10\n\t" \
  "s_mov_b32 s85, 0x3f803f80\n\t" \
  "s_lshr_b32 s32, s24, 1\n\t" \
  "s_lshl_b32 s32, s32, 12\n\t" \
  "s_and_b32 s33, s24, 1\n\t" \
  "s_lshl_b32 s34, s33, 8\n\t" \
  "buffer_load_dwordx4 v[88:91], v57, s[8:11], s88 offen offset:0\n\t" \
  "s_add_u32 s32, s32, s34\n\t" \
  "v_xor_b32 v8, v2, v3\n\t" \
  "v_lshlrev_b32 v8, 4, v8\n\t" \
  "v_lshl_or_b32 v8, v3, 9, v8\n\t" \
  "v_add_u32 v32, s32, v8\n\t" \
  "v_add_u32 v33, 128, v32\n\t" \
  "v_and_b32 v8, 31, v1\n\t" \
  "v_xor_b32 v8, v8, v9\n\t" \
  "v_lshlrev_b32 v8, 4, v8\n\t" \
  "v_lshl_or_b32 v8, v9, 9, v8\n\t" \
  "buffer_load_dwordx4 v[92:95], v57, s[8:11], s88 offen offset:128\n\t" \
  "s_lshl_b32 s33, s33, 12\n\t" \
  "v_or_b32 v34, s33, v8\n\t" \
  "v_xor_b32 v35, 1056, v34\n\t" \
  "v_xor_b32 v36, 2112, v34\n\t" \
  "v_xor_b32 v37, 3168, v34\n\t" \
  "v_lshl_or_b32 v6, v9, 3, v10\n\t" \
  "v_lshlrev_b32 v7, 3, v11\n\t" \
  "s_and_b32 s32, s24, 2\n\t" \
  "v_or_b32 v8, s32, v12\n\t" \
  "v_xor_b32 v8, v8, v10\n\t" \
  "buffer_load_dwordx4 v[96:99], v54, s[8:11], s89 offen offset:0\n\t" \
  "v_lshl_or_b32 v8, v8, 5, v7\n\t" \
  "v_lshl_or_b32 v8, v6, 7, v8\n\t" \
  "v_add_u32 v38, 65536, v8\n\t" \
  "v_mov_b32 v39, v8\n\t" \
  "s_mul_i32 s51, s24, 12288\n\t" \
  "s_add_u32 s51, s51, 131072\n\t" \
  "v_lshlrev_b32 v24, 4, v1\n\t" \
  "v_bfrev_b32 v8, 1\n\t" \
  "s_mov_b64 s[32:33], 1\n\t" \
  "v_cndmask_b32 v25, v8, 0, s[32:33]\n\t" \
  "buffer_load_dwordx4 v[100:103], v54, s[8:11], s89 offen offset:128\n\t" \
  "s_lshr_b32 s32, s61, 3\n\t" \
  "s_lshl2_add_u32 s27, s60, s32\n\t" \
  "s_and_b32 s28, s61, 7\n\t" \
  "s_lshr_b32 s32, s60, 2\n\t" \
  "s_lshr_b32 s33, s61, 2\n\t" \
  "s_lshl3_add_u32 s29, s32, s33\n\t" \
  "s_and_b32 s32, s60, 3\n\t" \
  "s_lshl2_add_u32 s32, s32, s35\n\t" \
  "s_lshl2_add_u32 s30, s32, s24\n\t" \
  "s_mul_i32 s31, s24, 20480\n\t" \
  "buffer_load_dwordx4 v[104:107], v55, s[8:11], s89 offen offset:0\n\t" \
  "s_lshl2_add_u32 s94, s2, s24\n\t" \
  "s_mul_i32 s94, s94, 1280\n\t" \
  "s_add_u32 s95, s94, 393216\n\t" \
  "v_bfrev_b32 v8, 1\n\t" \
  "s_lshl_b32 s96, s24, 8\n\t" \
  "s_add_u32 s96, s96, s2\n\t" \
  "s_sub_u32 s36, s96, 352\n\t" \
  "s_mul_i32 s37, s36, 2979\n\t" \
  "s_lshr_b32 s37, s37, 16\n\t" \
  "s_mul_i32 s32, s37, 22\n\t" \
  "buffer_load_dwordx4 v[108:111], v55, s[8:11], s89 offen offset:128\n\t" \
  "s_sub_u32 s32, s36, s32\n\t" \
  "s_lshl_b32 s32, s32, 3\n\t" \
  "s_mul_i32 s33, s32, 171\n\t" \
  "s_lshr_b32 s33, s33, 9\n\t" \
  "s_mul_i32 s34, s33, 3\n\t" \
  "s_sub_u32 s38, s32, s34\n\t" \
  "s_mul_i32 s43, s33, 24576\n\t" \
  "s_lshl_b32 s32, s37, 7\n\t" \
  "s_add_u32 s43, s43, s32\n\t" \
  "s_mov_b32 s39, 0x18040000\n\t" \
  "buffer_load_dwordx4 v[112:115], v56, s[8:11], s89 offen offset:0\n\t" \
  "s_mov_b32 s40, 0x30301c18\n\t" \
  "s_mov_b32 s41, 146\n\t" \
  "s_cmp_eq_u32 s38, 1\n\t" \
  "s_cselect_b32 s39, 0x18180400, s39\n\t" \
  "s_cselect_b32 s40, 0x3430301c, s40\n\t" \
  "s_cselect_b32 s41, 73, s41\n\t" \
  "s_cmp_eq_u32 s38, 2\n\t" \
  "s_cselect_b32 s39, 0x1c181804, s39\n\t" \
  "s_cselect_b32 s40, 0x48343030, s40\n\t" \
  "s_cselect_b32 s41, 36, s41\n\t" \
  "buffer_load_dwordx4 v[116:119], v56, s[8:11], s89 offen offset:128\n\t" \
  "s_lshl_b32 s56, s36, 13\n\t" \
  "s_add_u32 s56, s56, 0xf40000\n\t" \
  "s_sub_u32 s32, s96, 528\n\t" \
  "s_and_b32 s33, s32, 7\n\t" \
  "s_lshl_b32 s33, s33, 16\n\t" \
  "s_lshl_b32 s34, s32, 4\n\t" \
  "s_and_b32 s34, s34, 0x380\n\t" \
  "s_add_u32 s33, s33, s34\n\t" \
  "s_lshl_b32 s32, s32, 13\n\t" \
  "s_add_u32 s32, s32, 0xec0000\n\t" \
  "buffer_load_dwordx4 v[120:123], v57, s[8:11], s89 offen offset:0\n\t" \
  "s_mov_b32 s44, 0x9080b0a\n\t" \
  "s_mov_b32 s45, 0x13121110\n\t" \
  "s_mov_b32 s18, 0x80000\n\t" \
  "s_movk_i32 s57, 0x400\n\t" \
  "s_movk_i32 s47, 128\n\t" \
  "s_movk_i32 s49, 1024\n\t" \
  "s_cmpk_lt_u32 s96, 528\n\t" \
  "s_cselect_b32 s39, s39, 0x14100400\n\t" \
  "s_cselect_b32 s40, s40, 0x34302420\n\t" \
  "s_cselect_b32 s41, s41, 0\n\t" \
  "s_cselect_b32 s43, s43, s33\n\t" \
  "s_cselect_b32 s56, s56, s32\n\t" \
  "s_cselect_b32 s44, s44, 0x3020100\n\t" \
  "s_cselect_b32 s45, s45, 0xb0a0908\n\t" \
  "s_cselect_b64 s[16:17], s[82:83], s[76:77]\n\t" \
  "s_cselect_b32 s18, 0x158000, s18\n\t" \
  "buffer_load_dwordx4 v[124:127], v57, s[8:11], s89 offen offset:128\n\t" \
  "s_lshr_b32 s32, s96, 4\n\t" \
  "s_and_b32 s33, s96, 15\n\t" \
  "s_mul_i32 s34, s33, 88064\n\t" \
  "s_lshl_b32 s35, s32, 7\n\t" \
  "s_add_u32 s34, s34, s35\n\t" \
  "s_lshl_b32 s35, s32, 17\n\t" \
  "s_lshl_b32 s33, s33, 10\n\t" \
  "s_add_u32 s33, s33, s35\n\t" \
  "s_add_u32 s33, s33, 0x10a0000\n\t" \
  "s_cmpk_lt_u32 s96, 352\n\t" \
  "s_cselect_b32 s39, 0x3020100, s39\n\t" \
  "s_cselect_b32 s40, 0x3020100, s40\n\t" \
  "s_cselect_b32 s41, 0, s41\n\t" \
  "s_cselect_b32 s43, s34, s43\n\t" \
  "s_cselect_b32 s56, s33, s56\n\t" \
  "s_cselect_b32 s44, 0x3020100, s44\n\t" \
  "s_cselect_b32 s45, 0x7060504, s45\n\t" \
  "s_cselect_b64 s[20:21], s[80:81], s[16:17]\n\t" \
  "s_cselect_b64 s[16:17], s[78:79], s[16:17]\n\t" \
  "s_cselect_b32 s42, 22016, s57\n\t" \
  "s_cselect_b32 s46, 2752, s57\n\t" \
  "s_cselect_b32 s57, 0x4000, s57\n\t" \
  "s_cselect_b32 s47, 288, s47\n\t" \
  "s_cselect_b32 s48, 5, 4\n\t" \
  "s_cselect_b32 s49, 2432, s49\n\t" \
  "s_cselect_b32 s50, 3, 4\n\t" \
  "s_xor_b32 s88, s3, 0\n\t" \
  "s_mul_i32 s88, s88, 196608\n\t" \
  "s_add_u32 s88, s88, 1024\n\t" \
  "s_add_u32 m0, s84, 65536\n\t" \
  "s_lshl_b32 s33, s96, 7\n\t" \
  "buffer_load_dwordx4 v48, s[12:15], s88 offen offset:0 lds\n\t" \
  "s_add_u32 s33, s33, 0x9000\n\t" \
  "s_cmpk_lt_u32 s96, 592\n\t" \
  "s_cselect_b32 s18, s18, 0\n\t" \
  "s_cselect_b32 s56, s56, 0x80000000\n\t" \
  "s_cselect_b32 s58, s33, 0x80000000\n\t" \
  "s_and_b32 s17, s17, 0xffff\n\t" \
  "s_and_b32 s21, s21, 0xffff\n\t" \
  "s_mov_b32 s19, 0x20000\n\t" \
  "s_mov_b32 s22, s18\n\t" \
  "v_lshlrev_b32 v6, 3, v2\n\t" \
  "v_lshrrev_b64 v[16:17], v6, s[44:45]\n\t" \
  "s_add_u32 m0, s84, 69632\n\t" \
  "v_and_b32 v16, 0xff, v16\n\t" \
  "buffer_load_dwordx4 v49, s[12:15], s88 offen offset:0 lds\n\t" \
  "v_mad_u32_u24 v16, v16, s46, v4\n\t" \
  "s_cmp_eq_u32 s32, 21\n\t" \
  "s_cselect_b32 s32, 0xf0f0f0f0, 0\n\t" \
  "s_mov_b32 s33, s32\n\t" \
  "v_cndmask_b32 v6, v16, v8, s[32:33]\n\t" \
  "v_and_b32 v7, 32, v1\n\t" \
  "v_lshlrev_b32 v7, 7, v7\n\t" \
  "v_sub_u32 v7, 0xffffe000, v7\n\t" \
  "s_bfe_u32 s32, s39, 0x80000\n\t" \
  "s_mul_i32 s32, s32, s42\n\t" \
  "s_add_u32 s64, s32, s43\n\t" \
  "s_add_u32 s88, s88, 1024\n\t" \
  "s_add_u32 m0, s84, 73728\n\t" \
  "s_bfe_u32 s33, s41, 0x10000\n\t" \
  "buffer_load_dwordx4 v48, s[12:15], s88 offen offset:0 lds\n\t" \
  "v_mad_i32_i24 v248, v7, s33, v6\n\t" \
  "s_bfe_u32 s32, s39, 0x80008\n\t" \
  "s_mul_i32 s32, s32, s42\n\t" \
  "s_add_u32 s65, s32, s43\n\t" \
  "s_bfe_u32 s33, s41, 0x10001\n\t" \
  "v_mad_i32_i24 v249, v7, s33, v6\n\t" \
  "s_bfe_u32 s32, s39, 0x80010\n\t" \
  "s_mul_i32 s32, s32, s42\n\t" \
  "s_add_u32 s66, s32, s43\n\t" \
  "s_bfe_u32 s33, s41, 0x10002\n\t" \
  "v_mad_i32_i24 v250, v7, s33, v6\n\t" \
  "s_add_u32 m0, s84, 77824\n\t" \
  "s_bfe_u32 s32, s39, 0x80018\n\t" \
  "buffer_load_dwordx4 v49, s[12:15], s88 offen offset:0 lds\n\t" \
  "s_mul_i32 s32, s32, s42\n\t" \
  "s_add_u32 s67, s32, s43\n\t" \
  "s_bfe_u32 s33, s41, 0x10003\n\t" \
  "v_mad_i32_i24 v251, v7, s33, v6\n\t" \
  "s_bfe_u32 s32, s40, 0x80000\n\t" \
  "s_mul_i32 s32, s32, s42\n\t" \
  "s_add_u32 s68, s32, s43\n\t" \
  "s_bfe_u32 s33, s41, 0x10004\n\t" \
  "v_mad_i32_i24 v252, v7, s33, v6\n\t" \
  "s_bfe_u32 s32, s40, 0x80008\n\t" \
  "s_mul_i32 s32, s32, s42\n\t" \
  "s_xor_b32 s88, s3, 1\n\t" \
  "s_mul_i32 s88, s88, 196608\n\t" \
  "s_add_u32 s88, s88, 1024\n\t" \
  "s_add_u32 m0, s84, 81920\n\t" \
  "s_add_u32 s69, s32, s43\n\t" \
  "buffer_load_dwordx4 v48, s[12:15], s88 offen offset:0 lds\n\t" \
  "s_bfe_u32 s33, s41, 0x10005\n\t" \
  "v_mad_i32_i24 v253, v7, s33, v6\n\t" \
  "s_bfe_u32 s32, s40, 0x80010\n\t" \
  "s_mul_i32 s32, s32, s42\n\t" \
  "s_add_u32 s70, s32, s43\n\t" \
  "s_bfe_u32 s33, s41, 0x10006\n\t" \
  "v_mad_i32_i24 v254, v7, s33, v6\n\t" \
  "s_bfe_u32 s32, s40, 0x80018\n\t" \
  "s_mul_i32 s32, s32, s42\n\t" \
  "s_add_u32 s71, s32, s43\n\t" \
  "s_bfe_u32 s33, s41, 0x10007\n\t" \
  "s_add_u32 m0, s84, 86016\n\t" \
  "v_mad_i32_i24 v255, v7, s33, v6\n\t" \
  "buffer_load_dwordx4 v49, s[12:15], s88 offen offset:0 lds\n\t" \
  "v_lshlrev_b32 v8, s48, v3\n\t" \
  "v_mad_u32_u24 v8, v2, s47, v8\n\t" \
  "v_add_u32 v28, s51, v8\n\t" \
  "v_lshlrev_b32 v8, s50, v11\n\t" \
  "v_mad_u32_u24 v8, v10, s47, v8\n\t" \
  "v_mad_u32_u24 v8, v13, s49, v8\n\t" \
  "v_add_u32 v29, s51, v8\n\t" \
  "s_add_u32 s88, s88, 1024\n\t" \
  "s_add_u32 m0, s84, 90112\n\t" \
  "s_nop 0\n\t" \
  "buffer_load_dwordx4 v48, s[12:15], s88 offen offset:0 lds\n\t" \
  "s_add_u32 m0, s84, 94208\n\t" \
  "s_nop 0\n\t" \
  "buffer_load_dwordx4 v49, s[12:15], s88 offen offset:0 lds\n\t" \
  "s_lshl2_add_u32 s32, s2, s24\n\t" \
  "s_lshr_b32 s33, s32, 1\n\t" \
  "s_mul_i32 s33, s33, 3072\n\t" \
  "s_and_b32 s32, s32, 1\n\t" \
  "s_lshl_b32 s32, s32, 9\n\t" \
  "s_add_u32 s95, s32, s33\n\t" \
  "s_mov_b64 s[32:33], 15\n\t" \
  "v_bfrev_b32 v8, 1\n\t" \
  "v_cndmask_b32 v13, v8, v5, s[32:33]\n\t" \
  "s_waitcnt vmcnt(16)\n\t" \
  "v_readfirstlane_b32 s54, v26\n\t" \
  "v_readfirstlane_b32 s55, v27\n\t" \
  "s_add_u32 s54, s54, 0x7f4a7c15\n\t" \
  "s_addc_u32 s55, s55, 0x9e3779b9\n\t" \
  "v_mov_b32 v26, s54\n\t" \
  "v_mov_b32 v27, s55\n\t" \
  "v_dot2_f32_bf16 v16, v64, s85, 0\n\t" \
  "v_dot2_f32_bf16 v18, v64, v64, 0\n\t" \
  "v_dot2_f32_bf16 v17, v65, s85, 0\n\t" \
  "v_dot2_f32_bf16 v19, v65, v65, 0\n\t" \
  "v_dot2_f32_bf16 v16, v66, s85, v16\n\t" \
  "v_dot2_f32_bf16 v18, v66, v66, v18\n\t" \
  "v_dot2_f32_bf16 v17, v67, s85, v17\n\t" \
  "v_dot2_f32_bf16 v19, v67, v67, v19\n\t" \
  "buffer_load_dwordx4 v[224:227], v248, s[16:19], s64 offen offset:0 nt\n\t" \
  "v_dot2_f32_bf16 v16, v68, s85, v16\n\t" \
  "v_dot2_f32_bf16 v18, v68, v68, v18\n\t" \
  "v_dot2_f32_bf16 v17, v69, s85, v17\n\t" \
  "v_dot2_f32_bf16 v19, v69, v69, v19\n\t" \
  "v_dot2_f32_bf16 v16, v70, s85, v16\n\t" \
  "v_dot2_f32_bf16 v18, v70, v70, v18\n\t" \
  "v_dot2_f32_bf16 v17, v71, s85, v17\n\t" \
  "v_dot2_f32_bf16 v19, v71, v71, v19\n\t" \
  "buffer_load_dwordx4 v[228:231], v249, s[16:19], s65 offen offset:0 nt\n\t" \
  "v_dot2_f32_bf16 v16, v72, s85, v16\n\t" \
  "v_dot2_f32_bf16 v18, v72, v72, v18\n\t" \
  "v_dot2_f32_bf16 v17, v73, s85, v17\n\t" \
  "v_dot2_f32_bf16 v19, v73, v73, v19\n\t" \
  "v_dot2_f32_bf16 v16, v74, s85, v16\n\t" \
  "v_dot2_f32_bf16 v18, v74, v74, v18\n\t" \
  "v_dot2_f32_bf16 v17, v75, s85, v17\n\t" \
  "v_dot2_f32_bf16 v19, v75, v75, v19\n\t" \
  "buffer_load_dwordx4 v[232:235], v250, s[16:19], s66 offen offset:0 nt\n\t" \
  "v_dot2_f32_bf16 v16, v76, s85, v16\n\t" \
  "v_dot2_f32_bf16 v18, v76, v76, v18\n\t" \
  "v_dot2_f32_bf16 v17, v77, s85, v17\n\t" \
  "v_dot2_f32_bf16 v19, v77, v77, v19\n\t" \
  "v_dot2_f32_bf16 v16, v78, s85, v16\n\t" \
  "v_dot2_f32_bf16 v18, v78, v78, v18\n\t" \
  "v_dot2_f32_bf16 v17, v79, s85, v17\n\t" \
  "v_dot2_f32_bf16 v19, v79, v79, v19\n\t" \
  "buffer_load_dwordx4 v[236:239], v251, s[16:19], s67 offen offset:0 nt\n\t" \
  "v_dot2_f32_bf16 v16, v80, s85, v16\n\t" \
  "v_dot2_f32_bf16 v18, v80, v80, v18\n\t" \
  "v_dot2_f32_bf16 v17, v81, s85, v17\n\t" \
  "v_dot2_f32_bf16 v19, v81, v81, v19\n\t" \
  "v_dot2_f32_bf16 v16, v82, s85, v16\n\t" \
  "v_dot2_f32_bf16 v18, v82, v82, v18\n\t" \
  "v_dot2_f32_bf16 v17, v83, s85, v17\n\t" \
  "v_dot2_f32_bf16 v19, v83, v83, v19\n\t" \
  "buffer_load_dwordx4 v[240:243], v252, s[20:23], s68 offen offset:0 nt\n\t" \
  "v_dot2_f32_bf16 v16, v84, s85, v16\n\t" \
  "v_dot2_f32_bf16 v18, v84, v84, v18\n\t" \
  "v_dot2_f32_bf16 v17, v85, s85, v17\n\t" \
  "v_dot2_f32_bf16 v19, v85, v85, v19\n\t" \
  "v_dot2_f32_bf16 v16, v86, s85, v16\n\t" \
  "v_dot2_f32_bf16 v18, v86, v86, v18\n\t" \
  "v_dot2_f32_bf16 v17, v87, s85, v17\n\t" \
  "v_dot2_f32_bf16 v19, v87, v87, v19\n\t" \
  "buffer_load_dwordx4 v[244:247], v253, s[20:23], s69 offen offset:0 nt\n\t" \
  "v_dot2_f32_bf16 v16, v88, s85, v16\n\t" \
  "v_dot2_f32_bf16 v18, v88, v88, v18\n\t" \
  "v_dot2_f32_bf16 v17, v89, s85, v17\n\t" \
  "v_dot2_f32_bf16 v19, v89, v89, v19\n\t" \
  "v_dot2_f32_bf16 v16, v90, s85, v16\n\t" \
  "v_dot2_f32_bf16 v18, v90, v90, v18\n\t" \
  "v_dot2_f32_bf16 v17, v91, s85, v17\n\t" \
  "v_dot2_f32_bf16 v19, v91, v91, v19\n\t" \
  "buffer_load_dwordx4 v[248:251], v254, s[20:23], s70 offen offset:0 nt\n\t" \
  "v_dot2_f32_bf16 v16, v92, s85, v16\n\t" \
  "v_dot2_f32_bf16 v18, v92, v92, v18\n\t" \
  "v_dot2_f32_bf16 v17, v93, s85, v17\n\t" \
  "v_dot2_f32_bf16 v19, v93, v93, v19\n\t" \
  "v_dot2_f32_bf16 v16, v94, s85, v16\n\t" \
  "v_dot2_f32_bf16 v18, v94, v94, v18\n\t" \
  "v_dot2_f32_bf16 v17, v95, s85, v17\n\t" \
  "v_dot2_f32_bf16 v19, v95, v95, v19\n\t" \
  "buffer_load_dwordx4 v[252:255], v255, s[20:23], s71 offen offset:0 nt\n\t" \
  "s_waitcnt vmcnt(16)\n\t" \
  "v_dot2_f32_bf16 v20, v96, s85, 0\n\t" \
  "v_dot2_f32_bf16 v22, v96, v96, 0\n\t" \
  "v_dot2_f32_bf16 v21, v97, s85, 0\n\t" \
  "v_dot2_f32_bf16 v23, v97, v97, 0\n\t" \
  "v_dot2_f32_bf16 v20, v98, s85, v20\n\t" \
  "v_dot2_f32_bf16 v22, v98, v98, v22\n\t" \
  "v_dot2_f32_bf16 v21, v99, s85, v21\n\t" \
  "v_dot2_f32_bf16 v23, v99, v99, v23\n\t" \
  "buffer_load_dword v31, v13, s[12:15], s95 offen offset:0\n\t" \
  "v_dot2_f32_bf16 v20, v100, s85, v20\n\t" \
  "v_dot2_f32_bf16 v22, v100, v100, v22\n\t" \
  "v_dot2_f32_bf16 v21, v101, s85, v21\n\t" \
  "v_dot2_f32_bf16 v23, v101, v101, v23\n\t" \
  "v_dot2_f32_bf16 v20, v102, s85, v20\n\t" \
  "v_dot2_f32_bf16 v22, v102, v102, v22\n\t" \
  "v_dot2_f32_bf16 v21, v103, s85, v21\n\t" \
  "v_dot2_f32_bf16 v23, v103, v103, v23\n\t" \
  "buffer_load_dword v31, v5, s[4:7], s94 offen offset:0\n\t" \
  "v_dot2_f32_bf16 v20, v104, s85, v20\n\t" \
  "v_dot2_f32_bf16 v22, v104, v104, v22\n\t" \
  "v_dot2_f32_bf16 v21, v105, s85, v21\n\t" \
  "v_dot2_f32_bf16 v23, v105, v105, v23\n\t" \
  "v_dot2_f32_bf16 v20, v106, s85, v20\n\t" \
  "v_dot2_f32_bf16 v22, v106, v106, v22\n\t" \
  "v_dot2_f32_bf16 v21, v107, s85, v21\n\t" \
  "v_dot2_f32_bf16 v23, v107, v107, v23\n\t" \
  "v_dot2_f32_bf16 v20, v108, s85, v20\n\t" \
  "v_dot2_f32_bf16 v22, v108, v108, v22\n\t" \
  "v_dot2_f32_bf16 v21, v109, s85, v21\n\t" \
  "v_dot2_f32_bf16 v23, v109, v109, v23\n\t" \
  "v_dot2_f32_bf16 v20, v110, s85, v20\n\t" \
  "v_dot2_f32_bf16 v22, v110, v110, v22\n\t" \
  "v_dot2_f32_bf16 v21, v111, s85, v21\n\t" \
  "v_dot2_f32_bf16 v23, v111, v111, v23\n\t" \
  "v_dot2_f32_bf16 v20, v112, s85, v20\n\t" \
  "v_dot2_f32_bf16 v22, v112, v112, v22\n\t" \
  "v_dot2_f32_bf16 v21, v113, s85, v21\n\t" \
  "v_dot2_f32_bf16 v23, v113, v113, v23\n\t" \
  "v_dot2_f32_bf16 v20, v114, s85, v20\n\t" \
  "v_dot2_f32_bf16 v22, v114, v114, v22\n\t" \
  "v_dot2_f32_bf16 v21, v115, s85, v21\n\t" \
  "v_dot2_f32_bf16 v23, v115, v115, v23\n\t" \
  "v_dot2_f32_bf16 v20, v116, s85, v20\n\t" \
  "v_dot2_f32_bf16 v22, v116, v116, v22\n\t" \
  "v_dot2_f32_bf16 v21, v117, s85, v21\n\t" \
  "v_dot2_f32_bf16 v23, v117, v117, v23\n\t" \
  "v_dot2_f32_bf16 v20, v118, s85, v20\n\t" \
  "v_dot2_f32_bf16 v22, v118, v118, v22\n\t" \
  "v_dot2_f32_bf16 v21, v119, s85, v21\n\t" \
  "v_dot2_f32_bf16 v23, v119, v119, v23\n\t" \
  "v_dot2_f32_bf16 v20, v120, s85, v20\n\t" \
  "v_dot2_f32_bf16 v22, v120, v120, v22\n\t" \
  "v_dot2_f32_bf16 v21, v121, s85, v21\n\t" \
  "v_dot2_f32_bf16 v23, v121, v121, v23\n\t" \
  "v_dot2_f32_bf16 v20, v122, s85, v20\n\t" \
  "v_dot2_f32_bf16 v22, v122, v122, v22\n\t" \
  "v_dot2_f32_bf16 v21, v123, s85, v21\n\t" \
  "v_dot2_f32_bf16 v23, v123, v123, v23\n\t" \
  "v_dot2_f32_bf16 v20, v124, s85, v20\n\t" \
  "v_dot2_f32_bf16 v22, v124, v124, v22\n\t" \
  "v_dot2_f32_bf16 v21, v125, s85, v21\n\t" \
  "v_dot2_f32_bf16 v23, v125, v125, v23\n\t" \
  "v_dot2_f32_bf16 v20, v126, s85, v20\n\t" \
  "v_dot2_f32_bf16 v22, v126, v126, v22\n\t" \
  "v_dot2_f32_bf16 v21, v127, s85, v21\n\t" \
  "v_dot2_f32_bf16 v23, v127, v127, v23\n\t" \
  "v_add_f32 v16, v16, v17\n\t" \
  "v_add_f32 v18, v18, v19\n\t" \
  "v_add_f32 v20, v20, v21\n\t" \
  "v_add_f32 v22, v22, v23\n\t" \
  "v_add_f32_dpp v16, v16, v16 quad_perm:[1,0,3,2] row_mask:0xf bank_mask:0xf\n\t" \
  "v_add_f32_dpp v18, v18, v18 quad_perm:[1,0,3,2] row_mask:0xf bank_mask:0xf\n\t" \
  "v_add_f32_dpp v20, v20, v20 quad_perm:[1,0,3,2] row_mask:0xf bank_mask:0xf\n\t" \
  "v_add_f32_dpp v22, v22, v22 quad_perm:[1,0,3,2] row_mask:0xf bank_mask:0xf\n\t" \
  "v_add_f32_dpp v16, v16, v16 quad_perm:[2,3,0,1] row_mask:0xf bank_mask:0xf\n\t" \
  "v_add_f32_dpp v18, v18, v18 quad_perm:[2,3,0,1] row_mask:0xf bank_mask:0xf\n\t" \
  "v_add_f32_dpp v20, v20, v20 quad_perm:[2,3,0,1] row_mask:0xf bank_mask:0xf\n\t" \
  "v_add_f32_dpp v22, v22, v22 quad_perm:[2,3,0,1] row_mask:0xf bank_mask:0xf\n\t" \
  "v_add_f32_dpp v16, v16, v16 row_half_mirror row_mask:0xf bank_mask:0xf\n\t" \
  "v_add_f32_dpp v18, v18, v18 row_half_mirror row_mask:0xf bank_mask:0xf\n\t" \
  "v_add_f32_dpp v20, v20, v20 row_half_mirror row_mask:0xf bank_mask:0xf\n\t" \
  "v_add_f32_dpp v22, v22, v22 row_half_mirror row_mask:0xf bank_mask:0xf\n\t" \
  "v_mul_f32 v16, 0x3b000000, v16\n\t" \
  "v_mul_f32 v18, 0x3b000000, v18\n\t" \
  "v_mul_f32 v20, 0x3b000000, v20\n\t" \
  "v_mul_f32 v22, 0x3b000000, v22\n\t" \
  "v_fma_f32 v18, -v16, v16, v18\n\t" \
  "v_fma_f32 v22, -v20, v20, v22\n\t" \
  "v_max_f32 v18, 0, v18\n\t" \
  "v_max_f32 v22, 0, v22\n\t" \
  "v_add_f32 v18, 0x3727c5ac, v18\n\t" \
  "v_add_f32 v22, 0x3727c5ac, v22\n\t" \
  "v_rsq_f32 v60, v18\n\t" \
  "v_rsq_f32 v61, v22\n\t" \
  "v_mul_f32_e64 v62, -v16, v60\n\t" \
  "v_mul_f32_e64 v63, -v20, v61\n\t" \
  "s_add_u32 s32, s84, 98304\n\t" \
  "v_add_u32 v15, s32, v24\n\t" \
  "v_lshlrev_b32 v16, 16, v64\n\t" \
  "v_and_b32 v17, 0xffff0000, v64\n\t" \
  "v_lshlrev_b32 v18, 16, v65\n\t" \
  "v_and_b32 v19, 0xffff0000, v65\n\t" \
  "v_lshlrev_b32 v20, 16, v66\n\t" \
  "v_and_b32 v21, 0xffff0000, v66\n\t" \
  "v_lshlrev_b32 v22, 16, v67\n\t" \
  "v_and_b32 v23, 0xffff0000, v67\n\t" \
  "v_pk_fma_f32 v[16:17], v[16:17], v[60:61], v[62:63] op_sel:[0,0,0] op_sel_hi:[1,0,0]\n\t" \
  "v_pk_fma_f32 v[18:19], v[18:19], v[60:61], v[62:63] op_sel:[0,0,0] op_sel_hi:[1,0,0]\n\t" \
  "v_pk_fma_f32 v[20:21], v[20:21], v[60:61], v[62:63] op_sel:[0,0,0] op_sel_hi:[1,0,0]\n\t" \
  "v_pk_fma_f32 v[22:23], v[22:23], v[60:61], v[62:63] op_sel:[0,0,0] op_sel_hi:[1,0,0]\n\t" \
  "v_cvt_pk_bf16_f32 v64, v16, v17\n\t" \
  "v_cvt_pk_bf16_f32 v65, v18, v19\n\t" \
  "v_cvt_pk_bf16_f32 v66, v20, v21\n\t" \
  "v_cvt_pk_bf16_f32 v67, v22, v23\n\t" \
  "ds_write_b128 v32, v[64:67] offset:0\n\t" \
  "v_lshlrev_b32 v16, 16, v96\n\t" \
  "v_and_b32 v17, 0xffff0000, v96\n\t" \
  "v_lshlrev_b32 v18, 16, v97\n\t" \
  "v_and_b32 v19, 0xffff0000, v97\n\t" \
  "v_lshlrev_b32 v20, 16, v98\n\t" \
  "v_and_b32 v21, 0xffff0000, v98\n\t" \
  "v_lshlrev_b32 v22, 16, v99\n\t" \
  "v_and_b32 v23, 0xffff0000, v99\n\t" \
  "v_pk_fma_f32 v[16:17], v[16:17], v[60:61], v[62:63] op_sel:[0,1,1] op_sel_hi:[1,1,1]\n\t" \
  "v_pk_fma_f32 v[18:19], v[18:19], v[60:61], v[62:63] op_sel:[0,1,1] op_sel_hi:[1,1,1]\n\t" \
  "v_pk_fma_f32 v[20:21], v[20:21], v[60:61], v[62:63] op_sel:[0,1,1] op_sel_hi:[1,1,1]\n\t" \
  "v_pk_fma_f32 v[22:23], v[22:23], v[60:61], v[62:63] op_sel:[0,1,1] op_sel_hi:[1,1,1]\n\t" \
  "v_cvt_pk_bf16_f32 v96, v16, v17\n\t" \
  "v_cvt_pk_bf16_f32 v97, v18, v19\n\t" \
  "v_cvt_pk_bf16_f32 v98, v20, v21\n\t" \
  "v_cvt_pk_bf16_f32 v99, v22, v23\n\t" \
  "ds_write_b128 v33, v[96:99] offset:0\n\t" \
  "v_lshlrev_b32 v16, 16, v68\n\t" \
  "v_and_b32 v17, 0xffff0000, v68\n\t" \
  "v_lshlrev_b32 v18, 16, v69\n\t" \
  "v_and_b32 v19, 0xffff0000, v69\n\t" \
  "v_lshlrev_b32 v20, 16, v70\n\t" \
  "v_and_b32 v21, 0xffff0000, v70\n\t" \
  "v_lshlrev_b32 v22, 16, v71\n\t" \
  "v_and_b32 v23, 0xffff0000, v71\n\t" \
  "v_pk_fma_f32 v[16:17], v[16:17], v[60:61], v[62:63] op_sel:[0,0,0] op_sel_hi:[1,0,0]\n\t" \
  "v_pk_fma_f32 v[18:19], v[18:19], v[60:61], v[62:63] op_sel:[0,0,0] op_sel_hi:[1,0,0]\n\t" \
  "v_pk_fma_f32 v[20:21], v[20:21], v[60:61], v[62:63] op_sel:[0,0,0] op_sel_hi:[1,0,0]\n\t" \
  "v_pk_fma_f32 v[22:23], v[22:23], v[60:61], v[62:63] op_sel:[0,0,0] op_sel_hi:[1,0,0]\n\t" \
  "v_cvt_pk_bf16_f32 v68, v16, v17\n\t" \
  "v_cvt_pk_bf16_f32 v69, v18, v19\n\t" \
  "v_cvt_pk_bf16_f32 v70, v20, v21\n\t" \
  "v_cvt_pk_bf16_f32 v71, v22, v23\n\t" \
  "ds_write_b128 v32, v[68:71] offset:8192\n\t" \
  "s_xor_b32 s91, s3, 2\n\t" \
  "s_mul_i32 s91, s91, 196608\n\t" \
  "s_add_u32 s91, s91, 1024\n\t" \
  "buffer_load_dwordx4 a[128:131], v48, s[12:15], s91 offen offset:0\n\t" \
  "v_lshlrev_b32 v16, 16, v100\n\t" \
  "v_and_b32 v17, 0xffff0000, v100\n\t" \
  "v_lshlrev_b32 v18, 16, v101\n\t" \
  "v_and_b32 v19, 0xffff0000, v101\n\t" \
  "v_lshlrev_b32 v20, 16, v102\n\t" \
  "v_and_b32 v21, 0xffff0000, v102\n\t" \
  "v_lshlrev_b32 v22, 16, v103\n\t" \
  "v_and_b32 v23, 0xffff0000, v103\n\t" \
  "v_pk_fma_f32 v[16:17], v[16:17], v[60:61], v[62:63] op_sel:[0,1,1] op_sel_hi:[1,1,1]\n\t" \
  "v_pk_fma_f32 v[18:19], v[18:19], v[60:61], v[62:63] op_sel:[0,1,1] op_sel_hi:[1,1,1]\n\t" \
  "v_pk_fma_f32 v[20:21], v[20:21], v[60:61], v[62:63] op_sel:[0,1,1] op_sel_hi:[1,1,1]\n\t" \
  "v_pk_fma_f32 v[22:23], v[22:23], v[60:61], v[62:63] op_sel:[0,1,1] op_sel_hi:[1,1,1]\n\t" \
  "v_cvt_pk_bf16_f32 v100, v16, v17\n\t" \
  "v_cvt_pk_bf16_f32 v101, v18, v19\n\t" \
  "v_cvt_pk_bf16_f32 v102, v20, v21\n\t" \
  "v_cvt_pk_bf16_f32 v103, v22, v23\n\t" \
  "ds_write_b128 v33, v[100:103] offset:8192\n\t" \
  "buffer_load_dwordx4 a[132:135], v49, s[12:15], s91 offen offset:0\n\t" \
  "v_lshlrev_b32 v16, 16, v72\n\t" \
  "v_and_b32 v17, 0xffff0000, v72\n\t" \
  "v_lshlrev_b32 v18, 16, v73\n\t" \
  "v_and_b32 v19, 0xffff0000, v73\n\t" \
  "v_lshlrev_b32 v20, 16, v74\n\t" \
  "v_and_b32 v21, 0xffff0000, v74\n\t" \
  "v_lshlrev_b32 v22, 16, v75\n\t" \
  "v_and_b32 v23, 0xffff0000, v75\n\t" \
  "v_pk_fma_f32 v[16:17], v[16:17], v[60:61], v[62:63] op_sel:[0,0,0] op_sel_hi:[1,0,0]\n\t" \
  "v_pk_fma_f32 v[18:19], v[18:19], v[60:61], v[62:63] op_sel:[0,0,0] op_sel_hi:[1,0,0]\n\t" \
  "v_pk_fma_f32 v[20:21], v[20:21], v[60:61], v[62:63] op_sel:[0,0,0] op_sel_hi:[1,0,0]\n\t" \
  "v_pk_fma_f32 v[22:23], v[22:23], v[60:61], v[62:63] op_sel:[0,0,0] op_sel_hi:[1,0,0]\n\t" \
  "v_cvt_pk_bf16_f32 v72, v16, v17\n\t" \
  "v_cvt_pk_bf16_f32 v73, v18, v19\n\t" \
  "v_cvt_pk_bf16_f32 v74, v20, v21\n\t" \
  "v_cvt_pk_bf16_f32 v75, v22, v23\n\t" \
  "ds_write_b128 v32, v[72:75] offset:16384\n\t" \
  "s_add_u32 s91, s91, 1024\n\t" \
  "buffer_load_dwordx4 a[136:139], v48, s[12:15], s91 offen offset:0\n\t" \
  "v_lshlrev_b32 v16, 16, v104\n\t" \
  "v_and_b32 v17, 0xffff0000, v104\n\t" \
  "v_lshlrev_b32 v18, 16, v105\n\t" \
  "v_and_b32 v19, 0xffff0000, v105\n\t" \
  "v_lshlrev_b32 v20, 16, v106\n\t" \
  "v_and_b32 v21, 0xffff0000, v106\n\t" \
  "v_lshlrev_b32 v22, 16, v107\n\t" \
  "v_and_b32 v23, 0xffff0000, v107\n\t" \
  "v_pk_fma_f32 v[16:17], v[16:17], v[60:61], v[62:63] op_sel:[0,1,1] op_sel_hi:[1,1,1]\n\t" \
  "v_pk_fma_f32 v[18:19], v[18:19], v[60:61], v[62:63] op_sel:[0,1,1] op_sel_hi:[1,1,1]\n\t" \
  "v_pk_fma_f32 v[20:21], v[20:21], v[60:61], v[62:63] op_sel:[0,1,1] op_sel_hi:[1,1,1]\n\t" \
  "v_pk_fma_f32 v[22:23], v[22:23], v[60:61], v[62:63] op_sel:[0,1,1] op_sel_hi:[1,1,1]\n\t" \
  "v_cvt_pk_bf16_f32 v104, v16, v17\n\t" \
  "v_cvt_pk_bf16_f32 v105, v18, v19\n\t" \
  "v_cvt_pk_bf16_f32 v106, v20, v21\n\t" \
  "v_cvt_pk_bf16_f32 v107, v22, v23\n\t" \
  "ds_write_b128 v33, v[104:107] offset:16384\n\t" \
  "buffer_load_dwordx4 a[140:143], v49, s[12:15], s91 offen offset:0\n\t" \
  "v_lshlrev_b32 v16, 16, v76\n\t" \
  "v_and_b32 v17, 0xffff0000, v76\n\t" \
  "v_lshlrev_b32 v18, 16, v77\n\t" \
  "v_and_b32 v19, 0xffff0000, v77\n\t" \
  "v_lshlrev_b32 v20, 16, v78\n\t" \
  "v_and_b32 v21, 0xffff0000, v78\n\t" \
  "v_lshlrev_b32 v22, 16, v79\n\t" \
  "v_and_b32 v23, 0xffff0000, v79\n\t" \
  "v_pk_fma_f32 v[16:17], v[16:17], v[60:61], v[62:63] op_sel:[0,0,0] op_sel_hi:[1,0,0]\n\t" \
  "v_pk_fma_f32 v[18:19], v[18:19], v[60:61], v[62:63] op_sel:[0,0,0] op_sel_hi:[1,0,0]\n\t" \
  "v_pk_fma_f32 v[20:21], v[20:21], v[60:61], v[62:63] op_sel:[0,0,0] op_sel_hi:[1,0,0]\n\t" \
  "v_pk_fma_f32 v[22:23], v[22:23], v[60:61], v[62:63] op_sel:[0,0,0] op_sel_hi:[1,0,0]\n\t" \
  "v_cvt_pk_bf16_f32 v76, v16, v17\n\t" \
  "v_cvt_pk_bf16_f32 v77, v18, v19\n\t" \
  "v_cvt_pk_bf16_f32 v78, v20, v21\n\t" \
  "v_cvt_pk_bf16_f32 v79, v22, v23\n\t" \
  "ds_write_b128 v32, v[76:79] offset:24576\n\t" \
  "v_lshlrev_b32 v16, 16, v108\n\t" \
  "v_and_b32 v17, 0xffff0000, v108\n\t" \
  "v_lshlrev_b32 v18, 16, v109\n\t" \
  "v_and_b32 v19, 0xffff0000, v109\n\t" \
  "v_lshlrev_b32 v20, 16, v110\n\t" \
  "v_and_b32 v21, 0xffff0000, v110\n\t" \
  "v_lshlrev_b32 v22, 16, v111\n\t" \
  "v_and_b32 v23, 0xffff0000, v111\n\t" \
  "v_pk_fma_f32 v[16:17], v[16:17], v[60:61], v[62:63] op_sel:[0,1,1] op_sel_hi:[1,1,1]\n\t" \
  "v_pk_fma_f32 v[18:19], v[18:19], v[60:61], v[62:63] op_sel:[0,1,1] op_sel_hi:[1,1,1]\n\t" \
  "v_pk_fma_f32 v[20:21], v[20:21], v[60:61], v[62:63] op_sel:[0,1,1] op_sel_hi:[1,1,1]\n\t" \
  "v_pk_fma_f32 v[22:23], v[22:23], v[60:61], v[62:63] op_sel:[0,1,1] op_sel_hi:[1,1,1]\n\t" \
  "v_cvt_pk_bf16_f32 v108, v16, v17\n\t" \
  "v_cvt_pk_bf16_f32 v109, v18, v19\n\t" \
  "v_cvt_pk_bf16_f32 v110, v20, v21\n\t" \
  "v_cvt_pk_bf16_f32 v111, v22, v23\n\t" \
  "ds_write_b128 v33, v[108:111] offset:24576\n\t" \
  "v_lshlrev_b32 v16, 16, v80\n\t" \
  "v_and_b32 v17, 0xffff0000, v80\n\t" \
  "v_lshlrev_b32 v18, 16, v81\n\t" \
  "v_and_b32 v19, 0xffff0000, v81\n\t" \
  "v_lshlrev_b32 v20, 16, v82\n\t" \
  "v_and_b32 v21, 0xffff0000, v82\n\t" \
  "v_lshlrev_b32 v22, 16, v83\n\t" \
  "v_and_b32 v23, 0xffff0000, v83\n\t" \
  "v_pk_fma_f32 v[16:17], v[16:17], v[60:61], v[62:63] op_sel:[0,0,0] op_sel_hi:[1,0,0]\n\t" \
  "v_pk_fma_f32 v[18:19], v[18:19], v[60:61], v[62:63] op_sel:[0,0,0] op_sel_hi:[1,0,0]\n\t" \
  "v_pk_fma_f32 v[20:21], v[20:21], v[60:61], v[62:63] op_sel:[0,0,0] op_sel_hi:[1,0,0]\n\t" \
  "v_pk_fma_f32 v[22:23], v[22:23], v[60:61], v[62:63] op_sel:[0,0,0] op_sel_hi:[1,0,0]\n\t" \
  "v_cvt_pk_bf16_f32 v80, v16, v17\n\t" \
  "v_cvt_pk_bf16_f32 v81, v18, v19\n\t" \
  "v_cvt_pk_bf16_f32 v82, v20, v21\n\t" \
  "v_cvt_pk_bf16_f32 v83, v22, v23\n\t" \
  "ds_write_b128 v32, v[80:83] offset:32768\n\t" \
  "v_lshlrev_b32 v16, 16, v112\n\t" \
  "v_and_b32 v17, 0xffff0000, v112\n\t" \
  "v_lshlrev_b32 v18, 16, v113\n\t" \
  "v_and_b32 v19, 0xffff0000, v113\n\t" \
  "v_lshlrev_b32 v20, 16, v114\n\t" \
  "v_and_b32 v21, 0xffff0000, v114\n\t" \
  "v_lshlrev_b32 v22, 16, v115\n\t" \
  "v_and_b32 v23, 0xffff0000, v115\n\t" \
  "v_pk_fma_f32 v[16:17], v[16:17], v[60:61], v[62:63] op_sel:[0,1,1] op_sel_hi:[1,1,1]\n\t" \
  "v_pk_fma_f32 v[18:19], v[18:19], v[60:61], v[62:63] op_sel:[0,1,1] op_sel_hi:[1,1,1]\n\t" \
  "v_pk_fma_f32 v[20:21], v[20:21], v[60:61], v[62:63] op_sel:[0,1,1] op_sel_hi:[1,1,1]\n\t" \
  "v_pk_fma_f32 v[22:23], v[22:23], v[60:61], v[62:63] op_sel:[0,1,1] op_sel_hi:[1,1,1]\n\t" \
  "v_cvt_pk_bf16_f32 v112, v16, v17\n\t" \
  "v_cvt_pk_bf16_f32 v113, v18, v19\n\t" \
  "v_cvt_pk_bf16_f32 v114, v20, v21\n\t" \
  "v_cvt_pk_bf16_f32 v115, v22, v23\n\t" \
  "ds_write_b128 v33, v[112:115] offset:32768\n\t" \
  "v_lshlrev_b32 v16, 16, v84\n\t" \
  "v_and_b32 v17, 0xffff0000, v84\n\t" \
  "v_lshlrev_b32 v18, 16, v85\n\t" \
  "v_and_b32 v19, 0xffff0000, v85\n\t" \
  "v_lshlrev_b32 v20, 16, v86\n\t" \
  "v_and_b32 v21, 0xffff0000, v86\n\t" \
  "v_lshlrev_b32 v22, 16, v87\n\t" \
  "v_and_b32 v23, 0xffff0000, v87\n\t" \
  "v_pk_fma_f32 v[16:17], v[16:17], v[60:61], v[62:63] op_sel:[0,0,0] op_sel_hi:[1,0,0]\n\t" \
  "v_pk_fma_f32 v[18:19], v[18:19], v[60:61], v[62:63] op_sel:[0,0,0] op_sel_hi:[1,0,0]\n\t" \
  "v_pk_fma_f32 v[20:21], v[20:21], v[60:61], v[62:63] op_sel:[0,0,0] op_sel_hi:[1,0,0]\n\t" \
  "v_pk_fma_f32 v[22:23], v[22:23], v[60:61], v[62:63] op_sel:[0,0,0] op_sel_hi:[1,0,0]\n\t" \
  "v_cvt_pk_bf16_f32 v84, v16, v17\n\t" \
  "v_cvt_pk_bf16_f32 v85, v18, v19\n\t" \
  "v_cvt_pk_bf16_f32 v86, v20, v21\n\t" \
  "v_cvt_pk_bf16_f32 v87, v22, v23\n\t" \
  "ds_write_b128 v32, v[84:87] offset:40960\n\t" \
  "v_lshlrev_b32 v16, 16, v116\n\t" \
  "v_and_b32 v17, 0xffff0000, v116\n\t" \
  "v_lshlrev_b32 v18, 16, v117\n\t" \
  "v_and_b32 v19, 0xffff0000, v117\n\t" \
  "v_lshlrev_b32 v20, 16, v118\n\t" \
  "v_and_b32 v21, 0xffff0000, v118\n\t" \
  "v_lshlrev_b32 v22, 16, v119\n\t" \
  "v_and_b32 v23, 0xffff0000, v119\n\t" \
  "v_pk_fma_f32 v[16:17], v[16:17], v[60:61], v[62:63] op_sel:[0,1,1] op_sel_hi:[1,1,1]\n\t" \
  "v_pk_fma_f32 v[18:19], v[18:19], v[60:61], v[62:63] op_sel:[0,1,1] op_sel_hi:[1,1,1]\n\t" \
  "v_pk_fma_f32 v[20:21], v[20:21], v[60:61], v[62:63] op_sel:[0,1,1] op_sel_hi:[1,1,1]\n\t" \
  "v_pk_fma_f32 v[22:23], v[22:23], v[60:61], v[62:63] op_sel:[0,1,1] op_sel_hi:[1,1,1]\n\t" \
  "v_cvt_pk_bf16_f32 v116, v16, v17\n\t" \
  "v_cvt_pk_bf16_f32 v117, v18, v19\n\t" \
  "v_cvt_pk_bf16_f32 v118, v20, v21\n\t" \
  "v_cvt_pk_bf16_f32 v119, v22, v23\n\t" \
  "ds_write_b128 v33, v[116:119] offset:40960\n\t" \
  "v_lshlrev_b32 v16, 16, v88\n\t" \
  "v_and_b32 v17, 0xffff0000, v88\n\t" \
  "v_lshlrev_b32 v18, 16, v89\n\t" \
  "v_and_b32 v19, 0xffff0000, v89\n\t" \
  "v_lshlrev_b32 v20, 16, v90\n\t" \
  "v_and_b32 v21, 0xffff0000, v90\n\t" \
  "v_lshlrev_b32 v22, 16, v91\n\t" \
  "v_and_b32 v23, 0xffff0000, v91\n\t" \
  "v_pk_fma_f32 v[16:17], v[16:17], v[60:61], v[62:63] op_sel:[0,0,0] op_sel_hi:[1,0,0]\n\t" \
  "v_pk_fma_f32 v[18:19], v[18:19], v[60:61], v[62:63] op_sel:[0,0,0] op_sel_hi:[1,0,0]\n\t" \
  "v_pk_fma_f32 v[20:21], v[20:21], v[60:61], v[62:63] op_sel:[0,0,0] op_sel_hi:[1,0,0]\n\t" \
  "v_pk_fma_f32 v[22:23], v[22:23], v[60:61], v[62:63] op_sel:[0,0,0] op_sel_hi:[1,0,0]\n\t" \
  "v_cvt_pk_bf16_f32 v88, v16, v17\n\t" \
  "v_cvt_pk_bf16_f32 v89, v18, v19\n\t" \
  "v_cvt_pk_bf16_f32 v90, v20, v21\n\t" \
  "v_cvt_pk_bf16_f32 v91, v22, v23\n\t" \
  "ds_write_b128 v32, v[88:91] offset:49152\n\t" \
  "v_lshlrev_b32 v16, 16, v120\n\t" \
  "v_and_b32 v17, 0xffff0000, v120\n\t" \
  "v_lshlrev_b32 v18, 16, v121\n\t" \
  "v_and_b32 v19, 0xffff0000, v121\n\t" \
  "v_lshlrev_b32 v20, 16, v122\n\t" \
  "v_and_b32 v21, 0xffff0000, v122\n\t" \
  "v_lshlrev_b32 v22, 16, v123\n\t" \
  "v_and_b32 v23, 0xffff0000, v123\n\t" \
  "v_pk_fma_f32 v[16:17], v[16:17], v[60:61], v[62:63] op_sel:[0,1,1] op_sel_hi:[1,1,1]\n\t" \
  "v_pk_fma_f32 v[18:19], v[18:19], v[60:61], v[62:63] op_sel:[0,1,1] op_sel_hi:[1,1,1]\n\t" \
  "v_pk_fma_f32 v[20:21], v[20:21], v[60:61], v[62:63] op_sel:[0,1,1] op_sel_hi:[1,1,1]\n\t" \
  "v_pk_fma_f32 v[22:23], v[22:23], v[60:61], v[62:63] op_sel:[0,1,1] op_sel_hi:[1,1,1]\n\t" \
  "v_cvt_pk_bf16_f32 v120, v16, v17\n\t" \
  "v_cvt_pk_bf16_f32 v121, v18, v19\n\t" \
  "v_cvt_pk_bf16_f32 v122, v20, v21\n\t" \
  "v_cvt_pk_bf16_f32 v123, v22, v23\n\t" \
  "ds_write_b128 v33, v[120:123] offset:49152\n\t" \
  "v_lshlrev_b32 v16, 16, v92\n\t" \
  "v_and_b32 v17, 0xffff0000, v92\n\t" \
  "v_lshlrev_b32 v18, 16, v93\n\t" \
  "v_and_b32 v19, 0xffff0000, v93\n\t" \
  "v_lshlrev_b32 v20, 16, v94\n\t" \
  "v_and_b32 v21, 0xffff0000, v94\n\t" \
  "v_lshlrev_b32 v22, 16, v95\n\t" \
  "v_and_b32 v23, 0xffff0000, v95\n\t" \
  "v_pk_fma_f32 v[16:17], v[16:17], v[60:61], v[62:63] op_sel:[0,0,0] op_sel_hi:[1,0,0]\n\t" \
  "v_pk_fma_f32 v[18:19], v[18:19], v[60:61], v[62:63] op_sel:[0,0,0] op_sel_hi:[1,0,0]\n\t" \
  "v_pk_fma_f32 v[20:21], v[20:21], v[60:61], v[62:63] op_sel:[0,0,0] op_sel_hi:[1,0,0]\n\t" \
  "v_pk_fma_f32 v[22:23], v[22:23], v[60:61], v[62:63] op_sel:[0,0,0] op_sel_hi:[1,0,0]\n\t" \
  "v_cvt_pk_bf16_f32 v92, v16, v17\n\t" \
  "v_cvt_pk_bf16_f32 v93, v18, v19\n\t" \
  "v_cvt_pk_bf16_f32 v94, v20, v21\n\t" \
  "v_cvt_pk_bf16_f32 v95, v22, v23\n\t" \
  "ds_write_b128 v32, v[92:95] offset:57344\n\t" \
  "v_lshlrev_b32 v16, 16, v124\n\t" \
  "v_and_b32 v17, 0xffff0000, v124\n\t" \
  "v_lshlrev_b32 v18, 16, v125\n\t" \
  "v_and_b32 v19, 0xffff0000, v125\n\t" \
  "v_lshlrev_b32 v20, 16, v126\n\t" \
  "v_and_b32 v21, 0xffff0000, v126\n\t" \
  "v_lshlrev_b32 v22, 16, v127\n\t" \
  "v_and_b32 v23, 0xffff0000, v127\n\t" \
  "v_pk_fma_f32 v[16:17], v[16:17], v[60:61], v[62:63] op_sel:[0,1,1] op_sel_hi:[1,1,1]\n\t" \
  "v_pk_fma_f32 v[18:19], v[18:19], v[60:61], v[62:63] op_sel:[0,1,1] op_sel_hi:[1,1,1]\n\t" \
  "v_pk_fma_f32 v[20:21], v[20:21], v[60:61], v[62:63] op_sel:[0,1,1] op_sel_hi:[1,1,1]\n\t" \
  "v_pk_fma_f32 v[22:23], v[22:23], v[60:61], v[62:63] op_sel:[0,1,1] op_sel_hi:[1,1,1]\n\t" \
  "v_cvt_pk_bf16_f32 v124, v16, v17\n\t" \
  "v_cvt_pk_bf16_f32 v125, v18, v19\n\t" \
  "v_cvt_pk_bf16_f32 v126, v20, v21\n\t" \
  "v_cvt_pk_bf16_f32 v127, v22, v23\n\t" \
  "ds_write_b128 v33, v[124:127] offset:57344\n\t" \
  "v_add_u32 v40, 49152, v38\n\t" \
  "s_mov_b32 s16, s56\n\t" \
  "s_mov_b32 s17, s57\n\t" \
  "s_mov_b32 s18, s58\n\t" \
  "s_mov_b32 s19, s84\n\t" \
  "s_mov_b32 s53, s85\n\t" \
  "s_waitcnt vmcnt(6)\n\t" \
  "s_cmp_lt_i32 s18, 0\n\t" \
  "s_cbranch_scc1 Lrn_%=\n\t" \
  "s_cmpk_eq_u32 s17, 0x4000\n\t" \
  "s_cbranch_scc1 Lrg_%=\n\t" \
  "ds_write_b128 v28, v[224:227] offset:0\n\t" \
  "ds_write_b128 v28, v[228:231] offset:1024\n\t" \
  "ds_write_b128 v28, v[232:235] offset:2048\n\t" \
  "ds_write_b128 v28, v[236:239] offset:3072\n\t" \
  "ds_write_b128 v28, v[240:243] offset:4096\n\t" \
  "ds_write_b128 v28, v[244:247] offset:5120\n\t" \
  "ds_write_b128 v28, v[248:251] offset:6144\n\t" \
  "ds_write_b128 v28, v[252:255] offset:7168\n\t" \
  "ds_read_b64_tr_b16 v[224:225], v29 offset:0\n\t" \
  "ds_read_b64_tr_b16 v[226:227], v29 offset:512\n\t" \
  "ds_read_b64_tr_b16 v[228:229], v29 offset:8\n\t" \
  "ds_read_b64_tr_b16 v[230:231], v29 offset:520\n\t" \
  "ds_read_b64_tr_b16 v[232:233], v29 offset:64\n\t" \
  "ds_read_b64_tr_b16 v[234:235], v29 offset:576\n\t" \
  "ds_read_b64_tr_b16 v[236:237], v29 offset:72\n\t" \
  "ds_read_b64_tr_b16 v[238:239], v29 offset:584\n\t" \
  "ds_read_b64_tr_b16 v[240:241], v29 offset:4096\n\t" \
  "ds_read_b64_tr_b16 v[242:243], v29 offset:4608\n\t" \
  "ds_read_b64_tr_b16 v[244:245], v29 offset:4104\n\t" \
  "ds_read_b64_tr_b16 v[246:247], v29 offset:4616\n\t" \
  "ds_read_b64_tr_b16 v[248:249], v29 offset:4160\n\t" \
  "ds_read_b64_tr_b16 v[250:251], v29 offset:4672\n\t" \
  "ds_read_b64_tr_b16 v[252:253], v29 offset:4168\n\t" \
  "ds_read_b64_tr_b16 v[254:255], v29 offset:4680\n\t" \
  "s_branch Lre_%=\n\t" \
  "Lrg_%=:\n\t" \
  "v_swap_b32 v225, v240\n\t" \
  "v_swap_b32 v226, v240\n\t" \
  "v_swap_b32 v227, v241\n\t" \
  "v_swap_b32 v241, v242\n\t" \
  "ds_write_b128 v28, v[224:227] offset:0\n\t" \
  "ds_write_b128 v28, v[240:243] offset:16\n\t" \
  "v_swap_b32 v229, v244\n\t" \
  "v_swap_b32 v230, v244\n\t" \
  "v_swap_b32 v231, v245\n\t" \
  "v_swap_b32 v245, v246\n\t" \
  "ds_write_b128 v28, v[228:231] offset:2432\n\t" \
  "ds_write_b128 v28, v[244:247] offset:2448\n\t" \
  "v_swap_b32 v233, v248\n\t" \
  "v_swap_b32 v234, v248\n\t" \
  "v_swap_b32 v235, v249\n\t" \
  "v_swap_b32 v249, v250\n\t" \
  "ds_write_b128 v28, v[232:235] offset:4864\n\t" \
  "ds_write_b128 v28, v[248:251] offset:4880\n\t" \
  "v_swap_b32 v237, v252\n\t" \
  "v_swap_b32 v238, v252\n\t" \
  "v_swap_b32 v239, v253\n\t" \
  "v_swap_b32 v253, v254\n\t" \
  "ds_write_b128 v28, v[236:239] offset:7296\n\t" \
  "ds_write_b128 v28, v[252:255] offset:7312\n\t" \
  "ds_read_b64_tr_b16 v[224:225], v29 offset:0\n\t" \
  "ds_read_b64_tr_b16 v[226:227], v29 offset:1152\n\t" \
  "ds_read_b64_tr_b16 v[228:229], v29 offset:32\n\t" \
  "ds_read_b64_tr_b16 v[230:231], v29 offset:1184\n\t" \
  "ds_read_b64_tr_b16 v[232:233], v29 offset:64\n\t" \
  "ds_read_b64_tr_b16 v[234:235], v29 offset:1216\n\t" \
  "ds_read_b64_tr_b16 v[236:237], v29 offset:96\n\t" \
  "ds_read_b64_tr_b16 v[238:239], v29 offset:1248\n\t" \
  "ds_read_b64_tr_b16 v[240:241], v29 offset:128\n\t" \
  "ds_read_b64_tr_b16 v[242:243], v29 offset:1280\n\t" \
  "ds_read_b64_tr_b16 v[244:245], v29 offset:160\n\t" \
  "ds_read_b64_tr_b16 v[246:247], v29 offset:1312\n\t" \
  "ds_read_b64_tr_b16 v[248:249], v29 offset:192\n\t" \
  "ds_read_b64_tr_b16 v[250:251], v29 offset:1344\n\t" \
  "ds_read_b64_tr_b16 v[252:253], v29 offset:224\n\t" \
  "ds_read_b64_tr_b16 v[254:255], v29 offset:1376\n\t" \
  "s_branch Lre_%=\n\t" \
  "Lrn_%=:\n\t" \
  "Lre_%=:\n\t" \
  "ds_read_b32 v14, v24 offset:0\n\t" \
  "s_waitcnt vmcnt(0)\n\t" \
  "ds_write_b128 v15, a[128:131] offset:0\n\t" \
  "ds_write_b128 v15, a[132:135] offset:4096\n\t" \
  "ds_write_b128 v15, a[136:139] offset:8192\n\t" \
  "ds_write_b128 v15, a[140:143] offset:12288\n\t" \
  "s_waitcnt lgkmcnt(0)\n\t" \
  "s_barrier\n\t" \
  "ds_read_b64_tr_b16 v[176:177], v38 offset:0\n\t" \
  "ds_read_b64_tr_b16 v[178:179], v38 offset:512\n\t" \
  "ds_read_b128 a[0:3], v34 offset:0\n\t" \
  "ds_read_b64_tr_b16 v[180:181], v38 offset:8192\n\t" \
  "ds_read_b64_tr_b16 v[182:183], v38 offset:8704\n\t" \
  "ds_read_b64_tr_b16 v[184:185], v38 offset:2048\n\t" \
  "ds_read_b64_tr_b16 v[186:187], v38 offset:2560\n\t" \
  "ds_read_b128 a[4:7], v35 offset:0\n\t" \
  "ds_read_b64_tr_b16 v[188:189], v38 offset:10240\n\t" \
  "ds_read_b64_tr_b16 v[190:191], v38 offset:10752\n\t" \
  "ds_read_b64_tr_b16 v[192:193], v38 offset:4096\n\t" \
  "ds_read_b64_tr_b16 v[194:195], v38 offset:4608\n\t" \
  "ds_read_b128 a[8:11], v36 offset:0\n\t" \
  "ds_read_b64_tr_b16 v[196:197], v38 offset:12288\n\t" \
  "ds_read_b64_tr_b16 v[198:199], v38 offset:12800\n\t" \
  "s_waitcnt lgkmcnt(10)\n\t" \
  "v_mfma_f32_32x32x16_bf16 v[128:143], v[176:179], a[0:3], 0\n\t" \
  "ds_read_b64_tr_b16 v[200:201], v38 offset:6144\n\t" \
  "ds_read_b64_tr_b16 v[202:203], v38 offset:6656\n\t" \
  "ds_read_b128 a[12:15], v37 offset:0\n\t" \
  "s_xor_b32 s91, s3, 3\n\t" \
  "s_mul_i32 s91, s91, 196608\n\t" \
  "s_add_u32 s91, s91, 1024\n\t" \
  "v_mfma_f32_32x32x16_bf16 v[160:175], a[0:3], v[180:183], 0\n\t" \
  "ds_read_b64_tr_b16 v[204:205], v38 offset:14336\n\t" \
  "ds_read_b64_tr_b16 v[206:207], v38 offset:14848\n\t" \
  "s_add_u32 m0, s19, 114688\n\t" \
  "s_waitcnt lgkmcnt(10)\n\t" \
  "s_barrier\n\t" \
  "v_mfma_f32_32x32x16_bf16 v[128:143], v[184:187], a[4:7], v[128:143]\n\t" \
  "ds_read_b64_tr_b16 v[176:177], v38 offset:16384\n\t" \
  "ds_read_b64_tr_b16 v[178:179], v38 offset:16896\n\t" \
  "ds_read_b128 a[16:19], v34 offset:8192\n\t" \
  "buffer_load_dwordx4 v48, s[12:15], s91 offen offset:0 lds\n\t" \
  "s_add_u32 m0, s19, 118784\n\t" \
  "s_mov_b32 s56, 0x0\n\t" \
  "s_mov_b32 s57, 0xf\n\t" \
  "v_mfma_f32_32x32x16_bf16 v[160:175], a[4:7], v[188:191], v[160:175]\n\t" \
  "ds_read_b64_tr_b16 v[180:181], v38 offset:24576\n\t" \
  "ds_read_b64_tr_b16 v[182:183], v38 offset:25088\n\t" \
  "buffer_load_dwordx4 v49, s[12:15], s91 offen offset:0 lds\n\t" \
  "s_add_u32 s91, s91, 1024\n\t" \
  "s_add_u32 m0, s19, 122880\n\t" \
  "s_mov_b32 s58, 0x1\n\t" \
  "s_waitcnt lgkmcnt(10)\n\t" \
  "v_mfma_f32_32x32x16_bf16 v[128:143], v[192:195], a[8:11], v[128:143]\n\t" \
  "ds_read_b64_tr_b16 v[184:185], v38 offset:18432\n\t" \
  "ds_read_b64_tr_b16 v[186:187], v38 offset:18944\n\t" \
  "ds_read_b128 a[20:23], v35 offset:8192\n\t" \
  "buffer_load_dwordx4 v48, s[12:15], s91 offen offset:0 lds\n\t" \
  "s_add_u32 m0, s19, 126976\n\t" \
  "s_mov_b32 s59, 0x1f\n\t" \
  "s_mov_b32 s60, 0x3\n\t" \
  "v_mfma_f32_32x32x16_bf16 v[160:175], a[8:11], v[196:199], v[160:175]\n\t" \
  "ds_read_b64_tr_b16 v[188:189], v38 offset:26624\n\t" \
  "ds_read_b64_tr_b16 v[190:191], v38 offset:27136\n\t" \
  "buffer_load_dwordx4 v49, s[12:15], s91 offen offset:0 lds\n\t" \
  "s_xor_b32 s91, s3, 4\n\t" \
  "s_mul_i32 s91, s91, 196608\n\t" \
  "s_mov_b32 s61, 0x3f\n\t" \
  "s_waitcnt lgkmcnt(10)\n\t" \
  "v_mfma_f32_32x32x16_bf16 v[128:143], v[200:203], a[12:15], v[128:143]\n\t" \
  "ds_read_b64_tr_b16 v[192:193], v38 offset:20480\n\t" \
  "ds_read_b64_tr_b16 v[194:195], v38 offset:20992\n\t" \
  "ds_read_b128 a[24:27], v36 offset:8192\n\t" \
  "s_add_u32 s91, s91, 1024\n\t" \
  "s_add_u32 m0, s19, 131072\n\t" \
  "s_mov_b32 s62, 0x7\n\t" \
  "s_mov_b32 s63, 0x7f\n\t" \
  "v_mfma_f32_32x32x16_bf16 v[160:175], a[12:15], v[204:207], v[160:175]\n\t" \
  "ds_read_b64_tr_b16 v[196:197], v38 offset:28672\n\t" \
  "ds_read_b64_tr_b16 v[198:199], v38 offset:29184\n\t" \
  "buffer_load_dwordx4 v48, s[12:15], s91 offen offset:0 lds\n\t" \
  "s_add_u32 m0, s19, 135168\n\t" \
  "s_mov_b32 s64, 0xff\n\t" \
  "s_mov_b32 s65, 0xfff\n\t" \
  "s_waitcnt lgkmcnt(10)\n\t" \
  "v_mfma_f32_32x32x16_bf16 v[128:143], v[176:179], a[16:19], v[128:143]\n\t" \
  "ds_read_b64_tr_b16 v[200:201], v38 offset:22528\n\t" \
  "ds_read_b64_tr_b16 v[202:203], v38 offset:23040\n\t" \
  "ds_read_b128 a[28:31], v37 offset:8192\n\t" \
  "buffer_load_dwordx4 v49, s[12:15], s91 offen offset:0 lds\n\t" \
  "s_add_u32 s91, s91, 1024\n\t" \
  "s_add_u32 m0, s19, 139264\n\t" \
  "s_mov_b32 s66, 0x1ff\n\t" \
  "v_mfma_f32_32x32x16_bf16 v[160:175], a[16:19], v[180:183], v[160:175]\n\t" \
  "ds_read_b64_tr_b16 v[204:205], v38 offset:30720\n\t" \
  "ds_read_b64_tr_b16 v[206:207], v38 offset:31232\n\t" \
  "buffer_load_dwordx4 v48, s[12:15], s91 offen offset:0 lds\n\t" \
  "s_add_u32 m0, s19, 143360\n\t" \
  "s_mov_b32 s67, 0x1fff\n\t" \
  "s_mov_b32 s68, 0x3ff\n\t" \
  "s_waitcnt lgkmcnt(10)\n\t" \
  "s_barrier\n\t" \
  "v_mfma_f32_32x32x16_bf16 v[128:143], v[184:187], a[20:23], v[128:143]\n\t" \
  "ds_read_b64_tr_b16 v[176:177], v38 offset:32768\n\t" \
  "ds_read_b64_tr_b16 v[178:179], v38 offset:33280\n\t" \
  "ds_read_b128 a[32:35], v34 offset:16384\n\t" \
  "buffer_load_dwordx4 v49, s[12:15], s91 offen offset:0 lds\n\t" \
  "s_xor_b32 s91, s3, 5\n\t" \
  "s_mul_i32 s91, s91, 196608\n\t" \
  "s_mov_b32 s69, 0x3fff\n\t" \
  "v_mfma_f32_32x32x16_bf16 v[160:175], a[20:23], v[188:191], v[160:175]\n\t" \
  "ds_read_b64_tr_b16 v[180:181], v38 offset:40960\n\t" \
  "ds_read_b64_tr_b16 v[182:183], v38 offset:41472\n\t" \
  "s_add_u32 s91, s91, 1024\n\t" \
  "s_add_u32 m0, s19, 147456\n\t" \
  "s_mov_b32 s70, 0x7ff\n\t" \
  "s_mov_b32 s71, 0x7fff\n\t" \
  "s_waitcnt lgkmcnt(10)\n\t" \
  "v_mfma_f32_32x32x16_bf16 v[128:143], v[192:195], a[24:27], v[128:143]\n\t" \
  "ds_read_b64_tr_b16 v[184:185], v38 offset:34816\n\t" \
  "ds_read_b64_tr_b16 v[186:187], v38 offset:35328\n\t" \
  "ds_read_b128 a[36:39], v35 offset:16384\n\t" \
  "buffer_load_dwordx4 v48, s[12:15], s91 offen offset:0 lds\n\t" \
  "s_add_u32 m0, s19, 151552\n\t" \
  "s_mov_b32 s72, 0xffff\n\t" \
  "s_mov_b32 s73, 0xfffff\n\t" \
  "v_mfma_f32_32x32x16_bf16 v[160:175], a[24:27], v[196:199], v[160:175]\n\t" \
  "ds_read_b64_tr_b16 v[188:189], v38 offset:43008\n\t" \
  "ds_read_b64_tr_b16 v[190:191], v38 offset:43520\n\t" \
  "buffer_load_dwordx4 v49, s[12:15], s91 offen offset:0 lds\n\t" \
  "s_add_u32 s91, s91, 1024\n\t" \
  "s_add_u32 m0, s19, 155648\n\t" \
  "s_mov_b32 s74, 0x1ffff\n\t" \
  "s_waitcnt lgkmcnt(10)\n\t" \
  "v_mfma_f32_32x32x16_bf16 v[128:143], v[200:203], a[28:31], v[128:143]\n\t" \
  "ds_read_b64_tr_b16 v[192:193], v38 offset:36864\n\t" \
  "ds_read_b64_tr_b16 v[194:195], v38 offset:37376\n\t" \
  "ds_read_b128 a[40:43], v36 offset:16384\n\t" \
  "buffer_load_dwordx4 v48, s[12:15], s91 offen offset:0 lds\n\t" \
  "s_add_u32 m0, s19, 159744\n\t" \
  "s_mov_b32 s75, 0x1fffff\n\t" \
  "s_mov_b32 s76, 0x3ffff\n\t" \
  "v_mfma_f32_32x32x16_bf16 v[160:175], a[28:31], v[204:207], v[160:175]\n\t" \
  "ds_read_b64_tr_b16 v[196:197], v38 offset:45056\n\t" \
  "ds_read_b64_tr_b16 v[198:199], v38 offset:45568\n\t" \
  "buffer_load_dwordx4 v49, s[12:15], s91 offen offset:0 lds\n\t" \
  "s_mov_b32 s77, 0x3fffff\n\t" \
  "s_mov_b32 s78, 0x7ffff\n\t" \
  "s_mov_b32 s79, 0x7fffff\n\t" \
  "s_waitcnt lgkmcnt(10)\n\t" \
  "v_mfma_f32_32x32x16_bf16 v[128:143], v[176:179], a[32:35], v[128:143]\n\t" \
  "ds_read_b64_tr_b16 v[200:201], v38 offset:38912\n\t" \
  "ds_read_b64_tr_b16 v[202:203], v38 offset:39424\n\t" \
  "ds_read_b128 a[44:47], v37 offset:16384\n\t" \
  "buffer_store_dwordx4 v[224:227], v24, s[4:7], s16 offen offset:0 sc1\n\t" \
  "s_add_u32 s16, s16, s17\n\t" \
  "s_mov_b32 s80, 0xffffff\n\t" \
  "s_mov_b32 s81, 0xfffffff\n\t" \
  "v_mfma_f32_32x32x16_bf16 v[160:175], a[32:35], v[180:183], v[160:175]\n\t" \
  "ds_read_b64_tr_b16 v[204:205], v38 offset:47104\n\t" \
  "ds_read_b64_tr_b16 v[206:207], v38 offset:47616\n\t" \
  "buffer_store_dwordx4 v[228:231], v24, s[4:7], s16 offen offset:0 sc1\n\t" \
  "s_add_u32 s16, s16, s17\n\t" \
  "s_mov_b32 s82, 0x1ffffff\n\t" \
  "s_mov_b32 s83, 0x1fffffff\n\t" \
  "s_waitcnt vmcnt(10) lgkmcnt(10)\n\t" \
  "s_barrier\n\t" \
  "v_mfma_f32_32x32x16_bf16 v[128:143], v[184:187], a[36:39], v[128:143]\n\t" \
  "ds_read_b64_tr_b16 v[176:177], v40 offset:0\n\t" \
  "ds_read_b64_tr_b16 v[178:179], v40 offset:512\n\t" \
  "ds_read_b128 a[48:51], v34 offset:24576\n\t" \
  "buffer_store_dwordx4 v[232:235], v24, s[4:7], s16 offen offset:0 sc1\n\t" \
  "s_add_u32 s16, s16, s17\n\t" \
  "s_mov_b32 s84, 0x3ffffff\n\t" \
  "s_mov_b32 s85, 0x3fffffff\n\t" \
  "v_mfma_f32_32x32x16_bf16 v[160:175], a[36:39], v[188:191], v[160:175]\n\t" \
  "ds_read_b64_tr_b16 v[180:181], v40 offset:8192\n\t" \
  "ds_read_b64_tr_b16 v[182:183], v40 offset:8704\n\t" \
  "buffer_store_dwordx4 v[236:239], v24, s[4:7], s16 offen offset:0 sc1\n\t" \
  "s_add_u32 s16, s16, s17\n\t" \
  "s_mov_b32 s86, 0x7ffffff\n\t" \
  "s_mov_b32 s87, 0x7fffffff\n\t" \
  "s_waitcnt lgkmcnt(10)\n\t" \
  "v_mfma_f32_32x32x16_bf16 v[128:143], v[192:195], a[40:43], v[128:143]\n\t" \
  "ds_read_b64_tr_b16 v[184:185], v40 offset:2048\n\t" \
  "ds_read_b64_tr_b16 v[186:187], v40 offset:2560\n\t" \
  "ds_read_b128 a[52:55], v35 offset:24576\n\t" \
  "buffer_store_dwordx4 v[240:243], v24, s[4:7], s16 offen offset:0 sc1\n\t" \
  "s_add_u32 s16, s16, s17\n\t" \
  "s_and_b32 s34, s24, 1\n\t" \
  "s_mul_i32 s32, s30, 768\n\t" \
  "v_mfma_f32_32x32x16_bf16 v[160:175], a[40:43], v[196:199], v[160:175]\n\t" \
  "ds_read_b64_tr_b16 v[188:189], v40 offset:10240\n\t" \
  "ds_read_b64_tr_b16 v[190:191], v40 offset:10752\n\t" \
  "buffer_store_dwordx4 v[244:247], v24, s[4:7], s16 offen offset:0 sc1\n\t" \
  "s_add_u32 s16, s16, s17\n\t" \
  "s_mul_i32 s33, s29, 360448\n\t" \
  "s_add_u32 s32, s32, s33\n\t" \
  "s_waitcnt lgkmcnt(10)\n\t" \
  "v_mfma_f32_32x32x16_bf16 v[128:143], v[200:203], a[44:47], v[128:143]\n\t" \
  "ds_read_b64_tr_b16 v[192:193], v40 offset:4096\n\t" \
  "ds_read_b64_tr_b16 v[194:195], v40 offset:4608\n\t" \
  "ds_read_b128 a[56:59], v36 offset:24576\n\t" \
  "buffer_store_dwordx4 v[248:251], v24, s[4:7], s16 offen offset:0 sc1\n\t" \
  "s_add_u32 s16, s16, s17\n\t" \
  "s_add_u32 s32, s32, 0x940000\n\t" \
  "s_cmp_lt_u32 s30, 58\n\t" \
  "s_cselect_b32 s48, s32, 0x80000000\n\t" \
  "v_mfma_f32_32x32x16_bf16 v[160:175], a[44:47], v[204:207], v[160:175]\n\t" \
  "ds_read_b64_tr_b16 v[196:197], v40 offset:12288\n\t" \
  "ds_read_b64_tr_b16 v[198:199], v40 offset:12800\n\t" \
  "buffer_store_dwordx4 v[252:255], v24, s[4:7], s16 offen offset:0 sc1\n\t" \
  "s_xor_b32 s91, s3, 6\n\t" \
  "s_mul_i32 s91, s91, 196608\n\t" \
  "s_lshl_b32 s32, s25, 16\n\t" \
  "s_waitcnt lgkmcnt(10)\n\t" \
  "v_mfma_f32_32x32x16_bf16 v[128:143], v[176:179], a[48:51], v[128:143]\n\t" \
  "ds_read_b64_tr_b16 v[200:201], v40 offset:6144\n\t" \
  "ds_read_b64_tr_b16 v[202:203], v40 offset:6656\n\t" \
  "ds_read_b128 a[60:63], v37 offset:24576\n\t" \
  "s_add_u32 s91, s91, 1024\n\t" \
  "s_add_u32 m0, s19, 65536\n\t" \
  "s_lshl_b32 s33, s26, 9\n\t" \
  "s_add_u32 s32, s32, s33\n\t" \
  "v_mfma_f32_32x32x16_bf16 v[160:175], a[48:51], v[180:183], v[160:175]\n\t" \
  "ds_read_b64_tr_b16 v[204:205], v40 offset:14336\n\t" \
  "ds_read_b64_tr_b16 v[206:207], v40 offset:14848\n\t" \
  "buffer_load_dwordx4 v48, s[12:15], s91 offen offset:0 lds\n\t" \
  "s_add_u32 m0, s19, 69632\n\t" \
  "s_add_u32 s38, s32, 0x540000\n\t" \
  "s_lshl_b32 s32, s25, 12\n\t" \
  "s_waitcnt vmcnt(13) lgkmcnt(10)\n\t" \
  "s_barrier\n\t" \
  "v_mfma_f32_32x32x16_bf16 v[128:143], v[184:187], a[52:55], v[128:143]\n\t" \
  "ds_read_b64_tr_b16 v[176:177], v40 offset:16384\n\t" \
  "ds_read_b64_tr_b16 v[178:179], v40 offset:16896\n\t" \
  "ds_read_b128 a[64:67], v34 offset:32768\n\t" \
  "buffer_load_dwordx4 v49, s[12:15], s91 offen offset:0 lds\n\t" \
  "s_add_u32 s91, s91, 1024\n\t" \
  "s_add_u32 m0, s19, 73728\n\t" \
  "s_lshl_b32 s33, s26, 7\n\t" \
  "v_mfma_f32_32x32x16_bf16 v[160:175], a[52:55], v[188:191], v[160:175]\n\t" \
  "ds_read_b64_tr_b16 v[180:181], v40 offset:24576\n\t" \
  "ds_read_b64_tr_b16 v[182:183], v40 offset:25088\n\t" \
  "buffer_load_dwordx4 v48, s[12:15], s91 offen offset:0 lds\n\t" \
  "s_add_u32 m0, s19, 77824\n\t" \
  "s_add_u32 s32, s32, s33\n\t" \
  "s_add_u32 s39, s32, 0x1c000\n\t" \
  "s_waitcnt lgkmcnt(10)\n\t" \
  "v_mfma_f32_32x32x16_bf16 v[128:143], v[192:195], a[56:59], v[128:143]\n\t" \
  "ds_read_b64_tr_b16 v[184:185], v40 offset:18432\n\t" \
  "ds_read_b64_tr_b16 v[186:187], v40 offset:18944\n\t" \
  "ds_read_b128 a[68:71], v35 offset:32768\n\t" \
  "buffer_load_dwordx4 v49, s[12:15], s91 offen offset:0 lds\n\t" \
  "s_xor_b32 s91, s3, 7\n\t" \
  "s_mul_i32 s91, s91, 196608\n\t" \
  "s_lshr_b32 s35, s24, 1\n\t" \
  "v_mfma_f32_32x32x16_bf16 v[160:175], a[56:59], v[196:199], v[160:175]\n\t" \
  "ds_read_b64_tr_b16 v[188:189], v40 offset:26624\n\t" \
  "ds_read_b64_tr_b16 v[190:191], v40 offset:27136\n\t" \
  "s_add_u32 s91, s91, 1024\n\t" \
  "s_add_u32 m0, s19, 81920\n\t" \
  "s_lshl2_add_u32 s35, s27, s35\n\t" \
  "s_lshl_b32 s32, s35, 14\n\t" \
  "s_waitcnt lgkmcnt(10)\n\t" \
  "v_mfma_f32_32x32x16_bf16 v[128:143], v[200:203], a[60:63], v[128:143]\n\t" \
  "ds_read_b64_tr_b16 v[192:193], v40 offset:20480\n\t" \
  "ds_read_b64_tr_b16 v[194:195], v40 offset:20992\n\t" \
  "ds_read_b128 a[72:75], v36 offset:32768\n\t" \
  "buffer_load_dwordx4 v48, s[12:15], s91 offen offset:0 lds\n\t" \
  "s_add_u32 m0, s19, 86016\n\t" \
  "s_lshl_b32 s33, s24, 12\n\t" \
  "s_add_u32 s40, s32, s33\n\t" \
  "v_mfma_f32_32x32x16_bf16 v[160:175], a[60:63], v[204:207], v[160:175]\n\t" \
  "ds_read_b64_tr_b16 v[196:197], v40 offset:28672\n\t" \
  "ds_read_b64_tr_b16 v[198:199], v40 offset:29184\n\t" \
  "buffer_load_dwordx4 v49, s[12:15], s91 offen offset:0 lds\n\t" \
  "s_add_u32 s91, s91, 1024\n\t" \
  "s_add_u32 m0, s19, 90112\n\t" \
  "s_add_u32 s40, s40, 0x540000\n\t" \
  "s_waitcnt lgkmcnt(10)\n\t" \
  "v_mfma_f32_32x32x16_bf16 v[128:143], v[176:179], a[64:67], v[128:143]\n\t" \
  "ds_read_b64_tr_b16 v[200:201], v40 offset:22528\n\t" \
  "ds_read_b64_tr_b16 v[202:203], v40 offset:23040\n\t" \
  "ds_read_b128 a[76:79], v37 offset:32768\n\t" \
  "buffer_load_dwordx4 v48, s[12:15], s91 offen offset:0 lds\n\t" \
  "s_add_u32 m0, s19, 94208\n\t" \
  "s_xor_b32 s41, s40, 16384\n\t" \
  "s_mov_b32 s46, s33\n\t" \
  "v_mfma_f32_32x32x16_bf16 v[160:175], a[64:67], v[180:183], v[160:175]\n\t" \
  "ds_read_b64_tr_b16 v[204:205], v40 offset:30720\n\t" \
  "ds_read_b64_tr_b16 v[206:207], v40 offset:31232\n\t" \
  "buffer_load_dwordx4 v49, s[12:15], s91 offen offset:0 lds\n\t" \
  "s_xor_b32 s91, s3, 0\n\t" \
  "s_mul_i32 s91, s91, 196608\n\t" \
  "s_lshl_b32 s45, s29, 17\n\t" \
  "s_waitcnt vmcnt(16) lgkmcnt(10)\n\t" \
  "s_barrier\n\t" \
  "v_mfma_f32_32x32x16_bf16 v[128:143], v[184:187], a[68:71], v[128:143]\n\t" \
  "ds_read_b64_tr_b16 v[176:177], v40 offset:32768\n\t" \
  "ds_read_b64_tr_b16 v[178:179], v40 offset:33280\n\t" \
  "ds_read_b128 a[80:83], v34 offset:40960\n\t" \
  "s_add_u32 m0, s19, 0\n\t" \
  "s_add_u32 s45, s45, s33\n\t" \
  "s_add_u32 s45, s45, 0x740000\n\t" \
  "s_lshl1_add_u32 s34, s28, s34\n\t" \
  "v_mfma_f32_32x32x16_bf16 v[160:175], a[68:71], v[188:191], v[160:175]\n\t" \
  "ds_read_b64_tr_b16 v[180:181], v40 offset:40960\n\t" \
  "ds_read_b64_tr_b16 v[182:183], v40 offset:41472\n\t" \
  "buffer_load_dwordx4 v48, s[12:15], s91 offen offset:0 lds\n\t" \
  "s_add_u32 m0, s19, 4096\n\t" \
  "s_lshl_b32 s33, s34, 10\n\t" \
  "s_add_u32 s32, s32, s33\n\t" \
  "s_waitcnt lgkmcnt(10)\n\t" \
  "v_mfma_f32_32x32x16_bf16 v[128:143], v[192:195], a[72:75], v[128:143]\n\t" \
  "ds_read_b64_tr_b16 v[184:185], v40 offset:34816\n\t" \
  "ds_read_b64_tr_b16 v[186:187], v40 offset:35328\n\t" \
  "ds_read_b128 a[84:87], v35 offset:40960\n\t" \
  "buffer_load_dwordx4 v49, s[12:15], s91 offen offset:0 lds\n\t" \
  "s_xor_b32 s91, s3, 1\n\t" \
  "s_mul_i32 s91, s91, 196608\n\t" \
  "s_add_u32 s42, s32, 0x740000\n\t" \
  "v_mfma_f32_32x32x16_bf16 v[160:175], a[72:75], v[196:199], v[160:175]\n\t" \
  "ds_read_b64_tr_b16 v[188:189], v40 offset:43008\n\t" \
  "ds_read_b64_tr_b16 v[190:191], v40 offset:43520\n\t" \
  "s_add_u32 m0, s19, 8192\n\t" \
  "s_lshr_b32 s32, s32, 3\n\t" \
  "s_add_u32 s43, s32, 0x100000\n\t" \
  "s_lshl3_add_u32 s32, s27, s28\n\t" \
  "s_waitcnt lgkmcnt(10)\n\t" \
  "v_mfma_f32_32x32x16_bf16 v[128:143], v[200:203], a[76:79], v[128:143]\n\t" \
  "ds_read_b64_tr_b16 v[192:193], v40 offset:36864\n\t" \
  "ds_read_b64_tr_b16 v[194:195], v40 offset:37376\n\t" \
  "ds_read_b128 a[88:91], v36 offset:40960\n\t" \
  "buffer_load_dwordx4 v48, s[12:15], s91 offen offset:0 lds\n\t" \
  "s_add_u32 m0, s19, 12288\n\t" \
  "s_lshl2_add_u32 s32, s32, s24\n\t" \
  "s_lshl_b32 s32, s32, 7\n\t" \
  "v_mfma_f32_32x32x16_bf16 v[160:175], a[76:79], v[204:207], v[160:175]\n\t" \
  "ds_read_b64_tr_b16 v[196:197], v40 offset:45056\n\t" \
  "ds_read_b64_tr_b16 v[198:199], v40 offset:45568\n\t" \
  "buffer_load_dwordx4 v49, s[12:15], s91 offen offset:0 lds\n\t" \
  "s_xor_b32 s91, s3, 2\n\t" \
  "s_mul_i32 s91, s91, 196608\n\t" \
  "s_add_u32 s44, s32, 0xa0000\n\t" \
  "s_waitcnt lgkmcnt(10)\n\t" \
  "v_mfma_f32_32x32x16_bf16 v[128:143], v[176:179], a[80:83], v[128:143]\n\t" \
  "ds_read_b64_tr_b16 v[200:201], v40 offset:38912\n\t" \
  "ds_read_b64_tr_b16 v[202:203], v40 offset:39424\n\t" \
  "ds_read_b128 a[92:95], v37 offset:40960\n\t" \
  "s_add_u32 m0, s19, 16384\n\t" \
  "s_lshl_b32 s32, s24, 9\n\t" \
  "s_add_u32 s47, s32, 0x100000\n\t" \
  "s_lshl_b32 s32, s29, 6\n\t" \
  "v_mfma_f32_32x32x16_bf16 v[160:175], a[80:83], v[180:183], v[160:175]\n\t" \
  "ds_read_b64_tr_b16 v[204:205], v40 offset:47104\n\t" \
  "ds_read_b64_tr_b16 v[206:207], v40 offset:47616\n\t" \
  "buffer_load_dwordx4 v48, s[12:15], s91 offen offset:0 lds\n\t" \
  "s_add_u32 m0, s19, 20480\n\t" \
  "s_add_u32 s32, s32, s30\n\t" \
  "s_lshl_b32 s32, s32, 7\n\t" \
  "s_waitcnt vmcnt(9) lgkmcnt(10)\n\t" \
  "s_barrier\n\t" \
  "buffer_store_dwordx2 v[26:27], v25, s[4:7], s18 offen offset:0 sc1\n\t" \
  "v_mfma_f32_32x32x16_bf16 v[128:143], v[184:187], a[84:87], v[128:143]\n\t" \
  "ds_read_b64_tr_b16 v[176:177], v38 offset:0\n\t" \
  "ds_read_b64_tr_b16 v[178:179], v38 offset:512\n\t" \
  "ds_read_b128 a[96:99], v34 offset:49152\n\t" \
  "buffer_load_dwordx4 v49, s[12:15], s91 offen offset:0 lds\n\t" \
  "s_xor_b32 s91, s3, 3\n\t" \
  "s_mul_i32 s91, s91, 196608\n\t" \
  "s_add_u32 s49, s32, 0x5c000\n\t" \
  "v_mfma_f32_32x32x16_bf16 v[160:175], a[84:87], v[188:191], v[160:175]\n\t" \
  "ds_read_b64_tr_b16 v[180:181], v38 offset:8192\n\t" \
  "ds_read_b64_tr_b16 v[182:183], v38 offset:8704\n\t" \
  "s_add_u32 m0, s19, 24576\n\t" \
  "s_lshl2_add_u32 s32, s28, s24\n\t" \
  "s_mul_i32 s32, s32, 45056\n\t" \
  "s_add_u32 s50, s32, 0xf40000\n\t" \
  "s_waitcnt lgkmcnt(10)\n\t" \
  "v_mfma_f32_32x32x16_bf16 v[128:143], v[192:195], a[88:91], v[128:143]\n\t" \
  "ds_read_b64_tr_b16 v[184:185], v38 offset:2048\n\t" \
  "ds_read_b64_tr_b16 v[186:187], v38 offset:2560\n\t" \
  "ds_read_b128 a[100:103], v35 offset:49152\n\t" \
  "buffer_load_dwordx4 v48, s[12:15], s91 offen offset:0 lds\n\t" \
  "s_add_u32 m0, s19, 28672\n\t" \
  "s_mul_i32 s33, s24, 11264\n\t" \
  "s_add_u32 s33, s33, 0x940000\n\t" \
  "v_mfma_f32_32x32x16_bf16 v[160:175], a[88:91], v[196:199], v[160:175]\n\t" \
  "ds_read_b64_tr_b16 v[188:189], v38 offset:10240\n\t" \
  "ds_read_b64_tr_b16 v[190:191], v38 offset:10752\n\t" \
  "buffer_load_dwordx4 v49, s[12:15], s91 offen offset:0 lds\n\t" \
  "s_mul_i32 s32, s35, 45056\n\t" \
  "s_add_u32 s51, s32, s33\n\t" \
  "s_xor_b32 s32, s35, 1\n\t" \
  "s_waitcnt lgkmcnt(10)\n\t" \
  "v_mfma_f32_32x32x16_bf16 v[128:143], v[200:203], a[92:95], v[128:143]\n\t" \
  "ds_read_b64_tr_b16 v[192:193], v38 offset:4096\n\t" \
  "ds_read_b64_tr_b16 v[194:195], v38 offset:4608\n\t" \
  "ds_read_b128 a[104:107], v36 offset:49152\n\t" \
  "s_mul_i32 s32, s32, 45056\n\t" \
  "s_add_u32 s52, s32, s33\n\t" \
  "s_and_b32 s32, s26, -2\n\t" \
  "s_lshl_b32 s32, s32, 16\n\t" \
  "v_mfma_f32_32x32x16_bf16 v[160:175], a[92:95], v[204:207], v[160:175]\n\t" \
  "ds_read_b64_tr_b16 v[196:197], v38 offset:12288\n\t" \
  "ds_read_b64_tr_b16 v[198:199], v38 offset:12800\n\t" \
  "s_lshl_b32 s33, s25, 11\n\t" \
  "s_add_u32 s32, s32, s33\n\t" \
  "s_and_b32 s34, s24, 1\n\t" \
  "s_lshl_b32 s34, s34, 10\n\t" \
  "s_waitcnt lgkmcnt(10)\n\t" \
  "v_mfma_f32_32x32x16_bf16 v[128:143], v[176:179], a[96:99], v[128:143]\n\t" \
  "ds_read_b64_tr_b16 v[200:201], v38 offset:6144\n\t" \
  "ds_read_b64_tr_b16 v[202:203], v38 offset:6656\n\t" \
  "ds_read_b128 a[108:111], v37 offset:49152\n\t" \
  "s_add_u32 s32, s32, s34\n\t" \
  "s_add_u32 s88, s32, 0x140000\n\t" \
  "s_add_u32 s89, s88, 65536\n\t" \
  "s_add_u32 s94, s32, 0x340000\n\t" \
  "v_mfma_f32_32x32x16_bf16 v[160:175], a[96:99], v[180:183], v[160:175]\n\t" \
  "ds_read_b64_tr_b16 v[204:205], v38 offset:14336\n\t" \
  "ds_read_b64_tr_b16 v[206:207], v38 offset:14848\n\t" \
  "v_and_b32 v20, 0xf0, v24\n\t" \
  "v_lshl_or_b32 v19, v9, 8, v20\n\t" \
  "v_lshl_or_b32 v16, v12, 16, v19\n\t" \
  "s_and_b32 s33, s24, 2\n\t" \
  "s_waitcnt vmcnt(9) lgkmcnt(10)\n\t" \
  "s_barrier\n\t" \
  "v_mfma_f32_32x32x16_bf16 v[128:143], v[184:187], a[100:103], v[128:143]\n\t" \
  "ds_read_b64_tr_b16 v[176:177], v38 offset:16384\n\t" \
  "ds_read_b64_tr_b16 v[178:179], v38 offset:16896\n\t" \
  "ds_read_b128 a[112:115], v34 offset:57344\n\t" \
  "s_mul_i32 s33, s33, 20480\n\t" \
  "s_add_u32 s33, s33, s34\n\t" \
  "v_add_u32 v17, s33, v24\n\t" \
  "s_add_u32 s33, s33, 18432\n\t" \
  "v_mfma_f32_32x32x16_bf16 v[160:175], a[100:103], v[188:191], v[160:175]\n\t" \
  "ds_read_b64_tr_b16 v[180:181], v38 offset:24576\n\t" \
  "ds_read_b64_tr_b16 v[182:183], v38 offset:25088\n\t" \
  "s_movk_i32 s32, 20480\n\t" \
  "v_mad_u32_u24 v18, v12, s32, v19\n\t" \
  "v_add_u32 v18, s33, v18\n\t" \
  "s_mul_i32 s32, s30, 3\n\t" \
  "s_waitcnt lgkmcnt(10)\n\t" \
  "v_mfma_f32_32x32x16_bf16 v[128:143], v[192:195], a[104:107], v[128:143]\n\t" \
  "ds_read_b64_tr_b16 v[184:185], v38 offset:18432\n\t" \
  "ds_read_b64_tr_b16 v[186:187], v38 offset:18944\n\t" \
  "ds_read_b128 a[116:119], v35 offset:57344\n\t" \
  "s_add_u32 s33, s32, 2\n\t" \
  "s_min_u32 s32, s32, 173\n\t" \
  "s_min_u32 s33, s33, 173\n\t" \
  "s_lshr_b32 s32, s32, 3\n\t" \
  "v_mfma_f32_32x32x16_bf16 v[160:175], a[104:107], v[196:199], v[160:175]\n\t" \
  "ds_read_b64_tr_b16 v[188:189], v38 offset:26624\n\t" \
  "ds_read_b64_tr_b16 v[190:191], v38 offset:27136\n\t" \
  "s_lshr_b32 s33, s33, 3\n\t" \
  "s_sub_u32 s33, s33, s32\n\t" \
  "v_mul_u32_u24 v22, s33, v12\n\t" \
  "v_add_u32 v22, s32, v22\n\t" \
  "s_waitcnt lgkmcnt(10)\n\t" \
  "v_mfma_f32_32x32x16_bf16 v[128:143], v[200:203], a[108:111], v[128:143]\n\t" \
  "ds_read_b64_tr_b16 v[192:193], v38 offset:20480\n\t" \
  "ds_read_b64_tr_b16 v[194:195], v38 offset:20992\n\t" \
  "ds_read_b128 a[120:123], v36 offset:57344\n\t" \
  "v_and_b32 v23, 15, v1\n\t" \
  "v_lshl_or_b32 v22, v22, 4, v23\n\t" \
  "s_mul_i32 s32, s24, 11\n\t" \
  "s_add_u32 s33, s32, 10\n\t" \
  "v_mfma_f32_32x32x16_bf16 v[160:175], a[108:111], v[204:207], v[160:175]\n\t" \
  "ds_read_b64_tr_b16 v[196:197], v38 offset:28672\n\t" \
  "ds_read_b64_tr_b16 v[198:199], v38 offset:29184\n\t" \
  "s_lshr_b32 s32, s32, 1\n\t" \
  "s_lshr_b32 s33, s33, 1\n\t" \
  "v_and_b32 v23, 7, v1\n\t" \
  "v_add_u32 v23, s32, v23\n\t" \
  "s_waitcnt lgkmcnt(10)\n\t" \
  "v_mfma_f32_32x32x16_bf16 v[128:143], v[176:179], a[112:115], v[128:143]\n\t" \
  "ds_read_b64_tr_b16 v[200:201], v38 offset:22528\n\t" \
  "ds_read_b64_tr_b16 v[202:203], v38 offset:23040\n\t" \
  "ds_read_b128 a[124:127], v37 offset:57344\n\t" \
  "v_min_u32 v23, s33, v23\n\t" \
  "s_mul_i32 s32, s28, 22\n\t" \
  "s_add_u32 s32, s32, 352\n\t" \
  "v_add_u32 v23, s32, v23\n\t" \
  "v_mfma_f32_32x32x16_bf16 v[160:175], a[112:115], v[180:183], v[160:175]\n\t" \
  "ds_read_b64_tr_b16 v[204:205], v38 offset:30720\n\t" \
  "ds_read_b64_tr_b16 v[206:207], v38 offset:31232\n\t" \
  "s_mov_b32 s32, 0\n\t" \
  "s_mov_b32 s33, -1\n\t" \
  "v_cndmask_b32 v22, v22, v23, s[32:33]\n\t" \
  "s_lshl2_add_u32 s34, s28, s24\n\t" \
  "s_waitcnt lgkmcnt(10)\n\t" \
  "v_mfma_f32_32x32x16_bf16 v[128:143], v[184:187], a[116:119], v[128:143]\n\t" \
  "s_lshl1_add_u32 s34, s34, 528\n\t" \
  "v_and_b32 v23, 1, v1\n\t" \
  "v_add_u32 v23, s34, v23\n\t" \
  "s_movk_i32 s33, 0xff00\n\t" \
  "v_cndmask_b32 v22, v22, v23, s[32:33]\n\t" \
  "s_mov_b32 s32, 0x9000\n\t" \
  "v_lshl_add_u32 v62, v22, 7, s32\n\t" \
  "v_mfma_f32_32x32x16_bf16 v[160:175], a[116:119], v[188:191], v[160:175]\n\t" \
  "v_mov_b32 v32, v24\n\t" \
  "s_lshl_b32 s32, s28, 16\n\t" \
  "s_lshl_b32 s33, s24, 14\n\t" \
  "s_add_u32 s32, s32, s33\n\t" \
  "s_add_u32 s36, s32, 0xec0000\n\t" \
  "s_mul_i32 s32, s30, 49152\n\t" \
  "s_waitcnt lgkmcnt(5)\n\t" \
  "v_mfma_f32_32x32x16_bf16 v[128:143], v[192:195], a[120:123], v[128:143]\n\t" \
  "s_add_u32 s32, s32, 0x10a0000\n\t" \
  "s_cmp_lt_u32 s30, 58\n\t" \
  "s_cselect_b32 s37, s32, 0x80000000\n\t" \
  "v_mfma_f32_32x32x16_bf16 v[160:175], a[120:123], v[196:199], v[160:175]\n\t" \
  "s_waitcnt lgkmcnt(0)\n\t" \
  "v_mfma_f32_32x32x16_bf16 v[128:143], v[200:203], a[124:127], v[128:143]\n\t" \
  "v_mfma_f32_32x32x16_bf16 v[160:175], a[124:127], v[204:207], v[160:175]\n\t" \
  "s_nop 10\n\t" \
  "v_cvt_pk_bf16_f32 v64, v128, v129\n\t" \
  "v_cvt_pk_bf16_f32 v65, v130, v131\n\t" \
  "v_cvt_pk_bf16_f32 v66, v132, v133\n\t" \
  "v_cvt_pk_bf16_f32 v67, v134, v135\n\t" \
  "buffer_store_dwordx4 v[64:67], v24, s[4:7], s88 offen offset:0 sc1\n\t" \
  "v_cvt_pk_bf16_f32 v68, v136, v137\n\t" \
  "v_cvt_pk_bf16_f32 v69, v138, v139\n\t" \
  "v_cvt_pk_bf16_f32 v70, v140, v141\n\t" \
  "v_cvt_pk_bf16_f32 v71, v142, v143\n\t" \
  "buffer_store_dwordx4 v[68:71], v24, s[4:7], s89 offen offset:0 sc1\n\t" \
  "v_cvt_pk_bf16_f32 v72, v160, v161\n\t" \
  "v_cvt_pk_bf16_f32 v73, v162, v163\n\t" \
  "v_cvt_pk_bf16_f32 v74, v164, v165\n\t" \
  "v_cvt_pk_bf16_f32 v75, v166, v167\n\t" \
  "buffer_store_dwordx4 v[72:75], v16, s[4:7], s94 offen offset:0 sc1\n\t" \
  "v_cvt_pk_bf16_f32 v76, v168, v169\n\t" \
  "v_cvt_pk_bf16_f32 v77, v170, v171\n\t" \
  "v_cvt_pk_bf16_f32 v78, v172, v173\n\t" \
  "v_cvt_pk_bf16_f32 v79, v174, v175\n\t" \
  "buffer_store_dwordx4 v[76:79], v16, s[4:7], s94 offen offset:512 sc1\n\t" \
  "s_xor_b32 s91, s3, 4\n\t" \
  "s_mul_i32 s91, s91, 196608\n\t" \
  "s_add_u32 m0, s19, 98304\n\t" \
  "s_and_b32 s32, s24, 1\n\t" \
  "buffer_load_dwordx4 v48, s[12:15], s91 offen offset:0 lds\n\t" \
  "s_lshl_b32 s32, s32, 11\n\t" \
  "v_add_u32 v33, s32, v24\n\t" \
  "s_add_u32 m0, s19, 102400\n\t" \
  "v_xor_b32 v34, 0x800, v33\n\t" \
  "buffer_load_dwordx4 v49, s[12:15], s91 offen offset:0 lds\n\t" \
  "v_lshl_or_b32 v16, v12, 9, v20\n\t" \
  "v_lshl_or_b32 v16, v9, 10, v16\n\t" \
  "s_xor_b32 s91, s3, 5\n\t" \
  "s_mul_i32 s91, s91, 196608\n\t" \
  "s_add_u32 m0, s19, 106496\n\t" \
  "v_add_u32 v16, 81920, v16\n\t" \
  "buffer_load_dwordx4 v48, s[12:15], s91 offen offset:0 lds\n\t" \
  "v_mov_b32 v80, s53\n\t" \
  "v_mov_b32 v81, s53\n\t" \
  "s_add_u32 m0, s19, 110592\n\t" \
  "v_mov_b32 v82, s53\n\t" \
  "buffer_load_dwordx4 v49, s[12:15], s91 offen offset:0 lds\n\t" \
  "v_mov_b32 v83, s53\n\t" \
  "v_and_b32 v21, 3, v1\n\t" \
  "s_xor_b32 s91, s3, 6\n\t" \
  "s_mul_i32 s91, s91, 196608\n\t" \
  "s_add_u32 m0, s19, 114688\n\t" \
  "s_add_i32 s32, s25, -4\n\t" \
  "buffer_load_dwordx4 v48, s[12:15], s91 offen offset:0 lds\n\t" \
  "v_add_u32 v21, s32, v21\n\t" \
  "v_max_i32 v21, 0, v21\n\t" \
  "s_add_u32 m0, s19, 118784\n\t" \
  "s_lshl_b32 s32, s97, 7\n\t" \
  "buffer_load_dwordx4 v49, s[12:15], s91 offen offset:0 lds\n\t" \
  "s_add_u32 s32, s32, 4096\n\t" \
  "v_lshl_add_u32 v61, v21, 10, s32\n\t" \
  "s_xor_b32 s91, s3, 7\n\t" \
  "s_mul_i32 s91, s91, 196608\n\t" \
  "s_add_u32 m0, s19, 122880\n\t" \
  "s_lshl_b32 s33, s25, 10\n\t" \
  "buffer_load_dwordx4 v48, s[12:15], s91 offen offset:0 lds\n\t" \
  "s_add_u32 s32, s32, s33\n\t" \
  "s_cmp_eq_u32 s24, 0\n\t" \
  "s_cselect_b32 s95, s32, 0x80000000\n\t" \
  "s_add_u32 m0, s19, 126976\n\t" \
  "v_mov_b32 v56, s54\n\t" \
  "buffer_load_dwordx4 v49, s[12:15], s91 offen offset:0 lds\n\t" \
  "v_mov_b32 v57, s55\n\t" \
  "v_mov_b32 v55, v25\n\t" \
  "s_mov_b32 s12, s98\n\t" \
  "s_and_b32 s13, s99, 0xffff\n\t" \
  "s_mov_b32 s14, 2097152\n\t" \
  "buffer_load_dwordx2 v[30:31], v62, s[4:7], 0 offen offset:0 sc1\n\t" \
  "s_waitcnt vmcnt(9)\n\t" \
  "s_barrier\n\t" \
  "buffer_store_dwordx2 v[26:27], v25, s[4:7], s95 offen offset:0 sc1\n\t" \
  "ds_read_b64_tr_b16 v[176:177], v39 offset:0\n\t" \
  "ds_read_b64_tr_b16 v[178:179], v39 offset:512\n\t" \
  "ds_read_b64_tr_b16 v[180:181], v39 offset:2048\n\t" \
  "ds_read_b64_tr_b16 v[182:183], v39 offset:2560\n\t" \
  "ds_read_b64_tr_b16 v[184:185], v39 offset:4096\n\t" \
  "ds_read_b64_tr_b16 v[186:187], v39 offset:4608\n\t" \
  "ds_read_b64_tr_b16 v[188:189], v39 offset:6144\n\t" \
  "ds_read_b64_tr_b16 v[190:191], v39 offset:6656\n\t" \
  "ds_read_b64_tr_b16 v[192:193], v39 offset:8192\n\t" \
  "ds_read_b64_tr_b16 v[194:195], v39 offset:8704\n\t" \
  "ds_read_b64_tr_b16 v[196:197], v39 offset:10240\n\t" \
  "ds_read_b64_tr_b16 v[198:199], v39 offset:10752\n\t" \
  "s_waitcnt lgkmcnt(10)\n\t" \
  "v_mfma_f32_32x32x16_bf16 v[144:159], v[176:179], a[0:3], 0\n\t" \
  "ds_read_b64_tr_b16 v[200:201], v39 offset:12288\n\t" \
  "ds_read_b64_tr_b16 v[202:203], v39 offset:12800\n\t" \
  "s_waitcnt lgkmcnt(10)\n\t" \
  "v_mfma_f32_32x32x16_bf16 v[144:159], v[180:183], a[4:7], v[144:159]\n\t" \
  "ds_read_b64_tr_b16 v[204:205], v39 offset:14336\n\t" \
  "ds_read_b64_tr_b16 v[206:207], v39 offset:14848\n\t" \
  "s_waitcnt lgkmcnt(10)\n\t" \
  "v_mfma_f32_32x32x16_bf16 v[144:159], v[184:187], a[8:11], v[144:159]\n\t" \
  "ds_read_b64_tr_b16 v[176:177], v39 offset:16384\n\t" \
  "ds_read_b64_tr_b16 v[178:179], v39 offset:16896\n\t" \
  "s_waitcnt lgkmcnt(10)\n\t" \
  "v_mfma_f32_32x32x16_bf16 v[144:159], v[188:191], a[12:15], v[144:159]\n\t" \
  "ds_read_b64_tr_b16 v[180:181], v39 offset:18432\n\t" \
  "ds_read_b64_tr_b16 v[182:183], v39 offset:18944\n\t" \
  "s_waitcnt lgkmcnt(10)\n\t" \
  "v_mfma_f32_32x32x16_bf16 v[144:159], v[192:195], a[16:19], v[144:159]\n\t" \
  "ds_read_b64_tr_b16 v[184:185], v39 offset:20480\n\t" \
  "ds_read_b64_tr_b16 v[186:187], v39 offset:20992\n\t" \
  "s_waitcnt lgkmcnt(10)\n\t" \
  "v_mfma_f32_32x32x16_bf16 v[144:159], v[196:199], a[20:23], v[144:159]\n\t" \
  "ds_read_b64_tr_b16 v[188:189], v39 offset:22528\n\t" \
  "ds_read_b64_tr_b16 v[190:191], v39 offset:23040\n\t" \
  "s_waitcnt lgkmcnt(10)\n\t" \
  "v_mfma_f32_32x32x16_bf16 v[144:159], v[200:203], a[24:27], v[144:159]\n\t" \
  "ds_read_b64_tr_b16 v[192:193], v39 offset:24576\n\t" \
  "ds_read_b64_tr_b16 v[194:195], v39 offset:25088\n\t" \
  "s_waitcnt lgkmcnt(10)\n\t" \
  "v_mfma_f32_32x32x16_bf16 v[144:159], v[204:207], a[28:31], v[144:159]\n\t" \
  "ds_read_b64_tr_b16 v[196:197], v39 offset:26624\n\t" \
  "ds_read_b64_tr_b16 v[198:199], v39 offset:27136\n\t" \
  "s_waitcnt lgkmcnt(10)\n\t" \
  "v_mfma_f32_32x32x16_bf16 v[144:159], v[176:179], a[32:35], v[144:159]\n\t" \
  "ds_read_b64_tr_b16 v[200:201], v39 offset:28672\n\t" \
  "ds_read_b64_tr_b16 v[202:203], v39 offset:29184\n\t" \
  "s_waitcnt lgkmcnt(10)\n\t" \
  "v_mfma_f32_32x32x16_bf16 v[144:159], v[180:183], a[36:39], v[144:159]\n\t" \
  "ds_read_b64_tr_b16 v[204:205], v39 offset:30720\n\t" \
  "ds_read_b64_tr_b16 v[206:207], v39 offset:31232\n\t" \
  "s_waitcnt vmcnt(2) lgkmcnt(0)\n\t" \
  "s_barrier\n\t" \
  "s_waitcnt vmcnt(1)\n\t" \
  "s_movk_i32 s90, 0x7fff\n\t" \
  "Lp0_%=:\n\t" \
  "v_cmp_ne_u64 vcc, s[54:55], v[30:31]\n\t" \
  "s_and_b64 vcc, exec, vcc\n\t" \
  "s_cbranch_scc0 Lq0_%=\n\t" \
  "s_sub_u32 s90, s90, 1\n\t" \
  "s_cbranch_scc1 Lq0_%=\n\t" \
  "s_sleep 3\n\t" \
  "buffer_load_dwordx2 v[30:31], v62, s[4:7], 0 offen sc1\n\t" \
  "s_waitcnt vmcnt(0)\n\t" \
  "s_branch Lp0_%=\n\t" \
  "Lq0_%=:\n\t" \
  "v_mfma_f32_32x32x16_bf16 v[144:159], v[184:187], a[40:43], v[144:159]\n\t" \
  "ds_read_b64_tr_b16 v[176:177], v38 offset:32768\n\t" \
  "ds_read_b64_tr_b16 v[178:179], v38 offset:33280\n\t" \
  "buffer_load_dwordx4 a[192:195], v33, s[4:7], s36 offen offset:0 sc1\n\t" \
  "v_mfma_f32_32x32x16_bf16 v[144:159], v[188:191], a[44:47], v[144:159]\n\t" \
  "ds_read_b64_tr_b16 v[180:181], v38 offset:34816\n\t" \
  "ds_read_b64_tr_b16 v[182:183], v38 offset:35328\n\t" \
  "ds_write_b128 v17, v[64:67] offset:8192\n\t" \
  "v_mfma_f32_32x32x16_bf16 v[144:159], v[192:195], a[48:51], v[144:159]\n\t" \
  "ds_read_b64_tr_b16 v[184:185], v38 offset:36864\n\t" \
  "ds_read_b64_tr_b16 v[186:187], v38 offset:37376\n\t" \
  "ds_write_b128 v17, v[68:71] offset:28672\n\t" \
  "buffer_load_dwordx4 a[196:199], v33, s[4:7], s36 offen offset:1024 sc1\n\t" \
  "v_mfma_f32_32x32x16_bf16 v[144:159], v[196:199], a[52:55], v[144:159]\n\t" \
  "ds_read_b64_tr_b16 v[188:189], v38 offset:38912\n\t" \
  "ds_read_b64_tr_b16 v[190:191], v38 offset:39424\n\t" \
  "ds_write_b128 v18, v[72:75] offset:0\n\t" \
  "v_mfma_f32_32x32x16_bf16 v[144:159], v[200:203], a[56:59], v[144:159]\n\t" \
  "ds_read_b64_tr_b16 v[192:193], v38 offset:40960\n\t" \
  "ds_read_b64_tr_b16 v[194:195], v38 offset:41472\n\t" \
  "ds_write_b128 v18, v[76:79] offset:512\n\t" \
  "buffer_load_dwordx4 a[200:203], v34, s[4:7], s36 offen offset:0 sc1\n\t" \
  "v_mfma_f32_32x32x16_bf16 v[144:159], v[204:207], a[60:63], v[144:159]\n\t" \
  "ds_read_b64_tr_b16 v[196:197], v38 offset:43008\n\t" \
  "ds_read_b64_tr_b16 v[198:199], v38 offset:43520\n\t" \
  "ds_write_b128 v16, v[80:83] offset:0\n\t" \
  "s_waitcnt lgkmcnt(15)\n\t" \
  "v_mfma_f32_32x32x16_bf16 v[144:159], v[176:179], a[64:67], v[144:159]\n\t" \
  "ds_read_b64_tr_b16 v[200:201], v38 offset:45056\n\t" \
  "ds_read_b64_tr_b16 v[202:203], v38 offset:45568\n\t" \
  "ds_write_b128 v16, v[80:83] offset:2048\n\t" \
  "buffer_load_dwordx4 a[204:207], v34, s[4:7], s36 offen offset:1024 sc1\n\t" \
  "s_add_u32 s20, s36, 4096\n\t" \
  "s_waitcnt lgkmcnt(15)\n\t" \
  "v_mfma_f32_32x32x16_bf16 v[144:159], v[180:183], a[68:71], v[144:159]\n\t" \
  "ds_read_b64_tr_b16 v[204:205], v38 offset:47104\n\t" \
  "ds_read_b64_tr_b16 v[206:207], v38 offset:47616\n\t" \
  "ds_write_b128 v16, v[80:83] offset:4096\n\t" \
  "s_waitcnt lgkmcnt(15)\n\t" \
  "v_mfma_f32_32x32x16_bf16 v[144:159], v[184:187], a[72:75], v[144:159]\n\t" \
  "ds_read_b64_tr_b16 v[176:177], v38 offset:49152\n\t" \
  "ds_read_b64_tr_b16 v[178:179], v38 offset:49664\n\t" \
  "ds_write_b128 v16, v[80:83] offset:6144\n\t" \
  "buffer_load_dwordx4 a[208:211], v33, s[4:7], s20 offen offset:0 sc1\n\t" \
  "s_waitcnt lgkmcnt(15)\n\t" \
  "v_mfma_f32_32x32x16_bf16 v[144:159], v[188:191], a[76:79], v[144:159]\n\t" \
  "ds_read_b64_tr_b16 v[180:181], v38 offset:51200\n\t" \
  "ds_read_b64_tr_b16 v[182:183], v38 offset:51712\n\t" \
  "ds_write_b128 v16, v[80:83] offset:8192\n\t" \
  "s_waitcnt lgkmcnt(15)\n\t" \
  "v_mfma_f32_32x32x16_bf16 v[144:159], v[192:195], a[80:83], v[144:159]\n\t" \
  "ds_read_b64_tr_b16 v[184:185], v38 offset:53248\n\t" \
  "ds_read_b64_tr_b16 v[186:187], v38 offset:53760\n\t" \
  "buffer_load_dwordx4 a[212:215], v33, s[4:7], s20 offen offset:1024 sc1\n\t" \
  "s_waitcnt lgkmcnt(15)\n\t" \
  "v_mfma_f32_32x32x16_bf16 v[144:159], v[196:199], a[84:87], v[144:159]\n\t" \
  "ds_read_b64_tr_b16 v[188:189], v38 offset:55296\n\t" \
  "ds_read_b64_tr_b16 v[190:191], v38 offset:55808\n\t" \
  "s_waitcnt lgkmcnt(14)\n\t" \
  "v_mfma_f32_32x32x16_bf16 v[144:159], v[200:203], a[88:91], v[144:159]\n\t" \
  "ds_read_b64_tr_b16 v[192:193], v38 offset:57344\n\t" \
  "ds_read_b64_tr_b16 v[194:195], v38 offset:57856\n\t" \
  "buffer_load_dwordx4 a[216:219], v34, s[4:7], s20 offen offset:0 sc1\n\t" \
  "s_waitcnt lgkmcnt(13)\n\t" \
  "v_mfma_f32_32x32x16_bf16 v[144:159], v[204:207], a[92:95], v[144:159]\n\t" \
  "ds_read_b64_tr_b16 v[196:197], v38 offset:59392\n\t" \
  "ds_read_b64_tr_b16 v[198:199], v38 offset:59904\n\t" \
  "s_waitcnt lgkmcnt(12)\n\t" \
  "v_mfma_f32_32x32x16_bf16 v[144:159], v[176:179], a[96:99], v[144:159]\n\t" \
  "ds_read_b64_tr_b16 v[200:201], v38 offset:61440\n\t" \
  "ds_read_b64_tr_b16 v[202:203], v38 offset:61952\n\t" \
  "buffer_load_dwordx4 a[220:223], v34, s[4:7], s20 offen offset:1024 sc1\n\t" \
  "s_add_u32 s21, s36, 8192\n\t" \
  "s_waitcnt lgkmcnt(11)\n\t" \
  "v_mfma_f32_32x32x16_bf16 v[144:159], v[180:183], a[100:103], v[144:159]\n\t" \
  "ds_read_b64_tr_b16 v[204:205], v38 offset:63488\n\t" \
  "ds_read_b64_tr_b16 v[206:207], v38 offset:64000\n\t" \
  "s_waitcnt lgkmcnt(10)\n\t" \
  "v_mfma_f32_32x32x16_bf16 v[144:159], v[184:187], a[104:107], v[144:159]\n\t" \
  "buffer_load_dwordx4 a[224:227], v33, s[4:7], s21 offen offset:0 sc1\n\t" \
  "s_waitcnt lgkmcnt(8)\n\t" \
  "v_mfma_f32_32x32x16_bf16 v[144:159], v[188:191], a[108:111], v[144:159]\n\t" \
  "s_waitcnt lgkmcnt(6)\n\t" \
  "v_mfma_f32_32x32x16_bf16 v[144:159], v[192:195], a[112:115], v[144:159]\n\t" \
  "buffer_load_dwordx4 a[228:231], v33, s[4:7], s21 offen offset:1024 sc1\n\t" \
  "s_waitcnt lgkmcnt(4)\n\t" \
  "v_mfma_f32_32x32x16_bf16 v[144:159], v[196:199], a[116:119], v[144:159]\n\t" \
  "s_waitcnt lgkmcnt(2)\n\t" \
  "v_mfma_f32_32x32x16_bf16 v[144:159], v[200:203], a[120:123], v[144:159]\n\t" \
  "buffer_load_dwordx4 a[232:235], v34, s[4:7], s21 offen offset:0 sc1\n\t" \
  "s_waitcnt lgkmcnt(0)\n\t" \
  "v_mfma_f32_32x32x16_bf16 v[144:159], v[204:207], a[124:127], v[144:159]\n\t" \
  "buffer_load_dwordx4 a[236:239], v34, s[4:7], s21 offen offset:1024 sc1\n\t" \
  "s_add_u32 s20, s36, 12288\n\t" \
  "buffer_load_dwordx4 a[240:243], v33, s[4:7], s20 offen offset:0 sc1\n\t" \
  "s_nop 8\n\t" \
  "v_mul_f32 v144, 0x3eb8aa3b, v144\n\t" \
  "v_mul_f32 v145, 0x3eb8aa3b, v145\n\t" \
  "v_mul_f32 v146, 0x3eb8aa3b, v146\n\t" \
  "v_mul_f32 v147, 0x3eb8aa3b, v147\n\t" \
  "v_mul_f32 v148, 0x3eb8aa3b, v148\n\t" \
  "v_mul_f32 v149, 0x3eb8aa3b, v149\n\t" \
  "v_mul_f32 v150, 0x3eb8aa3b, v150\n\t" \
  "v_mul_f32 v151, 0x3eb8aa3b, v151\n\t" \
  "v_mul_f32 v152, 0x3eb8aa3b, v152\n\t" \
  "v_mul_f32 v153, 0x3eb8aa3b, v153\n\t" \
  "v_mul_f32 v154, 0x3eb8aa3b, v154\n\t" \
  "v_mul_f32 v155, 0x3eb8aa3b, v155\n\t" \
  "v_mul_f32 v156, 0x3eb8aa3b, v156\n\t" \
  "v_mul_f32 v157, 0x3eb8aa3b, v157\n\t" \
  "v_mul_f32 v158, 0x3eb8aa3b, v158\n\t" \
  "v_mul_f32 v159, 0x3eb8aa3b, v159\n\t" \
  "v_cvt_pk_bf16_f32 v0, v144, v145\n\t" \
  "v_cvt_pk_bf16_f32 v1, v146, v147\n\t" \
  "v_cvt_pk_bf16_f32 v2, v148, v149\n\t" \
  "v_cvt_pk_bf16_f32 v3, v150, v151\n\t" \
  "v_cvt_pk_bf16_f32 v4, v152, v153\n\t" \
  "v_cvt_pk_bf16_f32 v5, v154, v155\n\t" \
  "v_cvt_pk_bf16_f32 v6, v156, v157\n\t" \
  "v_cvt_pk_bf16_f32 v7, v158, v159\n\t" \
  "ds_write_b128 v17, v[0:3] offset:0\n\t" \
  "ds_write_b128 v17, v[4:7] offset:20480\n\t" \
  "s_cmp_gt_u32 s25, 3\n\t" \
  "s_cbranch_scc1 Lfull_%=\n\t" \
  "v_mov_b32 v80, 0\n\t" \
  "v_mov_b32 v81, 0\n\t" \
  "v_mov_b32 v82, 0\n\t" \
  "v_mov_b32 v83, 0\n\t" \
  "ds_write_b128 v16, v[80:83] offset:0\n\t" \
  "s_cmp_eq_u32 s25, 3\n\t" \
  "s_cbranch_scc1 Lfull_%=\n\t" \
  "ds_write_b128 v16, v[80:83] offset:2048\n\t" \
  "s_cmp_eq_u32 s25, 2\n\t" \
  "s_cbranch_scc1 Lfull_%=\n\t" \
  "ds_write_b128 v16, v[80:83] offset:4096\n\t" \
  "s_cmp_eq_u32 s25, 1\n\t" \
  "s_cbranch_scc1 Lfull_%=\n\t" \
  "ds_write_b128 v16, v[80:83] offset:6144\n\t" \
  "Lfull_%=:\n\t" \
  "s_cmp_lg_u32 s24, 3\n\t" \
  "s_cbranch_scc1 Lkv_%=\n\t" \
  "s_movk_i32 s90, 0x7fff\n\t" \
  "Lkw_%=:\n\t" \
  "buffer_load_dwordx2 v[30:31], v61, s[4:7], 0 offen sc1\n\t" \
  "s_waitcnt vmcnt(0)\n\t" \
  "v_cmp_ne_u64 vcc, s[54:55], v[30:31]\n\t" \
  "s_and_b64 vcc, exec, vcc\n\t" \
  "s_cbranch_scc0 Lkv_%=\n\t" \
  "s_sub_u32 s90, s90, 1\n\t" \
  "s_cbranch_scc0 Lkw_%=\n\t" \
  "Lkv_%=:\n\t" \
  "buffer_load_dwordx4 a[244:247], v33, s[4:7], s20 offen offset:1024 sc1\n\t" \
  "buffer_load_dwordx4 a[248:251], v34, s[4:7], s20 offen offset:0 sc1\n\t" \
  "buffer_load_dwordx4 a[252:255], v34, s[4:7], s20 offen offset:1024 sc1\n\t" \
  "s_add_u32 s21, s37, 32768\n\t" \
  "buffer_load_dwordx4 a[128:131], v32, s[4:7], s21 offen offset:0 sc1\n\t" \
  "s_waitcnt lgkmcnt(0)\n\t" \
  "s_barrier\n\t" \
  "v_add_u32 v17, s31, v24\n\t" \
  "ds_read_b128 v[0:3], v17 offset:0\n\t" \
  "ds_read_b128 v[4:7], v17 offset:1024\n\t" \
  "s_cmp_lg_u32 s25, 0\n\t" \
  "s_cbranch_scc1 Lnz_%=\n\t" \
  "v_lshlrev_b32 v18, 9, v9\n\t" \
  "v_add_u32 v18, s31, v18\n\t" \
  "ds_read_b128 v[80:83], v18 offset:8192\n\t" \
  "s_waitcnt lgkmcnt(0)\n\t" \
  "ds_write_b128 v17, v[80:83] offset:0\n\t" \
  "ds_write_b128 v17, v[80:83] offset:1024\n\t" \
  "ds_write_b128 v17, v[80:83] offset:2048\n\t" \
  "ds_write_b128 v17, v[80:83] offset:3072\n\t" \
  "ds_write_b128 v17, v[80:83] offset:4096\n\t" \
  "ds_write_b128 v17, v[80:83] offset:5120\n\t" \
  "ds_write_b128 v17, v[80:83] offset:6144\n\t" \
  "ds_write_b128 v17, v[80:83] offset:7168\n\t" \
  "Lnz_%=:\n\t" \
  "s_waitcnt lgkmcnt(0)\n\t" \
  "s_lshl_b32 s94, s26, 16\n\t" \
  "v_add_u32 v242, 0x140000, v32\n\t" \
  "v_add_u32 v243, 0x340000, v32\n\t" \
  "s_cmp_eq_u32 s25, 0\n\t" \
  "s_cselect_b32 s34, 0x80000000, 0\n\t" \
  "s_cselect_b32 s95, 98304, s31\n\t" \
  "v_or_b32 v242, s34, v242\n\t" \
  "s_add_i32 s88, s25, -4\n\t" \
  "s_max_i32 s89, s88, 0\n\t" \
  "s_lshl_b32 s89, s89, 11\n\t" \
  "s_add_u32 s89, s89, s94\n\t" \
  "s_add_u32 m0, s95, 0\n\t" \
  "v_mov_b32 v240, 0xffff\n\t" \
  "buffer_load_dwordx4 v242, s[4:7], s89 offen offset:0 sc1 lds\n\t" \
  "v_mov_b32 v241, 0xffff0000\n\t" \
  "v_cndmask_b32 v16, 0, v240, s[56:57]\n\t" \
  "v_cndmask_b32 v244, 0, v241, s[58:59]\n\t" \
  "v_or_b32 v16, v16, v244\n\t" \
  "v_cndmask_b32 v17, 0, v240, s[60:61]\n\t" \
  "v_cndmask_b32 v244, 0, v241, s[62:63]\n\t" \
  "v_or_b32 v17, v17, v244\n\t" \
  "v_cndmask_b32 v18, 0, v240, s[64:65]\n\t" \
  "buffer_load_dwordx4 v242, s[4:7], s89 offen offset:1024 sc1 lds\n\t" \
  "v_cndmask_b32 v244, 0, v241, s[66:67]\n\t" \
  "v_or_b32 v18, v18, v244\n\t" \
  "v_cndmask_b32 v19, 0, v240, s[68:69]\n\t" \
  "v_cndmask_b32 v244, 0, v241, s[70:71]\n\t" \
  "v_or_b32 v19, v19, v244\n\t" \
  "v_cndmask_b32 v20, 0, v240, s[72:73]\n\t" \
  "v_cndmask_b32 v244, 0, v241, s[74:75]\n\t" \
  "v_or_b32 v20, v20, v244\n\t" \
  "s_add_i32 s88, s25, -1\n\t" \
  "s_max_i32 s89, s88, 0\n\t" \
  "s_lshl_b32 s89, s89, 11\n\t" \
  "s_add_u32 s89, s89, s94\n\t" \
  "s_add_u32 m0, s95, 6144\n\t" \
  "v_cndmask_b32 v21, 0, v240, s[76:77]\n\t" \
  "buffer_load_dwordx4 v242, s[4:7], s89 offen offset:0 sc1 lds\n\t" \
  "v_cndmask_b32 v244, 0, v241, s[78:79]\n\t" \
  "v_or_b32 v21, v21, v244\n\t" \
  "v_cndmask_b32 v22, 0, v240, s[80:81]\n\t" \
  "v_cndmask_b32 v244, 0, v241, s[82:83]\n\t" \
  "v_or_b32 v22, v22, v244\n\t" \
  "v_cndmask_b32 v23, 0, v240, s[84:85]\n\t" \
  "v_cndmask_b32 v244, 0, v241, s[86:87]\n\t" \
  "v_or_b32 v23, v23, v244\n\t" \
  "buffer_load_dwordx4 v242, s[4:7], s89 offen offset:1024 sc1 lds\n\t" \
  "v_and_b32 v245, 0xf0, v32\n\t" \
  "v_lshrrev_b32 v240, 1, v32\n\t" \
  "v_and_b32 v240, 0x100, v240\n\t" \
  "v_or_b32 v246, v245, v240\n\t" \
  "v_bfrev_b32 v247, 1\n\t" \
  "v_add_u32 v35, s31, v32\n\t" \
  "s_add_u32 s32, s31, 10240\n\t" \
  "v_add_u32 v240, s32, v246\n\t" \
  "s_add_i32 s88, s25, -2\n\t" \
  "s_max_i32 s89, s88, 0\n\t" \
  "s_lshl_b32 s89, s89, 11\n\t" \
  "s_add_u32 s89, s89, s94\n\t" \
  "s_add_u32 m0, s95, 4096\n\t" \
  "v_add_u32 v241, 81920, v245\n\t" \
  "buffer_load_dwordx4 v242, s[4:7], s89 offen offset:0 sc1 lds\n\t" \
  "s_mov_b32 s32, 0xffff0000\n\t" \
  "s_mov_b32 s33, 0xffff0000\n\t" \
  "v_cndmask_b32 v36, v240, v241, s[32:33]\n\t" \
  "v_and_b32 v240, 0x100, v32\n\t" \
  "v_lshl_or_b32 v37, v240, 6, v246\n\t" \
  "s_and_b32 s34, s24, 1\n\t" \
  "s_lshr_b32 s35, s24, 1\n\t" \
  "s_lshl2_add_u32 s35, s27, s35\n\t" \
  "buffer_load_dwordx4 v242, s[4:7], s89 offen offset:1024 sc1 lds\n\t" \
  "s_lshl_b32 s32, s35, 14\n\t" \
  "s_lshl_b32 s33, s28, 7\n\t" \
  "s_add_u32 s32, s32, s33\n\t" \
  "s_lshl_b32 s33, s34, 6\n\t" \
  "s_add_u32 s32, s32, s33\n\t" \
  "v_lshrrev_b32 v240, 4, v32\n\t" \
  "v_and_b32 v240, 0x30, v240\n\t" \
  "v_lshl_or_b32 v240, v245, 6, v240\n\t" \
  "s_add_i32 s88, s25, -3\n\t" \
  "s_max_i32 s89, s88, 0\n\t" \
  "s_lshl_b32 s89, s89, 11\n\t" \
  "s_add_u32 s89, s89, s94\n\t" \
  "s_add_u32 m0, s95, 2048\n\t" \
  "v_add_u32 v43, s32, v240\n\t" \
  "buffer_load_dwordx4 v242, s[4:7], s89 offen offset:0 sc1 lds\n\t" \
  "v_and_b32 v240, 0x70, v32\n\t" \
  "v_lshlrev_b32 v240, 3, v240\n\t" \
  "s_lshl2_add_u32 s32, s27, s24\n\t" \
  "s_lshl_b32 s32, s32, 10\n\t" \
  "s_add_u32 s32, s32, 0x1c000\n\t" \
  "v_add_u32 v38, s32, v240\n\t" \
  "v_and_b32 v241, 0x80, v32\n\t" \
  "v_lshl_or_b32 v240, v241, 5, v240\n\t" \
  "buffer_load_dwordx4 v242, s[4:7], s89 offen offset:1024 sc1 lds\n\t" \
  "s_lshl3_add_u32 s32, s29, s24\n\t" \
  "s_lshl_b32 s32, s32, 10\n\t" \
  "s_add_u32 s32, s32, 0xa0000\n\t" \
  "v_add_u32 v45, s32, v240\n\t" \
  "s_xor_b32 s32, s24, 1\n\t" \
  "s_mul_i32 s32, s32, 6144\n\t" \
  "s_add_u32 s32, s32, 82944\n\t" \
  "v_add_u32 v39, s32, v32\n\t" \
  "s_add_i32 s88, s25, -4\n\t" \
  "s_max_i32 s89, s88, 0\n\t" \
  "s_lshl_b32 s89, s89, 11\n\t" \
  "s_add_u32 s89, s89, s94\n\t" \
  "s_cmp_lt_i32 s88, 0\n\t" \
  "s_cselect_b32 s89, 0x80000000, s89\n\t" \
  "s_add_u32 m0, s31, 10240\n\t" \
  "s_xor_b32 s32, s24, 2\n\t" \
  "buffer_load_dwordx4 v243, s[4:7], s89 offen offset:0 sc1 lds\n\t" \
  "s_mul_i32 s32, s32, 6144\n\t" \
  "s_add_u32 s32, s32, 84992\n\t" \
  "v_add_u32 v40, s32, v32\n\t" \
  "s_xor_b32 s32, s24, 3\n\t" \
  "s_mul_i32 s32, s32, 6144\n\t" \
  "s_add_u32 s32, s32, 87040\n\t" \
  "v_add_u32 v41, s32, v32\n\t" \
  "s_mul_i32 s32, s24, 6144\n\t" \
  "s_add_u32 s32, s32, 82944\n\t" \
  "v_add_u32 v42, s32, v32\n\t" \
  "v_lshrrev_b32 v240, 1, v32\n\t" \
  "s_mov_b64 s[32:33], 0xffff\n\t" \
  "v_cndmask_b32 v44, v247, v240, s[32:33]\n\t" \
  "v_lshrrev_b32 v240, 1, v245\n\t" \
  "buffer_load_dwordx4 v243, s[4:7], s89 offen offset:1024 sc1 lds\n\t" \
  "v_and_b32 v241, 0x100, v32\n\t" \
  "v_lshl_or_b32 v240, v241, 5, v240\n\t" \
  "v_and_b32 v241, 0x200, v32\n\t" \
  "v_lshl_or_b32 v240, v241, 2, v240\n\t" \
  "s_lshl_b32 s32, s29, 14\n\t" \
  "v_add_u32 v46, s32, v240\n\t" \
  "v_lshlrev_b32 v240, 1, v32\n\t" \
  "v_add_u32 v48, 156672, v240\n\t" \
  "s_lshl_b32 s32, s24, 3\n\t" \
  "v_add_u32 v47, s32, v48\n\t" \
  "v_add_u32 v49, 160768, v32\n\t" \
  "v_add_u32 v50, 160768, v245\n\t" \
  "v_add_u32 v51, 65536, v32\n\t" \
  "s_mov_b32 s32, -1\n\t" \
  "s_add_i32 s88, s25, -3\n\t" \
  "s_max_i32 s89, s88, 0\n\t" \
  "s_lshl_b32 s89, s89, 11\n\t" \
  "s_add_u32 s89, s89, s94\n\t" \
  "s_cmp_lt_i32 s88, 0\n\t" \
  "s_cselect_b32 s89, 0x80000000, s89\n\t" \
  "s_add_u32 m0, s31, 12288\n\t" \
  "s_mov_b32 s33, 0xffff\n\t" \
  "buffer_load_dwordx4 v243, s[4:7], s89 offen offset:0 sc1 lds\n\t" \
  "v_cndmask_b32 v52, v247, v32, s[32:33]\n\t" \
  "s_lshl_b32 s34, s24, 3\n\t" \
  "s_lshr_b32 s32, 0x2c1d0e00, s34\n\t" \
  "s_and_b32 s32, s32, 255\n\t" \
  "s_lshr_b32 s33, 0x392b1d0e, s34\n\t" \
  "s_and_b32 s33, s33, 255\n\t" \
  "v_lshrrev_b32 v240, 4, v245\n\t" \
  "v_add_u32 v240, s32, v240\n\t" \
  "v_min_u32 v240, s33, v240\n\t" \
  "s_lshr_b32 s32, s27, 1\n\t" \
  "s_lshl_b32 s32, s32, 13\n\t" \
  "s_add_u32 s32, s32, 0x5c000\n\t" \
  "v_lshl_add_u32 v53, v240, 7, s32\n\t" \
  "s_cmp_eq_u32 s24, 3\n\t" \
  "s_cselect_b32 s33, -1, 0\n\t" \
  "buffer_load_dwordx4 v243, s[4:7], s89 offen offset:1024 sc1 lds\n\t" \
  "s_mov_b32 s32, 0\n\t" \
  "v_cndmask_b32 v54, v32, v247, s[32:33]\n\t" \
  "v_lshlrev_b32 v240, 3, v32\n\t" \
  "v_add_u32 v63, 0x9000, v240\n\t" \
  "ds_read_b128 v[96:99], v35 offset:8192\n\t" \
  "ds_read_b128 v[100:103], v35 offset:9216\n\t" \
  "s_waitcnt vmcnt(10)\n\t" \
  "ds_read_b128 v[64:67], v35 offset:0\n\t" \
  "ds_read_b128 v[68:71], v35 offset:1024\n\t" \
  "s_waitcnt vmcnt(8)\n\t" \
  "ds_read_b128 v[92:95], v35 offset:7168\n\t" \
  "ds_read_b128 v[88:91], v35 offset:6144\n\t" \
  "s_waitcnt lgkmcnt(1)\n\t" \
  "v_mfma_f32_32x32x16_bf16 v[104:119], v[96:99], v[0:3], 0\n\t" \
  "v_mfma_f32_32x32x16_bf16 v[120:135], v[64:67], v[0:3], 0\n\t" \
  "v_mfma_f32_32x32x16_bf16 v[152:167], v[92:95], v[0:3], 0\n\t" \
  "s_nop 10\n\t" \
  "v_cndmask_b32 v136, v104, v120, s[56:57]\n\t" \
  "v_cndmask_b32 v137, v105, v121, s[58:59]\n\t" \
  "v_cndmask_b32 v138, v106, v122, s[60:61]\n\t" \
  "v_cndmask_b32 v139, v107, v123, s[62:63]\n\t" \
  "v_cndmask_b32 v140, v108, v124, s[64:65]\n\t" \
  "v_cndmask_b32 v141, v109, v125, s[66:67]\n\t" \
  "v_cndmask_b32 v142, v110, v126, s[68:69]\n\t" \
  "v_cndmask_b32 v143, v111, v127, s[70:71]\n\t" \
  "v_cndmask_b32 v144, v112, v128, s[72:73]\n\t" \
  "v_cndmask_b32 v145, v113, v129, s[74:75]\n\t" \
  "v_cndmask_b32 v146, v114, v130, s[76:77]\n\t" \
  "v_cndmask_b32 v147, v115, v131, s[78:79]\n\t" \
  "v_cndmask_b32 v148, v116, v132, s[80:81]\n\t" \
  "v_cndmask_b32 v149, v117, v133, s[82:83]\n\t" \
  "v_cndmask_b32 v150, v118, v134, s[84:85]\n\t" \
  "v_cndmask_b32 v151, v119, v135, s[86:87]\n\t" \
  "s_waitcnt vmcnt(4)\n\t" \
  "ds_read_b128 v[84:87], v35 offset:5120\n\t" \
  "ds_read_b128 v[80:83], v35 offset:4096\n\t" \
  "ds_read_b128 v[76:79], v35 offset:3072\n\t" \
  "ds_read_b128 v[72:75], v35 offset:2048\n\t" \
  "s_waitcnt lgkmcnt(4)\n\t" \
  "v_mfma_f32_32x32x16_bf16 v[104:119], v[88:91], v[0:3], 0\n\t" \
  "v_max3_f32 v232, v136, v137, v136\n\t" \
  "v_max3_f32 v233, v138, v139, v138\n\t" \
  "v_max3_f32 v232, v140, v141, v232\n\t" \
  "v_max3_f32 v233, v142, v143, v233\n\t" \
  "v_max3_f32 v232, v144, v145, v232\n\t" \
  "v_max3_f32 v233, v146, v147, v233\n\t" \
  "v_max3_f32 v232, v148, v149, v232\n\t" \
  "v_max3_f32 v233, v150, v151, v233\n\t" \
  "s_waitcnt lgkmcnt(0)\n\t" \
  "v_mfma_f32_32x32x16_bf16 v[120:135], v[84:87], v[0:3], 0\n\t" \
  "v_max3_f32 v232, v152, v153, v232\n\t" \
  "v_max3_f32 v233, v154, v155, v233\n\t" \
  "v_max3_f32 v232, v156, v157, v232\n\t" \
  "v_max3_f32 v233, v158, v159, v233\n\t" \
  "v_max3_f32 v232, v160, v161, v232\n\t" \
  "v_max3_f32 v233, v162, v163, v233\n\t" \
  "v_max3_f32 v232, v164, v165, v232\n\t" \
  "v_max3_f32 v233, v166, v167, v233\n\t" \
  "s_add_i32 s88, s25, -2\n\t" \
  "s_max_i32 s89, s88, 0\n\t" \
  "s_lshl_b32 s89, s89, 11\n\t" \
  "s_add_u32 s89, s89, s94\n\t" \
  "s_cmp_lt_i32 s88, 0\n\t" \
  "s_cselect_b32 s89, 0x80000000, s89\n\t" \
  "s_add_u32 m0, s31, 14336\n\t" \
  "s_nop 0\n\t" \
  "buffer_load_dwordx4 v243, s[4:7], s89 offen offset:0 sc1 lds\n\t" \
  "v_mfma_f32_32x32x16_bf16 v[152:167], v[80:83], v[0:3], 0\n\t" \
  "v_max3_f32 v232, v104, v105, v232\n\t" \
  "v_max3_f32 v233, v106, v107, v233\n\t" \
  "v_max3_f32 v232, v108, v109, v232\n\t" \
  "v_max3_f32 v233, v110, v111, v233\n\t" \
  "v_max3_f32 v232, v112, v113, v232\n\t" \
  "v_max3_f32 v233, v114, v115, v233\n\t" \
  "v_max3_f32 v232, v116, v117, v232\n\t" \
  "v_max3_f32 v233, v118, v119, v233\n\t" \
  "buffer_load_dwordx4 v243, s[4:7], s89 offen offset:1024 sc1 lds\n\t" \
  "v_mfma_f32_32x32x16_bf16 v[104:119], v[76:79], v[0:3], 0\n\t" \
  "v_max3_f32 v232, v120, v121, v232\n\t" \
  "v_max3_f32 v233, v122, v123, v233\n\t" \
  "v_max3_f32 v232, v124, v125, v232\n\t" \
  "v_max3_f32 v233, v126, v127, v233\n\t" \
  "v_max3_f32 v232, v128, v129, v232\n\t" \
  "v_max3_f32 v233, v130, v131, v233\n\t" \
  "v_max3_f32 v232, v132, v133, v232\n\t" \
  "v_max3_f32 v233, v134, v135, v233\n\t" \
  "s_add_i32 s88, s25, -1\n\t" \
  "s_max_i32 s89, s88, 0\n\t" \
  "s_lshl_b32 s89, s89, 11\n\t" \
  "s_add_u32 s89, s89, s94\n\t" \
  "s_cmp_lt_i32 s88, 0\n\t" \
  "s_cselect_b32 s89, 0x80000000, s89\n\t" \
  "s_add_u32 m0, s31, 16384\n\t" \
  "s_nop 0\n\t" \
  "buffer_load_dwordx4 v243, s[4:7], s89 offen offset:0 sc1 lds\n\t" \
  "v_mfma_f32_32x32x16_bf16 v[120:135], v[72:75], v[0:3], 0\n\t" \
  "v_max3_f32 v232, v152, v153, v232\n\t" \
  "v_max3_f32 v233, v154, v155, v233\n\t" \
  "v_max3_f32 v232, v156, v157, v232\n\t" \
  "v_max3_f32 v233, v158, v159, v233\n\t" \
  "v_max3_f32 v232, v160, v161, v232\n\t" \
  "v_max3_f32 v233, v162, v163, v233\n\t" \
  "v_max3_f32 v232, v164, v165, v232\n\t" \
  "v_max3_f32 v233, v166, v167, v233\n\t" \
  "buffer_load_dwordx4 v243, s[4:7], s89 offen offset:1024 sc1 lds\n\t" \
  "v_mfma_f32_32x32x16_bf16 v[152:167], v[68:71], v[0:3], 0\n\t" \
  "v_max3_f32 v232, v104, v105, v232\n\t" \
  "v_max3_f32 v233, v106, v107, v233\n\t" \
  "v_max3_f32 v232, v108, v109, v232\n\t" \
  "v_max3_f32 v233, v110, v111, v233\n\t" \
  "v_max3_f32 v232, v112, v113, v232\n\t" \
  "v_max3_f32 v233, v114, v115, v233\n\t" \
  "v_max3_f32 v232, v116, v117, v232\n\t" \
  "v_max3_f32 v233, v118, v119, v233\n\t" \
  "buffer_load_dwordx4 v[8:11], v43, s[8:11], 0 offen offset:0\n\t" \
  "s_mov_b32 s88, 32768\n\t" \
  "buffer_load_dwordx4 v[12:15], v43, s[8:11], s88 offen offset:0\n\t" \
  "v_max3_f32 v232, v120, v121, v232\n\t" \
  "v_max3_f32 v233, v122, v123, v233\n\t" \
  "v_max3_f32 v232, v124, v125, v232\n\t" \
  "v_max3_f32 v233, v126, v127, v233\n\t" \
  "v_max3_f32 v232, v128, v129, v232\n\t" \
  "v_max3_f32 v233, v130, v131, v233\n\t" \
  "v_max3_f32 v232, v132, v133, v232\n\t" \
  "v_max3_f32 v233, v134, v135, v233\n\t" \
  "v_max3_f32 v232, v152, v153, v232\n\t" \
  "v_max3_f32 v233, v154, v155, v233\n\t" \
  "v_max3_f32 v232, v156, v157, v232\n\t" \
  "v_max3_f32 v233, v158, v159, v233\n\t" \
  "v_max3_f32 v232, v160, v161, v232\n\t" \
  "v_max3_f32 v233, v162, v163, v233\n\t" \
  "v_max3_f32 v232, v164, v165, v232\n\t" \
  "v_max3_f32 v233, v166, v167, v233\n\t" \
  "s_waitcnt vmcnt(2)\n\t" \
  "v_mov_b32 v248, -1\n\t" \
  "v_mov_b32 v249, -1\n\t" \
  "v_mov_b32 v250, -1\n\t" \
  "v_mov_b32 v251, -1\n\t" \
  "buffer_store_dwordx4 v[248:251], v32, s[4:7], s42 offen offset:0 sc1\n\t" \
  "buffer_store_dwordx2 v[248:249], v44, s[4:7], s43 offen offset:0 sc1\n\t" \
  "v_max_f32 v232, v232, v233\n\t" \
  "v_mov_b32 v233, v232\n\t" \
  "ds_read_b128 v[208:211], v36 offset:8192\n\t" \
  "ds_read_b128 v[212:215], v36 offset:8704\n\t" \
  "v_permlane32_swap_b32 v232, v233\n\t" \
  "ds_read_b128 v[216:219], v36 offset:0\n\t" \
  "ds_read_b128 v[220:223], v36 offset:512\n\t" \
  "v_max_f32 v232, v232, v233\n\t" \
  "buffer_load_dwordx4 a[132:135], v32, s[4:7], s21 offen offset:1024 sc1\n\t" \
  "v_xor_b32 v152, 0x80000000, v232\n\t" \
  "v_xor_b32 v153, 0x80000000, v232\n\t" \
  "v_xor_b32 v154, 0x80000000, v232\n\t" \
  "v_xor_b32 v155, 0x80000000, v232\n\t" \
  "v_xor_b32 v156, 0x80000000, v232\n\t" \
  "v_xor_b32 v157, 0x80000000, v232\n\t" \
  "v_xor_b32 v158, 0x80000000, v232\n\t" \
  "v_xor_b32 v159, 0x80000000, v232\n\t" \
  "v_xor_b32 v160, 0x80000000, v232\n\t" \
  "v_xor_b32 v161, 0x80000000, v232\n\t" \
  "v_xor_b32 v162, 0x80000000, v232\n\t" \
  "v_xor_b32 v163, 0x80000000, v232\n\t" \
  "v_xor_b32 v164, 0x80000000, v232\n\t" \
  "v_xor_b32 v165, 0x80000000, v232\n\t" \
  "v_xor_b32 v166, 0x80000000, v232\n\t" \
  "v_xor_b32 v167, 0x80000000, v232\n\t" \
  "buffer_load_dwordx4 a[136:139], v32, s[4:7], s21 offen offset:2048 sc1\n\t" \
  "v_sub_f32 v136, v136, v232\n\t" \
  "v_sub_f32 v137, v137, v232\n\t" \
  "v_mfma_f32_32x32x16_bf16 v[104:119], v[92:95], v[0:3], v[152:167]\n\t" \
  "v_sub_f32 v138, v138, v232\n\t" \
  "v_sub_f32 v139, v139, v232\n\t" \
  "v_sub_f32 v140, v140, v232\n\t" \
  "v_sub_f32 v141, v141, v232\n\t" \
  "v_sub_f32 v142, v142, v232\n\t" \
  "v_sub_f32 v143, v143, v232\n\t" \
  "v_sub_f32 v144, v144, v232\n\t" \
  "v_sub_f32 v145, v145, v232\n\t" \
  "v_sub_f32 v146, v146, v232\n\t" \
  "v_sub_f32 v147, v147, v232\n\t" \
  "v_sub_f32 v148, v148, v232\n\t" \
  "v_sub_f32 v149, v149, v232\n\t" \
  "v_sub_f32 v150, v150, v232\n\t" \
  "v_sub_f32 v151, v151, v232\n\t" \
  "buffer_load_dwordx4 a[140:143], v32, s[4:7], s21 offen offset:3072 sc1\n\t" \
  "v_exp_f32 v224, v136\n\t" \
  "v_exp_f32 v225, v137\n\t" \
  "v_exp_f32 v226, v138\n\t" \
  "v_exp_f32 v227, v139\n\t" \
  "v_cvt_pk_bf16_f32 v184, v224, v225\n\t" \
  "v_cvt_pk_bf16_f32 v185, v226, v227\n\t" \
  "v_exp_f32 v228, v140\n\t" \
  "v_exp_f32 v229, v141\n\t" \
  "v_exp_f32 v230, v142\n\t" \
  "v_exp_f32 v231, v143\n\t" \
  "v_cvt_pk_bf16_f32 v186, v228, v229\n\t" \
  "v_cvt_pk_bf16_f32 v187, v230, v231\n\t" \
  "s_add_u32 s20, s37, 36864\n\t" \
  "buffer_load_dwordx4 a[144:147], v32, s[4:7], s20 offen offset:0 sc1\n\t" \
  "v_exp_f32 v224, v144\n\t" \
  "v_exp_f32 v225, v145\n\t" \
  "v_exp_f32 v226, v146\n\t" \
  "v_exp_f32 v227, v147\n\t" \
  "v_cvt_pk_bf16_f32 v188, v224, v225\n\t" \
  "v_cvt_pk_bf16_f32 v189, v226, v227\n\t" \
  "v_exp_f32 v228, v148\n\t" \
  "v_exp_f32 v229, v149\n\t" \
  "v_exp_f32 v230, v150\n\t" \
  "v_exp_f32 v231, v151\n\t" \
  "v_cvt_pk_bf16_f32 v190, v228, v229\n\t" \
  "v_cvt_pk_bf16_f32 v191, v230, v231\n\t" \
  "buffer_load_dwordx4 a[148:151], v32, s[4:7], s20 offen offset:1024 sc1\n\t" \
  "v_and_b32 v200, v184, v16\n\t" \
  "v_and_b32 v201, v185, v17\n\t" \
  "v_and_b32 v202, v186, v18\n\t" \
  "v_and_b32 v203, v187, v19\n\t" \
  "v_and_b32 v204, v188, v20\n\t" \
  "v_and_b32 v205, v189, v21\n\t" \
  "v_and_b32 v206, v190, v22\n\t" \
  "v_and_b32 v207, v191, v23\n\t" \
  "s_waitcnt lgkmcnt(0)\n\t" \
  "v_xor_b32 v184, v184, v200\n\t" \
  "v_mfma_f32_32x32x16_bf16 v[168:183], v[216:219], v[200:203], 0\n\t" \
  "v_xor_b32 v185, v185, v201\n\t" \
  "v_xor_b32 v186, v186, v202\n\t" \
  "v_xor_b32 v187, v187, v203\n\t" \
  "v_xor_b32 v188, v188, v204\n\t" \
  "v_xor_b32 v189, v189, v205\n\t" \
  "v_xor_b32 v190, v190, v206\n\t" \
  "v_xor_b32 v191, v191, v207\n\t" \
  "v_mfma_f32_32x32x16_bf16 v[168:183], v[220:223], v[204:207], v[168:183]\n\t" \
  "ds_read_b128 v[216:219], v36 offset:7168\n\t" \
  "ds_read_b128 v[220:223], v36 offset:7680\n\t" \
  "v_exp_f32 v224, v104\n\t" \
  "v_exp_f32 v225, v105\n\t" \
  "v_exp_f32 v226, v106\n\t" \
  "buffer_load_dwordx4 a[152:155], v32, s[4:7], s20 offen offset:2048 sc1\n\t" \
  "v_mfma_f32_32x32x16_bf16 v[120:135], v[88:91], v[0:3], v[152:167]\n\t" \
  "v_exp_f32 v227, v107\n\t" \
  "v_cvt_pk_bf16_f32 v192, v224, v225\n\t" \
  "v_cvt_pk_bf16_f32 v193, v226, v227\n\t" \
  "v_exp_f32 v228, v108\n\t" \
  "v_exp_f32 v229, v109\n\t" \
  "v_exp_f32 v230, v110\n\t" \
  "buffer_load_dwordx4 a[156:159], v32, s[4:7], s20 offen offset:3072 sc1\n\t" \
  "v_mfma_f32_32x32x16_bf16 v[168:183], v[208:211], v[184:187], v[168:183]\n\t" \
  "v_exp_f32 v231, v111\n\t" \
  "v_cvt_pk_bf16_f32 v194, v228, v229\n\t" \
  "v_cvt_pk_bf16_f32 v195, v230, v231\n\t" \
  "v_exp_f32 v224, v112\n\t" \
  "v_exp_f32 v225, v113\n\t" \
  "v_exp_f32 v226, v114\n\t" \
  "v_exp_f32 v227, v115\n\t" \
  "s_add_u32 s21, s37, 40960\n\t" \
  "buffer_load_dwordx4 a[160:163], v32, s[4:7], s21 offen offset:0 sc1\n\t" \
  "v_mfma_f32_32x32x16_bf16 v[168:183], v[212:215], v[188:191], v[168:183]\n\t" \
  "v_cvt_pk_bf16_f32 v196, v224, v225\n\t" \
  "v_cvt_pk_bf16_f32 v197, v226, v227\n\t" \
  "v_exp_f32 v228, v116\n\t" \
  "v_exp_f32 v229, v117\n\t" \
  "v_exp_f32 v230, v118\n\t" \
  "v_exp_f32 v231, v119\n\t" \
  "v_cvt_pk_bf16_f32 v198, v228, v229\n\t" \
  "v_cvt_pk_bf16_f32 v199, v230, v231\n\t" \
  "buffer_load_dwordx4 a[164:167], v32, s[4:7], s21 offen offset:1024 sc1\n\t" \
  "ds_read_b128 v[208:211], v36 offset:6144\n\t" \
  "ds_read_b128 v[212:215], v36 offset:6656\n\t" \
  "v_exp_f32 v224, v120\n\t" \
  "v_exp_f32 v225, v121\n\t" \
  "v_exp_f32 v226, v122\n\t" \
  "buffer_load_dwordx4 a[168:171], v32, s[4:7], s21 offen offset:2048 sc1\n\t" \
  "v_mfma_f32_32x32x16_bf16 v[104:119], v[84:87], v[0:3], v[152:167]\n\t" \
  "v_exp_f32 v227, v123\n\t" \
  "v_cvt_pk_bf16_f32 v184, v224, v225\n\t" \
  "v_cvt_pk_bf16_f32 v185, v226, v227\n\t" \
  "v_exp_f32 v228, v124\n\t" \
  "v_exp_f32 v229, v125\n\t" \
  "v_exp_f32 v230, v126\n\t" \
  "buffer_load_dwordx4 a[172:175], v32, s[4:7], s21 offen offset:3072 sc1\n\t" \
  "s_waitcnt lgkmcnt(2)\n\t" \
  "v_mfma_f32_32x32x16_bf16 v[168:183], v[216:219], v[192:195], v[168:183]\n\t" \
  "v_exp_f32 v231, v127\n\t" \
  "v_cvt_pk_bf16_f32 v186, v228, v229\n\t" \
  "v_cvt_pk_bf16_f32 v187, v230, v231\n\t" \
  "v_exp_f32 v224, v128\n\t" \
  "v_exp_f32 v225, v129\n\t" \
  "v_exp_f32 v226, v130\n\t" \
  "v_exp_f32 v227, v131\n\t" \
  "s_add_u32 s20, s37, 45056\n\t" \
  "buffer_load_dwordx4 a[176:179], v32, s[4:7], s20 offen offset:0 sc1\n\t" \
  "v_mfma_f32_32x32x16_bf16 v[168:183], v[220:223], v[196:199], v[168:183]\n\t" \
  "v_cvt_pk_bf16_f32 v188, v224, v225\n\t" \
  "v_cvt_pk_bf16_f32 v189, v226, v227\n\t" \
  "v_exp_f32 v228, v132\n\t" \
  "v_exp_f32 v229, v133\n\t" \
  "v_exp_f32 v230, v134\n\t" \
  "v_exp_f32 v231, v135\n\t" \
  "v_cvt_pk_bf16_f32 v190, v228, v229\n\t" \
  "v_cvt_pk_bf16_f32 v191, v230, v231\n\t" \
  "buffer_load_dwordx4 a[180:183], v32, s[4:7], s20 offen offset:1024 sc1\n\t" \
  "ds_read_b128 v[216:219], v36 offset:5120\n\t" \
  "ds_read_b128 v[220:223], v36 offset:5632\n\t" \
  "v_exp_f32 v224, v104\n\t" \
  "v_exp_f32 v225, v105\n\t" \
  "v_exp_f32 v226, v106\n\t" \
  "buffer_load_dwordx4 a[184:187], v32, s[4:7], s20 offen offset:2048 sc1\n\t" \
  "v_mfma_f32_32x32x16_bf16 v[120:135], v[80:83], v[0:3], v[152:167]\n\t" \
  "v_exp_f32 v227, v107\n\t" \
  "v_cvt_pk_bf16_f32 v192, v224, v225\n\t" \
  "v_cvt_pk_bf16_f32 v193, v226, v227\n\t" \
  "v_exp_f32 v228, v108\n\t" \
  "v_exp_f32 v229, v109\n\t" \
  "v_exp_f32 v230, v110\n\t" \
  "buffer_load_dwordx4 a[188:191], v32, s[4:7], s20 offen offset:3072 sc1\n\t" \
  "s_waitcnt lgkmcnt(2)\n\t" \
  "v_mfma_f32_32x32x16_bf16 v[168:183], v[208:211], v[184:187], v[168:183]\n\t" \
  "v_exp_f32 v231, v111\n\t" \
  "v_cvt_pk_bf16_f32 v194, v228, v229\n\t" \
  "v_cvt_pk_bf16_f32 v195, v230, v231\n\t" \
  "v_exp_f32 v224, v112\n\t" \
  "v_exp_f32 v225, v113\n\t" \
  "v_exp_f32 v226, v114\n\t" \
  "v_exp_f32 v227, v115\n\t" \
  "buffer_load_dwordx4 a[0:3], v32, s[4:7], s37 offen offset:0 sc1\n\t" \
  "v_mfma_f32_32x32x16_bf16 v[168:183], v[212:215], v[188:191], v[168:183]\n\t" \
  "v_cvt_pk_bf16_f32 v196, v224, v225\n\t" \
  "v_cvt_pk_bf16_f32 v197, v226, v227\n\t" \
  "v_exp_f32 v228, v116\n\t" \
  "v_exp_f32 v229, v117\n\t" \
  "v_exp_f32 v230, v118\n\t" \
  "v_exp_f32 v231, v119\n\t" \
  "v_cvt_pk_bf16_f32 v198, v228, v229\n\t" \
  "v_cvt_pk_bf16_f32 v199, v230, v231\n\t" \
  "buffer_load_dwordx4 a[4:7], v32, s[4:7], s37 offen offset:1024 sc1\n\t" \
  "s_waitcnt vmcnt(17)\n\t" \
  "s_add_u32 s94, s44, 262144\n\t" \
  "buffer_store_dwordx2 v[56:57], v55, s[4:7], s94 offen offset:0 sc1\n\t" \
  "ds_read_b128 v[208:211], v36 offset:4096\n\t" \
  "ds_read_b128 v[212:215], v36 offset:4608\n\t" \
  "v_exp_f32 v224, v120\n\t" \
  "v_exp_f32 v225, v121\n\t" \
  "v_exp_f32 v226, v122\n\t" \
  "buffer_load_dwordx4 a[8:11], v32, s[4:7], s37 offen offset:2048 sc1\n\t" \
  "v_mfma_f32_32x32x16_bf16 v[104:119], v[76:79], v[0:3], v[152:167]\n\t" \
  "v_exp_f32 v227, v123\n\t" \
  "v_cvt_pk_bf16_f32 v184, v224, v225\n\t" \
  "v_cvt_pk_bf16_f32 v185, v226, v227\n\t" \
  "v_exp_f32 v228, v124\n\t" \
  "v_exp_f32 v229, v125\n\t" \
  "v_exp_f32 v230, v126\n\t" \
  "buffer_load_dwordx4 a[12:15], v32, s[4:7], s37 offen offset:3072 sc1\n\t" \
  "s_waitcnt lgkmcnt(2)\n\t" \
  "v_mfma_f32_32x32x16_bf16 v[168:183], v[216:219], v[192:195], v[168:183]\n\t" \
  "v_exp_f32 v231, v127\n\t" \
  "v_cvt_pk_bf16_f32 v186, v228, v229\n\t" \
  "v_cvt_pk_bf16_f32 v187, v230, v231\n\t" \
  "v_exp_f32 v224, v128\n\t" \
  "v_exp_f32 v225, v129\n\t" \
  "v_exp_f32 v226, v130\n\t" \
  "v_exp_f32 v227, v131\n\t" \
  "s_add_u32 s20, s37, 4096\n\t" \
  "buffer_load_dwordx4 a[16:19], v32, s[4:7], s20 offen offset:0 sc1\n\t" \
  "v_mfma_f32_32x32x16_bf16 v[168:183], v[220:223], v[196:199], v[168:183]\n\t" \
  "v_cvt_pk_bf16_f32 v188, v224, v225\n\t" \
  "v_cvt_pk_bf16_f32 v189, v226, v227\n\t" \
  "v_exp_f32 v228, v132\n\t" \
  "v_exp_f32 v229, v133\n\t" \
  "v_exp_f32 v230, v134\n\t" \
  "v_exp_f32 v231, v135\n\t" \
  "v_cvt_pk_bf16_f32 v190, v228, v229\n\t" \
  "v_cvt_pk_bf16_f32 v191, v230, v231\n\t" \
  "buffer_load_dwordx4 a[20:23], v32, s[4:7], s20 offen offset:1024 sc1\n\t" \
  "ds_read_b128 v[216:219], v36 offset:3072\n\t" \
  "ds_read_b128 v[220:223], v36 offset:3584\n\t" \
  "v_exp_f32 v224, v104\n\t" \
  "v_exp_f32 v225, v105\n\t" \
  "v_exp_f32 v226, v106\n\t" \
  "buffer_load_dwordx4 a[24:27], v32, s[4:7], s20 offen offset:2048 sc1\n\t" \
  "v_mfma_f32_32x32x16_bf16 v[120:135], v[72:75], v[0:3], v[152:167]\n\t" \
  "v_exp_f32 v227, v107\n\t" \
  "v_cvt_pk_bf16_f32 v192, v224, v225\n\t" \
  "v_cvt_pk_bf16_f32 v193, v226, v227\n\t" \
  "v_exp_f32 v228, v108\n\t" \
  "v_exp_f32 v229, v109\n\t" \
  "v_exp_f32 v230, v110\n\t" \
  "s_waitcnt lgkmcnt(2)\n\t" \
  "v_mfma_f32_32x32x16_bf16 v[168:183], v[208:211], v[184:187], v[168:183]\n\t" \
  "v_exp_f32 v231, v111\n\t" \
  "v_cvt_pk_bf16_f32 v194, v228, v229\n\t" \
  "v_cvt_pk_bf16_f32 v195, v230, v231\n\t" \
  "v_exp_f32 v224, v112\n\t" \
  "v_exp_f32 v225, v113\n\t" \
  "v_exp_f32 v226, v114\n\t" \
  "v_exp_f32 v227, v115\n\t" \
  "v_mfma_f32_32x32x16_bf16 v[168:183], v[212:215], v[188:191], v[168:183]\n\t" \
  "v_cvt_pk_bf16_f32 v196, v224, v225\n\t" \
  "v_cvt_pk_bf16_f32 v197, v226, v227\n\t" \
  "v_exp_f32 v228, v116\n\t" \
  "v_exp_f32 v229, v117\n\t" \
  "v_exp_f32 v230, v118\n\t" \
  "v_exp_f32 v231, v119\n\t" \
  "v_cvt_pk_bf16_f32 v198, v228, v229\n\t" \
  "v_cvt_pk_bf16_f32 v199, v230, v231\n\t" \
  "ds_read_b128 v[208:211], v36 offset:2048\n\t" \
  "ds_read_b128 v[212:215], v36 offset:2560\n\t" \
  "v_exp_f32 v224, v120\n\t" \
  "v_exp_f32 v225, v121\n\t" \
  "v_exp_f32 v226, v122\n\t" \
  "v_mfma_f32_32x32x16_bf16 v[104:119], v[68:71], v[0:3], v[152:167]\n\t" \
  "v_exp_f32 v227, v123\n\t" \
  "v_cvt_pk_bf16_f32 v184, v224, v225\n\t" \
  "v_cvt_pk_bf16_f32 v185, v226, v227\n\t" \
  "v_exp_f32 v228, v124\n\t" \
  "v_exp_f32 v229, v125\n\t" \
  "v_exp_f32 v230, v126\n\t" \
  "s_waitcnt lgkmcnt(2)\n\t" \
  "v_mfma_f32_32x32x16_bf16 v[168:183], v[216:219], v[192:195], v[168:183]\n\t" \
  "v_exp_f32 v231, v127\n\t" \
  "v_cvt_pk_bf16_f32 v186, v228, v229\n\t" \
  "v_cvt_pk_bf16_f32 v187, v230, v231\n\t" \
  "v_exp_f32 v224, v128\n\t" \
  "v_exp_f32 v225, v129\n\t" \
  "v_exp_f32 v226, v130\n\t" \
  "v_exp_f32 v227, v131\n\t" \
  "v_mfma_f32_32x32x16_bf16 v[168:183], v[220:223], v[196:199], v[168:183]\n\t" \
  "v_cvt_pk_bf16_f32 v188, v224, v225\n\t" \
  "v_cvt_pk_bf16_f32 v189, v226, v227\n\t" \
  "v_exp_f32 v228, v132\n\t" \
  "v_exp_f32 v229, v133\n\t" \
  "v_exp_f32 v230, v134\n\t" \
  "v_exp_f32 v231, v135\n\t" \
  "v_cvt_pk_bf16_f32 v190, v228, v229\n\t" \
  "v_cvt_pk_bf16_f32 v191, v230, v231\n\t" \
  "ds_read_b128 v[216:219], v36 offset:1024\n\t" \
  "ds_read_b128 v[220:223], v36 offset:1536\n\t" \
  "v_exp_f32 v224, v104\n\t" \
  "v_exp_f32 v225, v105\n\t" \
  "v_exp_f32 v226, v106\n\t" \
  "v_exp_f32 v227, v107\n\t" \
  "v_cvt_pk_bf16_f32 v192, v224, v225\n\t" \
  "v_cvt_pk_bf16_f32 v193, v226, v227\n\t" \
  "v_exp_f32 v228, v108\n\t" \
  "v_exp_f32 v229, v109\n\t" \
  "v_exp_f32 v230, v110\n\t" \
  "s_waitcnt lgkmcnt(2)\n\t" \
  "v_mfma_f32_32x32x16_bf16 v[168:183], v[208:211], v[184:187], v[168:183]\n\t" \
  "v_exp_f32 v231, v111\n\t" \
  "v_cvt_pk_bf16_f32 v194, v228, v229\n\t" \
  "v_cvt_pk_bf16_f32 v195, v230, v231\n\t" \
  "v_exp_f32 v224, v112\n\t" \
  "v_exp_f32 v225, v113\n\t" \
  "v_exp_f32 v226, v114\n\t" \
  "v_exp_f32 v227, v115\n\t" \
  "v_mfma_f32_32x32x16_bf16 v[168:183], v[212:215], v[188:191], v[168:183]\n\t" \
  "v_cvt_pk_bf16_f32 v196, v224, v225\n\t" \
  "v_cvt_pk_bf16_f32 v197, v226, v227\n\t" \
  "v_exp_f32 v228, v116\n\t" \
  "v_exp_f32 v229, v117\n\t" \
  "v_exp_f32 v230, v118\n\t" \
  "v_exp_f32 v231, v119\n\t" \
  "v_cvt_pk_bf16_f32 v198, v228, v229\n\t" \
  "v_cvt_pk_bf16_f32 v199, v230, v231\n\t" \
  "s_waitcnt lgkmcnt(0)\n\t" \
  "s_nop 0\n\t" \
  "v_mfma_f32_32x32x16_bf16 v[168:183], v[216:219], v[192:195], v[168:183]\n\t" \
  "v_mfma_f32_32x32x16_bf16 v[168:183], v[220:223], v[196:199], v[168:183]\n\t" \
  "v_mfma_f32_32x32x16_bf16 v[104:119], v[100:103], v[4:7], 0\n\t" \
  "v_mfma_f32_32x32x16_bf16 v[120:135], v[68:71], v[4:7], 0\n\t" \
  "v_mfma_f32_32x32x16_bf16 v[152:167], v[96:99], v[4:7], 0\n\t" \
  "s_nop 8\n\t" \
  "v_rcp_f32 v233, v176\n\t" \
  "s_nop 0\n\t" \
  "v_mul_f32 v224, v168, v233\n\t" \
  "v_mul_f32 v225, v169, v233\n\t" \
  "v_mul_f32 v226, v170, v233\n\t" \
  "v_mul_f32 v227, v171, v233\n\t" \
  "v_mul_f32 v228, v172, v233\n\t" \
  "v_mul_f32 v229, v173, v233\n\t" \
  "v_mul_f32 v230, v174, v233\n\t" \
  "v_mul_f32 v231, v175, v233\n\t" \
  "v_cvt_pk_bf16_f32 v236, v224, v225\n\t" \
  "v_cvt_pk_bf16_f32 v237, v226, v227\n\t" \
  "v_cvt_pk_bf16_f32 v238, v228, v229\n\t" \
  "v_cvt_pk_bf16_f32 v239, v230, v231\n\t" \
  "buffer_store_dwordx4 v[236:239], v37, s[4:7], s38 offen offset:0 sc1\n\t" \
  "v_mov_b32 v240, -1\n\t" \
  "v_mov_b32 v241, -1\n\t" \
  "v_mov_b32 v242, -1\n\t" \
  "v_mov_b32 v243, -1\n\t" \
  "s_add_u32 s94, s38, 32768\n\t" \
  "buffer_store_dwordx4 v[240:243], v37, s[4:7], s94 offen offset:0 sc1\n\t" \
  "v_cndmask_b32 v136, v104, v120, s[56:57]\n\t" \
  "v_cndmask_b32 v137, v105, v121, s[58:59]\n\t" \
  "v_cndmask_b32 v138, v106, v122, s[60:61]\n\t" \
  "v_cndmask_b32 v139, v107, v123, s[62:63]\n\t" \
  "v_cndmask_b32 v140, v108, v124, s[64:65]\n\t" \
  "v_cndmask_b32 v141, v109, v125, s[66:67]\n\t" \
  "v_cndmask_b32 v142, v110, v126, s[68:69]\n\t" \
  "v_cndmask_b32 v143, v111, v127, s[70:71]\n\t" \
  "v_cndmask_b32 v144, v112, v128, s[72:73]\n\t" \
  "v_cndmask_b32 v145, v113, v129, s[74:75]\n\t" \
  "v_cndmask_b32 v146, v114, v130, s[76:77]\n\t" \
  "v_cndmask_b32 v147, v115, v131, s[78:79]\n\t" \
  "v_cndmask_b32 v148, v116, v132, s[80:81]\n\t" \
  "v_cndmask_b32 v149, v117, v133, s[82:83]\n\t" \
  "v_cndmask_b32 v150, v118, v134, s[84:85]\n\t" \
  "v_cndmask_b32 v151, v119, v135, s[86:87]\n\t" \
  "v_mfma_f32_32x32x16_bf16 v[104:119], v[92:95], v[4:7], 0\n\t" \
  "v_max3_f32 v232, v136, v137, v136\n\t" \
  "v_max3_f32 v233, v138, v139, v138\n\t" \
  "v_max3_f32 v232, v140, v141, v232\n\t" \
  "v_max3_f32 v233, v142, v143, v233\n\t" \
  "v_max3_f32 v232, v144, v145, v232\n\t" \
  "v_max3_f32 v233, v146, v147, v233\n\t" \
  "v_max3_f32 v232, v148, v149, v232\n\t" \
  "v_max3_f32 v233, v150, v151, v233\n\t" \
  "buffer_load_dwordx4 a[28:31], v32, s[4:7], s20 offen offset:3072 sc1\n\t" \
  "s_waitcnt lgkmcnt(0)\n\t" \
  "v_mfma_f32_32x32x16_bf16 v[120:135], v[88:91], v[4:7], 0\n\t" \
  "v_max3_f32 v232, v152, v153, v232\n\t" \
  "v_max3_f32 v233, v154, v155, v233\n\t" \
  "v_max3_f32 v232, v156, v157, v232\n\t" \
  "v_max3_f32 v233, v158, v159, v233\n\t" \
  "v_max3_f32 v232, v160, v161, v232\n\t" \
  "v_max3_f32 v233, v162, v163, v233\n\t" \
  "v_max3_f32 v232, v164, v165, v232\n\t" \
  "v_max3_f32 v233, v166, v167, v233\n\t" \
  "s_add_u32 s21, s37, 8192\n\t" \
  "buffer_load_dwordx4 a[32:35], v32, s[4:7], s21 offen offset:0 sc1\n\t" \
  "v_mfma_f32_32x32x16_bf16 v[152:167], v[84:87], v[4:7], 0\n\t" \
  "v_max3_f32 v232, v104, v105, v232\n\t" \
  "v_max3_f32 v233, v106, v107, v233\n\t" \
  "v_max3_f32 v232, v108, v109, v232\n\t" \
  "v_max3_f32 v233, v110, v111, v233\n\t" \
  "v_max3_f32 v232, v112, v113, v232\n\t" \
  "v_max3_f32 v233, v114, v115, v233\n\t" \
  "v_max3_f32 v232, v116, v117, v232\n\t" \
  "v_max3_f32 v233, v118, v119, v233\n\t" \
  "buffer_load_dwordx4 a[36:39], v32, s[4:7], s21 offen offset:1024 sc1\n\t" \
  "v_mfma_f32_32x32x16_bf16 v[104:119], v[80:83], v[4:7], 0\n\t" \
  "v_max3_f32 v232, v120, v121, v232\n\t" \
  "v_max3_f32 v233, v122, v123, v233\n\t" \
  "v_max3_f32 v232, v124, v125, v232\n\t" \
  "v_max3_f32 v233, v126, v127, v233\n\t" \
  "v_max3_f32 v232, v128, v129, v232\n\t" \
  "v_max3_f32 v233, v130, v131, v233\n\t" \
  "v_max3_f32 v232, v132, v133, v232\n\t" \
  "v_max3_f32 v233, v134, v135, v233\n\t" \
  "buffer_load_dwordx4 a[40:43], v32, s[4:7], s21 offen offset:2048 sc1\n\t" \
  "v_mfma_f32_32x32x16_bf16 v[120:135], v[76:79], v[4:7], 0\n\t" \
  "v_max3_f32 v232, v152, v153, v232\n\t" \
  "v_max3_f32 v233, v154, v155, v233\n\t" \
  "v_max3_f32 v232, v156, v157, v232\n\t" \
  "v_max3_f32 v233, v158, v159, v233\n\t" \
  "v_max3_f32 v232, v160, v161, v232\n\t" \
  "v_max3_f32 v233, v162, v163, v233\n\t" \
  "v_max3_f32 v232, v164, v165, v232\n\t" \
  "v_max3_f32 v233, v166, v167, v233\n\t" \
  "buffer_load_dwordx4 a[44:47], v32, s[4:7], s21 offen offset:3072 sc1\n\t" \
  "v_mfma_f32_32x32x16_bf16 v[152:167], v[72:75], v[4:7], 0\n\t" \
  "v_max3_f32 v232, v104, v105, v232\n\t" \
  "v_max3_f32 v233, v106, v107, v233\n\t" \
  "v_max3_f32 v232, v108, v109, v232\n\t" \
  "v_max3_f32 v233, v110, v111, v233\n\t" \
  "v_max3_f32 v232, v112, v113, v232\n\t" \
  "v_max3_f32 v233, v114, v115, v233\n\t" \
  "v_max3_f32 v232, v116, v117, v232\n\t" \
  "v_max3_f32 v233, v118, v119, v233\n\t" \
  "s_add_u32 s20, s37, 12288\n\t" \
  "buffer_load_dwordx4 a[48:51], v32, s[4:7], s20 offen offset:0 sc1\n\t" \
  "v_max3_f32 v232, v120, v121, v232\n\t" \
  "v_max3_f32 v233, v122, v123, v233\n\t" \
  "v_max3_f32 v232, v124, v125, v232\n\t" \
  "v_max3_f32 v233, v126, v127, v233\n\t" \
  "v_max3_f32 v232, v128, v129, v232\n\t" \
  "v_max3_f32 v233, v130, v131, v233\n\t" \
  "v_max3_f32 v232, v132, v133, v232\n\t" \
  "v_max3_f32 v233, v134, v135, v233\n\t" \
  "buffer_load_dwordx4 a[52:55], v32, s[4:7], s20 offen offset:1024 sc1\n\t" \
  "v_max3_f32 v232, v152, v153, v232\n\t" \
  "v_max3_f32 v233, v154, v155, v233\n\t" \
  "v_max3_f32 v232, v156, v157, v232\n\t" \
  "v_max3_f32 v233, v158, v159, v233\n\t" \
  "v_max3_f32 v232, v160, v161, v232\n\t" \
  "v_max3_f32 v233, v162, v163, v233\n\t" \
  "v_max3_f32 v232, v164, v165, v232\n\t" \
  "v_max3_f32 v233, v166, v167, v233\n\t" \
  "buffer_load_dwordx4 a[56:59], v32, s[4:7], s20 offen offset:2048 sc1\n\t" \
  "v_max_f32 v232, v232, v233\n\t" \
  "v_mov_b32 v233, v232\n\t" \
  "ds_read_b128 v[208:211], v36 offset:9216\n\t" \
  "ds_read_b128 v[212:215], v36 offset:9728\n\t" \
  "v_permlane32_swap_b32 v232, v233\n\t" \
  "ds_read_b128 v[216:219], v36 offset:1024\n\t" \
  "ds_read_b128 v[220:223], v36 offset:1536\n\t" \
  "v_max_f32 v232, v232, v233\n\t" \
  "buffer_load_dwordx4 a[60:63], v32, s[4:7], s20 offen offset:3072 sc1\n\t" \
  "v_xor_b32 v152, 0x80000000, v232\n\t" \
  "v_xor_b32 v153, 0x80000000, v232\n\t" \
  "v_xor_b32 v154, 0x80000000, v232\n\t" \
  "v_xor_b32 v155, 0x80000000, v232\n\t" \
  "v_xor_b32 v156, 0x80000000, v232\n\t" \
  "v_xor_b32 v157, 0x80000000, v232\n\t" \
  "v_xor_b32 v158, 0x80000000, v232\n\t" \
  "v_xor_b32 v159, 0x80000000, v232\n\t" \
  "v_xor_b32 v160, 0x80000000, v232\n\t" \
  "v_xor_b32 v161, 0x80000000, v232\n\t" \
  "v_xor_b32 v162, 0x80000000, v232\n\t" \
  "v_xor_b32 v163, 0x80000000, v232\n\t" \
  "v_xor_b32 v164, 0x80000000, v232\n\t" \
  "v_xor_b32 v165, 0x80000000, v232\n\t" \
  "v_xor_b32 v166, 0x80000000, v232\n\t" \
  "v_xor_b32 v167, 0x80000000, v232\n\t" \
  "s_add_u32 s21, s37, 16384\n\t" \
  "buffer_load_dwordx4 a[64:67], v32, s[4:7], s21 offen offset:0 sc1\n\t" \
  "v_sub_f32 v136, v136, v232\n\t" \
  "v_sub_f32 v137, v137, v232\n\t" \
  "v_mfma_f32_32x32x16_bf16 v[104:119], v[96:99], v[4:7], v[152:167]\n\t" \
  "v_sub_f32 v138, v138, v232\n\t" \
  "v_sub_f32 v139, v139, v232\n\t" \
  "v_sub_f32 v140, v140, v232\n\t" \
  "v_sub_f32 v141, v141, v232\n\t" \
  "v_sub_f32 v142, v142, v232\n\t" \
  "v_sub_f32 v143, v143, v232\n\t" \
  "v_sub_f32 v144, v144, v232\n\t" \
  "v_sub_f32 v145, v145, v232\n\t" \
  "v_sub_f32 v146, v146, v232\n\t" \
  "v_sub_f32 v147, v147, v232\n\t" \
  "v_sub_f32 v148, v148, v232\n\t" \
  "v_sub_f32 v149, v149, v232\n\t" \
  "v_sub_f32 v150, v150, v232\n\t" \
  "v_sub_f32 v151, v151, v232\n\t" \
  "s_waitcnt vmcnt(10)\n\t" \
  "buffer_store_dwordx2 v[56:57], v55, s[4:7], s39 offen offset:0 sc1\n\t" \
  "s_mov_b32 s91, 262144\n\t" \
  "buffer_load_dwordx2 v[30:31], v45, s[4:7], s91 offen offset:0 sc1\n\t" \
  "buffer_load_dwordx4 a[68:71], v32, s[4:7], s21 offen offset:1024 sc1\n\t" \
  "v_exp_f32 v224, v136\n\t" \
  "v_exp_f32 v225, v137\n\t" \
  "v_exp_f32 v226, v138\n\t" \
  "v_exp_f32 v227, v139\n\t" \
  "v_cvt_pk_bf16_f32 v184, v224, v225\n\t" \
  "v_cvt_pk_bf16_f32 v185, v226, v227\n\t" \
  "v_exp_f32 v228, v140\n\t" \
  "v_exp_f32 v229, v141\n\t" \
  "v_exp_f32 v230, v142\n\t" \
  "v_exp_f32 v231, v143\n\t" \
  "v_cvt_pk_bf16_f32 v186, v228, v229\n\t" \
  "v_cvt_pk_bf16_f32 v187, v230, v231\n\t" \
  "buffer_load_dwordx4 a[72:75], v32, s[4:7], s21 offen offset:2048 sc1\n\t" \
  "v_exp_f32 v224, v144\n\t" \
  "v_exp_f32 v225, v145\n\t" \
  "v_exp_f32 v226, v146\n\t" \
  "v_exp_f32 v227, v147\n\t" \
  "v_cvt_pk_bf16_f32 v188, v224, v225\n\t" \
  "v_cvt_pk_bf16_f32 v189, v226, v227\n\t" \
  "v_exp_f32 v228, v148\n\t" \
  "v_exp_f32 v229, v149\n\t" \
  "v_exp_f32 v230, v150\n\t" \
  "v_exp_f32 v231, v151\n\t" \
  "v_cvt_pk_bf16_f32 v190, v228, v229\n\t" \
  "v_cvt_pk_bf16_f32 v191, v230, v231\n\t" \
  "buffer_load_dwordx4 a[76:79], v32, s[4:7], s21 offen offset:3072 sc1\n\t" \
  "v_and_b32 v200, v184, v16\n\t" \
  "v_and_b32 v201, v185, v17\n\t" \
  "v_and_b32 v202, v186, v18\n\t" \
  "v_and_b32 v203, v187, v19\n\t" \
  "v_and_b32 v204, v188, v20\n\t" \
  "v_and_b32 v205, v189, v21\n\t" \
  "v_and_b32 v206, v190, v22\n\t" \
  "v_and_b32 v207, v191, v23\n\t" \
  "s_waitcnt lgkmcnt(0)\n\t" \
  "v_xor_b32 v184, v184, v200\n\t" \
  "v_mfma_f32_32x32x16_bf16 v[168:183], v[216:219], v[200:203], 0\n\t" \
  "v_xor_b32 v185, v185, v201\n\t" \
  "v_xor_b32 v186, v186, v202\n\t" \
  "v_xor_b32 v187, v187, v203\n\t" \
  "v_xor_b32 v188, v188, v204\n\t" \
  "v_xor_b32 v189, v189, v205\n\t" \
  "v_xor_b32 v190, v190, v206\n\t" \
  "v_xor_b32 v191, v191, v207\n\t" \
  "v_mfma_f32_32x32x16_bf16 v[168:183], v[220:223], v[204:207], v[168:183]\n\t" \
  "ds_read_b128 v[216:219], v36 offset:8192\n\t" \
  "ds_read_b128 v[220:223], v36 offset:8704\n\t" \
  "v_exp_f32 v224, v104\n\t" \
  "v_exp_f32 v225, v105\n\t" \
  "v_exp_f32 v226, v106\n\t" \
  "s_add_u32 s20, s37, 20480\n\t" \
  "buffer_load_dwordx4 a[80:83], v32, s[4:7], s20 offen offset:0 sc1\n\t" \
  "v_mfma_f32_32x32x16_bf16 v[120:135], v[92:95], v[4:7], v[152:167]\n\t" \
  "v_exp_f32 v227, v107\n\t" \
  "v_cvt_pk_bf16_f32 v192, v224, v225\n\t" \
  "v_cvt_pk_bf16_f32 v193, v226, v227\n\t" \
  "v_exp_f32 v228, v108\n\t" \
  "v_exp_f32 v229, v109\n\t" \
  "v_exp_f32 v230, v110\n\t" \
  "buffer_load_dwordx4 a[84:87], v32, s[4:7], s20 offen offset:1024 sc1\n\t" \
  "v_mfma_f32_32x32x16_bf16 v[168:183], v[208:211], v[184:187], v[168:183]\n\t" \
  "v_exp_f32 v231, v111\n\t" \
  "v_cvt_pk_bf16_f32 v194, v228, v229\n\t" \
  "v_cvt_pk_bf16_f32 v195, v230, v231\n\t" \
  "v_exp_f32 v224, v112\n\t" \
  "v_exp_f32 v225, v113\n\t" \
  "v_exp_f32 v226, v114\n\t" \
  "v_exp_f32 v227, v115\n\t" \
  "buffer_load_dwordx4 a[88:91], v32, s[4:7], s20 offen offset:2048 sc1\n\t" \
  "v_mfma_f32_32x32x16_bf16 v[168:183], v[212:215], v[188:191], v[168:183]\n\t" \
  "v_cvt_pk_bf16_f32 v196, v224, v225\n\t" \
  "v_cvt_pk_bf16_f32 v197, v226, v227\n\t" \
  "v_exp_f32 v228, v116\n\t" \
  "v_exp_f32 v229, v117\n\t" \
  "v_exp_f32 v230, v118\n\t" \
  "v_exp_f32 v231, v119\n\t" \
  "v_cvt_pk_bf16_f32 v198, v228, v229\n\t" \
  "v_cvt_pk_bf16_f32 v199, v230, v231\n\t" \
  "buffer_load_dwordx4 a[92:95], v32, s[4:7], s20 offen offset:3072 sc1\n\t" \
  "ds_read_b128 v[208:211], v36 offset:7168\n\t" \
  "ds_read_b128 v[212:215], v36 offset:7680\n\t" \
  "v_exp_f32 v224, v120\n\t" \
  "v_exp_f32 v225, v121\n\t" \
  "v_exp_f32 v226, v122\n\t" \
  "s_add_u32 s21, s37, 24576\n\t" \
  "buffer_load_dwordx4 a[96:99], v32, s[4:7], s21 offen offset:0 sc1\n\t" \
  "v_mfma_f32_32x32x16_bf16 v[104:119], v[88:91], v[4:7], v[152:167]\n\t" \
  "v_exp_f32 v227, v123\n\t" \
  "v_cvt_pk_bf16_f32 v184, v224, v225\n\t" \
  "v_cvt_pk_bf16_f32 v185, v226, v227\n\t" \
  "v_exp_f32 v228, v124\n\t" \
  "v_exp_f32 v229, v125\n\t" \
  "v_exp_f32 v230, v126\n\t" \
  "buffer_load_dwordx4 a[100:103], v32, s[4:7], s21 offen offset:1024 sc1\n\t" \
  "s_waitcnt lgkmcnt(2)\n\t" \
  "v_mfma_f32_32x32x16_bf16 v[168:183], v[216:219], v[192:195], v[168:183]\n\t" \
  "v_exp_f32 v231, v127\n\t" \
  "v_cvt_pk_bf16_f32 v186, v228, v229\n\t" \
  "v_cvt_pk_bf16_f32 v187, v230, v231\n\t" \
  "v_exp_f32 v224, v128\n\t" \
  "v_exp_f32 v225, v129\n\t" \
  "v_exp_f32 v226, v130\n\t" \
  "v_exp_f32 v227, v131\n\t" \
  "buffer_load_dwordx4 a[104:107], v32, s[4:7], s21 offen offset:2048 sc1\n\t" \
  "v_mfma_f32_32x32x16_bf16 v[168:183], v[220:223], v[196:199], v[168:183]\n\t" \
  "v_cvt_pk_bf16_f32 v188, v224, v225\n\t" \
  "v_cvt_pk_bf16_f32 v189, v226, v227\n\t" \
  "v_exp_f32 v228, v132\n\t" \
  "v_exp_f32 v229, v133\n\t" \
  "v_exp_f32 v230, v134\n\t" \
  "v_exp_f32 v231, v135\n\t" \
  "v_cvt_pk_bf16_f32 v190, v228, v229\n\t" \
  "v_cvt_pk_bf16_f32 v191, v230, v231\n\t" \
  "buffer_load_dwordx4 a[108:111], v32, s[4:7], s21 offen offset:3072 sc1\n\t" \
  "ds_read_b128 v[216:219], v36 offset:6144\n\t" \
  "ds_read_b128 v[220:223], v36 offset:6656\n\t" \
  "v_exp_f32 v224, v104\n\t" \
  "v_exp_f32 v225, v105\n\t" \
  "v_exp_f32 v226, v106\n\t" \
  "s_add_u32 s20, s37, 28672\n\t" \
  "buffer_load_dwordx4 a[112:115], v32, s[4:7], s20 offen offset:0 sc1\n\t" \
  "v_mfma_f32_32x32x16_bf16 v[120:135], v[84:87], v[4:7], v[152:167]\n\t" \
  "v_exp_f32 v227, v107\n\t" \
  "v_cvt_pk_bf16_f32 v192, v224, v225\n\t" \
  "v_cvt_pk_bf16_f32 v193, v226, v227\n\t" \
  "v_exp_f32 v228, v108\n\t" \
  "v_exp_f32 v229, v109\n\t" \
  "v_exp_f32 v230, v110\n\t" \
  "buffer_load_dwordx4 a[116:119], v32, s[4:7], s20 offen offset:1024 sc1\n\t" \
  "s_waitcnt lgkmcnt(2)\n\t" \
  "v_mfma_f32_32x32x16_bf16 v[168:183], v[208:211], v[184:187], v[168:183]\n\t" \
  "v_exp_f32 v231, v111\n\t" \
  "v_cvt_pk_bf16_f32 v194, v228, v229\n\t" \
  "v_cvt_pk_bf16_f32 v195, v230, v231\n\t" \
  "v_exp_f32 v224, v112\n\t" \
  "v_exp_f32 v225, v113\n\t" \
  "v_exp_f32 v226, v114\n\t" \
  "v_exp_f32 v227, v115\n\t" \
  "buffer_load_dwordx4 a[120:123], v32, s[4:7], s20 offen offset:2048 sc1\n\t" \
  "v_mfma_f32_32x32x16_bf16 v[168:183], v[212:215], v[188:191], v[168:183]\n\t" \
  "v_cvt_pk_bf16_f32 v196, v224, v225\n\t" \
  "v_cvt_pk_bf16_f32 v197, v226, v227\n\t" \
  "v_exp_f32 v228, v116\n\t" \
  "v_exp_f32 v229, v117\n\t" \
  "v_exp_f32 v230, v118\n\t" \
  "v_exp_f32 v231, v119\n\t" \
  "v_cvt_pk_bf16_f32 v198, v228, v229\n\t" \
  "v_cvt_pk_bf16_f32 v199, v230, v231\n\t" \
  "buffer_load_dwordx4 a[124:127], v32, s[4:7], s20 offen offset:3072 sc1\n\t" \
  "ds_read_b128 v[208:211], v36 offset:5120\n\t" \
  "ds_read_b128 v[212:215], v36 offset:5632\n\t" \
  "v_exp_f32 v224, v120\n\t" \
  "v_exp_f32 v225, v121\n\t" \
  "v_exp_f32 v226, v122\n\t" \
  "v_mfma_f32_32x32x16_bf16 v[104:119], v[80:83], v[4:7], v[152:167]\n\t" \
  "v_exp_f32 v227, v123\n\t" \
  "v_cvt_pk_bf16_f32 v184, v224, v225\n\t" \
  "v_cvt_pk_bf16_f32 v185, v226, v227\n\t" \
  "v_exp_f32 v228, v124\n\t" \
  "v_exp_f32 v229, v125\n\t" \
  "v_exp_f32 v230, v126\n\t" \
  "s_waitcnt lgkmcnt(2)\n\t" \
  "v_mfma_f32_32x32x16_bf16 v[168:183], v[216:219], v[192:195], v[168:183]\n\t" \
  "v_exp_f32 v231, v127\n\t" \
  "v_cvt_pk_bf16_f32 v186, v228, v229\n\t" \
  "v_cvt_pk_bf16_f32 v187, v230, v231\n\t" \
  "v_exp_f32 v224, v128\n\t" \
  "v_exp_f32 v225, v129\n\t" \
  "v_exp_f32 v226, v130\n\t" \
  "v_exp_f32 v227, v131\n\t" \
  "v_mfma_f32_32x32x16_bf16 v[168:183], v[220:223], v[196:199], v[168:183]\n\t" \
  "v_cvt_pk_bf16_f32 v188, v224, v225\n\t" \
  "v_cvt_pk_bf16_f32 v189, v226, v227\n\t" \
  "v_exp_f32 v228, v132\n\t" \
  "v_exp_f32 v229, v133\n\t" \
  "v_exp_f32 v230, v134\n\t" \
  "v_exp_f32 v231, v135\n\t" \
  "v_cvt_pk_bf16_f32 v190, v228, v229\n\t" \
  "v_cvt_pk_bf16_f32 v191, v230, v231\n\t" \
  "ds_read_b128 v[216:219], v36 offset:4096\n\t" \
  "ds_read_b128 v[220:223], v36 offset:4608\n\t" \
  "v_exp_f32 v224, v104\n\t" \
  "v_exp_f32 v225, v105\n\t" \
  "v_exp_f32 v226, v106\n\t" \
  "v_mfma_f32_32x32x16_bf16 v[120:135], v[76:79], v[4:7], v[152:167]\n\t" \
  "v_exp_f32 v227, v107\n\t" \
  "v_cvt_pk_bf16_f32 v192, v224, v225\n\t" \
  "v_cvt_pk_bf16_f32 v193, v226, v227\n\t" \
  "v_exp_f32 v228, v108\n\t" \
  "v_exp_f32 v229, v109\n\t" \
  "v_exp_f32 v230, v110\n\t" \
  "s_waitcnt lgkmcnt(2)\n\t" \
  "v_mfma_f32_32x32x16_bf16 v[168:183], v[208:211], v[184:187], v[168:183]\n\t" \
  "v_exp_f32 v231, v111\n\t" \
  "v_cvt_pk_bf16_f32 v194, v228, v229\n\t" \
  "v_cvt_pk_bf16_f32 v195, v230, v231\n\t" \
  "v_exp_f32 v224, v112\n\t" \
  "v_exp_f32 v225, v113\n\t" \
  "v_exp_f32 v226, v114\n\t" \
  "v_exp_f32 v227, v115\n\t" \
  "v_mfma_f32_32x32x16_bf16 v[168:183], v[212:215], v[188:191], v[168:183]\n\t" \
  "v_cvt_pk_bf16_f32 v196, v224, v225\n\t" \
  "v_cvt_pk_bf16_f32 v197, v226, v227\n\t" \
  "v_exp_f32 v228, v116\n\t" \
  "v_exp_f32 v229, v117\n\t" \
  "v_exp_f32 v230, v118\n\t" \
  "v_exp_f32 v231, v119\n\t" \
  "v_cvt_pk_bf16_f32 v198, v228, v229\n\t" \
  "v_cvt_pk_bf16_f32 v199, v230, v231\n\t" \
  "buffer_load_dwordx2 v[60:61], v38, s[4:7], 0 offen offset:0 sc1\n\t" \
  "ds_read_b128 v[208:211], v36 offset:3072\n\t" \
  "ds_read_b128 v[212:215], v36 offset:3584\n\t" \
  "v_exp_f32 v224, v120\n\t" \
  "v_exp_f32 v225, v121\n\t" \
  "v_exp_f32 v226, v122\n\t" \
  "v_mfma_f32_32x32x16_bf16 v[104:119], v[72:75], v[4:7], v[152:167]\n\t" \
  "v_exp_f32 v227, v123\n\t" \
  "v_cvt_pk_bf16_f32 v184, v224, v225\n\t" \
  "v_cvt_pk_bf16_f32 v185, v226, v227\n\t" \
  "v_exp_f32 v228, v124\n\t" \
  "v_exp_f32 v229, v125\n\t" \
  "v_exp_f32 v230, v126\n\t" \
  "s_waitcnt lgkmcnt(2)\n\t" \
  "v_mfma_f32_32x32x16_bf16 v[168:183], v[216:219], v[192:195], v[168:183]\n\t" \
  "v_exp_f32 v231, v127\n\t" \
  "v_cvt_pk_bf16_f32 v186, v228, v229\n\t" \
  "v_cvt_pk_bf16_f32 v187, v230, v231\n\t" \
  "v_exp_f32 v224, v128\n\t" \
  "v_exp_f32 v225, v129\n\t" \
  "v_exp_f32 v226, v130\n\t" \
  "v_exp_f32 v227, v131\n\t" \
  "v_mfma_f32_32x32x16_bf16 v[168:183], v[220:223], v[196:199], v[168:183]\n\t" \
  "v_cvt_pk_bf16_f32 v188, v224, v225\n\t" \
  "v_cvt_pk_bf16_f32 v189, v226, v227\n\t" \
  "v_exp_f32 v228, v132\n\t" \
  "v_exp_f32 v229, v133\n\t" \
  "v_exp_f32 v230, v134\n\t" \
  "v_exp_f32 v231, v135\n\t" \
  "v_cvt_pk_bf16_f32 v190, v228, v229\n\t" \
  "v_cvt_pk_bf16_f32 v191, v230, v231\n\t" \
  "s_waitcnt vmcnt(0)\n\t" \
  "s_movk_i32 s90, 0x7fff\n\t" \
  "Lp1_%=:\n\t" \
  "v_cmp_ne_u64 vcc, s[54:55], v[60:61]\n\t" \
  "s_and_b64 vcc, exec, vcc\n\t" \
  "s_cbranch_scc0 Lq1_%=\n\t" \
  "s_sub_u32 s90, s90, 1\n\t" \
  "s_cbranch_scc1 Lq1_%=\n\t" \
  "s_sleep 3\n\t" \
  "buffer_load_dwordx2 v[60:61], v38, s[4:7], 0 offen sc1\n\t" \
  "s_waitcnt vmcnt(0)\n\t" \
  "s_branch Lp1_%=\n\t" \
  "Lq1_%=:\n\t" \
  "ds_read_b128 v[216:219], v36 offset:2048\n\t" \
  "ds_read_b128 v[220:223], v36 offset:2560\n\t" \
  "v_exp_f32 v224, v104\n\t" \
  "v_exp_f32 v225, v105\n\t" \
  "v_exp_f32 v226, v106\n\t" \
  "buffer_load_dwordx4 v[240:243], v32, s[4:7], s41 offen offset:0 sc1\n\t" \
  "v_exp_f32 v227, v107\n\t" \
  "v_cvt_pk_bf16_f32 v192, v224, v225\n\t" \
  "v_cvt_pk_bf16_f32 v193, v226, v227\n\t" \
  "v_exp_f32 v228, v108\n\t" \
  "v_exp_f32 v229, v109\n\t" \
  "v_exp_f32 v230, v110\n\t" \
  "buffer_load_dwordx4 v[244:247], v32, s[4:7], s41 offen offset:1024 sc1\n\t" \
  "s_waitcnt lgkmcnt(2)\n\t" \
  "v_mfma_f32_32x32x16_bf16 v[168:183], v[208:211], v[184:187], v[168:183]\n\t" \
  "v_exp_f32 v231, v111\n\t" \
  "v_cvt_pk_bf16_f32 v194, v228, v229\n\t" \
  "v_cvt_pk_bf16_f32 v195, v230, v231\n\t" \
  "v_exp_f32 v224, v112\n\t" \
  "v_exp_f32 v225, v113\n\t" \
  "v_exp_f32 v226, v114\n\t" \
  "v_exp_f32 v227, v115\n\t" \
  "buffer_load_dwordx4 v[248:251], v32, s[4:7], s41 offen offset:2048 sc1\n\t" \
  "v_mfma_f32_32x32x16_bf16 v[168:183], v[212:215], v[188:191], v[168:183]\n\t" \
  "v_cvt_pk_bf16_f32 v196, v224, v225\n\t" \
  "v_cvt_pk_bf16_f32 v197, v226, v227\n\t" \
  "v_exp_f32 v228, v116\n\t" \
  "v_exp_f32 v229, v117\n\t" \
  "v_exp_f32 v230, v118\n\t" \
  "v_exp_f32 v231, v119\n\t" \
  "v_cvt_pk_bf16_f32 v198, v228, v229\n\t" \
  "v_cvt_pk_bf16_f32 v199, v230, v231\n\t" \
  "buffer_load_dwordx4 v[252:255], v32, s[4:7], s41 offen offset:3072 sc1\n\t" \
  "buffer_load_dwordx4 v[136:139], v32, s[4:7], s40 offen offset:0 sc1\n\t" \
  "buffer_load_dwordx4 v[140:143], v32, s[4:7], s40 offen offset:1024 sc1\n\t" \
  "buffer_load_dwordx4 v[144:147], v32, s[4:7], s40 offen offset:2048 sc1\n\t" \
  "buffer_load_dwordx4 v[148:151], v32, s[4:7], s40 offen offset:3072 sc1\n\t" \
  "s_waitcnt lgkmcnt(0)\n\t" \
  "s_nop 0\n\t" \
  "v_mfma_f32_32x32x16_bf16 v[168:183], v[216:219], v[192:195], v[168:183]\n\t" \
  "v_mfma_f32_32x32x16_bf16 v[168:183], v[220:223], v[196:199], v[168:183]\n\t" \
  "s_movk_i32 s90, 0x7fff\n\t" \
  "Lp2_%=:\n\t" \
  "v_cmp_ne_u64 vcc, s[54:55], v[30:31]\n\t" \
  "s_and_b64 vcc, exec, vcc\n\t" \
  "s_cbranch_scc0 Lq2_%=\n\t" \
  "s_sub_u32 s90, s90, 1\n\t" \
  "s_cbranch_scc1 Lq2_%=\n\t" \
  "s_sleep 3\n\t" \
  "buffer_load_dwordx2 v[30:31], v45, s[4:7], s91 offen sc1\n\t" \
  "s_waitcnt vmcnt(0)\n\t" \
  "s_branch Lp2_%=\n\t" \
  "Lq2_%=:\n\t" \
  "s_lshl_b32 s94, s24, 12\n\t" \
  "v_add_u32 v62, s94, v32\n\t" \
  "s_cmpk_lg_u32 s96, 768\n\t" \
  "s_cbranch_scc1 Le0_%=\n\t" \
  "s_movk_i32 s95, 0\n\t" \
  "buffer_load_dwordx2 v[200:201], v63, s[4:7], s95 offen sc1\n\t" \
  "s_movk_i32 s95, 8192\n\t" \
  "buffer_load_dwordx2 v[202:203], v63, s[4:7], s95 offen sc1\n\t" \
  "s_movk_i32 s95, 16384\n\t" \
  "buffer_load_dwordx2 v[204:205], v63, s[4:7], s95 offen sc1\n\t" \
  "s_movk_i32 s95, 24576\n\t" \
  "buffer_load_dwordx2 v[206:207], v63, s[4:7], s95 offen sc1\n\t" \
  "Le0_%=:\n\t" \
  "s_nop 5\n\t" \
  "v_rcp_f32 v233, v176\n\t" \
  "s_nop 0\n\t" \
  "v_mul_f32 v224, v168, v233\n\t" \
  "v_mul_f32 v225, v169, v233\n\t" \
  "v_mul_f32 v226, v170, v233\n\t" \
  "v_mul_f32 v227, v171, v233\n\t" \
  "v_mul_f32 v228, v172, v233\n\t" \
  "v_mul_f32 v229, v173, v233\n\t" \
  "v_mul_f32 v230, v174, v233\n\t" \
  "v_mul_f32 v231, v175, v233\n\t" \
  "v_cvt_pk_bf16_f32 v236, v224, v225\n\t" \
  "v_cvt_pk_bf16_f32 v237, v226, v227\n\t" \
  "v_cvt_pk_bf16_f32 v238, v228, v229\n\t" \
  "v_cvt_pk_bf16_f32 v239, v230, v231\n\t" \
  "s_add_u32 s88, s38, 32768\n\t" \
  "buffer_store_dwordx4 v[236:239], v37, s[4:7], s88 offen offset:0 sc1\n\t" \
  "s_waitcnt vmcnt(5)\n\t" \
  "v_mfma_f32_16x16x32_bf16 v[120:123], a[192:195], v[240:243], 0\n\t" \
  "v_mfma_f32_16x16x32_bf16 v[124:127], a[196:199], v[240:243], 0\n\t" \
  "v_mfma_f32_16x16x32_bf16 v[128:131], a[200:203], v[240:243], 0\n\t" \
  "v_mfma_f32_16x16x32_bf16 v[132:135], a[204:207], v[240:243], 0\n\t" \
  "v_mfma_f32_16x16x32_bf16 v[120:123], a[208:211], v[244:247], v[120:123]\n\t" \
  "v_mfma_f32_16x16x32_bf16 v[124:127], a[212:215], v[244:247], v[124:127]\n\t" \
  "v_mfma_f32_16x16x32_bf16 v[128:131], a[216:219], v[244:247], v[128:131]\n\t" \
  "v_mfma_f32_16x16x32_bf16 v[132:135], a[220:223], v[244:247], v[132:135]\n\t" \
  "v_mfma_f32_16x16x32_bf16 v[120:123], a[224:227], v[248:251], v[120:123]\n\t" \
  "v_mfma_f32_16x16x32_bf16 v[124:127], a[228:231], v[248:251], v[124:127]\n\t" \
  "v_mfma_f32_16x16x32_bf16 v[128:131], a[232:235], v[248:251], v[128:131]\n\t" \
  "v_mfma_f32_16x16x32_bf16 v[132:135], a[236:239], v[248:251], v[132:135]\n\t" \
  "v_mfma_f32_16x16x32_bf16 v[120:123], a[240:243], v[252:255], v[120:123]\n\t" \
  "v_mfma_f32_16x16x32_bf16 v[124:127], a[244:247], v[252:255], v[124:127]\n\t" \
  "v_mfma_f32_16x16x32_bf16 v[128:131], a[248:251], v[252:255], v[128:131]\n\t" \
  "v_mfma_f32_16x16x32_bf16 v[132:135], a[252:255], v[252:255], v[132:135]\n\t" \
  "s_waitcnt vmcnt(1)\n\t" \
  "v_mfma_f32_16x16x32_bf16 v[112:115], a[200:203], v[136:139], 0\n\t" \
  "v_mfma_f32_16x16x32_bf16 v[116:119], a[204:207], v[136:139], 0\n\t" \
  "v_mfma_f32_16x16x32_bf16 v[112:115], a[216:219], v[140:143], v[112:115]\n\t" \
  "v_mfma_f32_16x16x32_bf16 v[116:119], a[220:223], v[140:143], v[116:119]\n\t" \
  "v_mfma_f32_16x16x32_bf16 v[112:115], a[232:235], v[144:147], v[112:115]\n\t" \
  "v_mfma_f32_16x16x32_bf16 v[116:119], a[236:239], v[144:147], v[116:119]\n\t" \
  "ds_write_b128 v40, v[120:123] offset:49152\n\t" \
  "v_mfma_f32_16x16x32_bf16 v[112:115], a[248:251], v[148:151], v[112:115]\n\t" \
  "ds_write_b128 v40, v[124:127] offset:50176\n\t" \
  "v_mfma_f32_16x16x32_bf16 v[116:119], a[252:255], v[148:151], v[116:119]\n\t" \
  "ds_write_b128 v41, v[128:131] offset:49152\n\t" \
  "v_mfma_f32_16x16x32_bf16 v[104:107], a[192:195], v[136:139], 0\n\t" \
  "ds_write_b128 v41, v[132:135] offset:50176\n\t" \
  "v_mfma_f32_16x16x32_bf16 v[108:111], a[196:199], v[136:139], 0\n\t" \
  "v_mfma_f32_16x16x32_bf16 v[104:107], a[208:211], v[140:143], v[104:107]\n\t" \
  "v_mfma_f32_16x16x32_bf16 v[108:111], a[212:215], v[140:143], v[108:111]\n\t" \
  "v_mfma_f32_16x16x32_bf16 v[104:107], a[224:227], v[144:147], v[104:107]\n\t" \
  "v_mfma_f32_16x16x32_bf16 v[108:111], a[228:231], v[144:147], v[108:111]\n\t" \
  "v_mfma_f32_16x16x32_bf16 v[104:107], a[240:243], v[148:151], v[104:107]\n\t" \
  "ds_write_b128 v39, v[112:115] offset:49152\n\t" \
  "v_mfma_f32_16x16x32_bf16 v[108:111], a[244:247], v[148:151], v[108:111]\n\t" \
  "ds_write_b128 v39, v[116:119] offset:50176\n\t" \
  "s_waitcnt vmcnt(0)\n\t" \
  "s_add_u32 s88, s39, 131072\n\t" \
  "buffer_store_dwordx2 v[56:57], v55, s[4:7], s88 offen offset:0 sc1\n\t" \
  "s_cmpk_lg_u32 s96, 768\n\t" \
  "s_cbranch_scc1 Le9_%=\n\t" \
  "s_movk_i32 s90, 0x7fff\n\t" \
  "s_movk_i32 s95, 0\n\t" \
  "Le10_%=:\n\t" \
  "v_cmp_ne_u64 vcc, s[54:55], v[200:201]\n\t" \
  "s_and_b64 vcc, exec, vcc\n\t" \
  "s_cbranch_scc0 Le20_%=\n\t" \
  "s_sub_u32 s90, s90, 1\n\t" \
  "s_cbranch_scc1 Le9_%=\n\t" \
  "buffer_load_dwordx2 v[200:201], v63, s[4:7], s95 offen sc1\n\t" \
  "s_waitcnt vmcnt(0)\n\t" \
  "s_branch Le10_%=\n\t" \
  "Le20_%=:\n\t" \
  "s_movk_i32 s95, 8192\n\t" \
  "Le11_%=:\n\t" \
  "v_cmp_ne_u64 vcc, s[54:55], v[202:203]\n\t" \
  "s_and_b64 vcc, exec, vcc\n\t" \
  "s_cbranch_scc0 Le21_%=\n\t" \
  "s_sub_u32 s90, s90, 1\n\t" \
  "s_cbranch_scc1 Le9_%=\n\t" \
  "buffer_load_dwordx2 v[202:203], v63, s[4:7], s95 offen sc1\n\t" \
  "s_waitcnt vmcnt(0)\n\t" \
  "s_branch Le11_%=\n\t" \
  "Le21_%=:\n\t" \
  "s_movk_i32 s95, 16384\n\t" \
  "Le12_%=:\n\t" \
  "v_cmp_ne_u64 vcc, s[54:55], v[204:205]\n\t" \
  "s_and_b64 vcc, exec, vcc\n\t" \
  "s_cbranch_scc0 Le22_%=\n\t" \
  "s_sub_u32 s90, s90, 1\n\t" \
  "s_cbranch_scc1 Le9_%=\n\t" \
  "buffer_load_dwordx2 v[204:205], v63, s[4:7], s95 offen sc1\n\t" \
  "s_waitcnt vmcnt(0)\n\t" \
  "s_branch Le12_%=\n\t" \
  "Le22_%=:\n\t" \
  "s_movk_i32 s95, 24576\n\t" \
  "Le13_%=:\n\t" \
  "v_cmp_ne_u64 vcc, s[54:55], v[206:207]\n\t" \
  "s_and_b64 vcc, exec, vcc\n\t" \
  "s_cbranch_scc0 Le23_%=\n\t" \
  "s_sub_u32 s90, s90, 1\n\t" \
  "s_cbranch_scc1 Le9_%=\n\t" \
  "buffer_load_dwordx2 v[206:207], v63, s[4:7], s95 offen sc1\n\t" \
  "s_waitcnt vmcnt(0)\n\t" \
  "s_branch Le13_%=\n\t" \
  "Le23_%=:\n\t" \
  "buffer_store_dwordx2 v[56:57], v55, s[4:7], 0 offen sc1\n\t" \
  "Le9_%=:\n\t" \
  "s_waitcnt lgkmcnt(0)\n\t" \
  "s_barrier\n\t" \
  "ds_read_b128 v[136:139], v42 offset:49152\n\t" \
  "ds_read_b128 v[140:143], v42 offset:50176\n\t" \
  "ds_read_b128 v[144:147], v42 offset:51200\n\t" \
  "ds_read_b128 v[148:151], v42 offset:52224\n\t" \
  "ds_read_b128 v[152:155], v42 offset:53248\n\t" \
  "ds_read_b128 v[156:159], v42 offset:54272\n\t" \
  "v_lshlrev_b32 v160, 16, v8\n\t" \
  "v_and_b32 v161, 0xffff0000, v8\n\t" \
  "v_lshlrev_b32 v162, 16, v9\n\t" \
  "v_and_b32 v163, 0xffff0000, v9\n\t" \
  "v_lshlrev_b32 v164, 16, v10\n\t" \
  "v_and_b32 v165, 0xffff0000, v10\n\t" \
  "v_lshlrev_b32 v166, 16, v11\n\t" \
  "v_and_b32 v167, 0xffff0000, v11\n\t" \
  "v_add_f32 v104, v104, v160\n\t" \
  "v_add_f32 v105, v105, v161\n\t" \
  "v_add_f32 v106, v106, v162\n\t" \
  "v_add_f32 v107, v107, v163\n\t" \
  "v_add_f32 v108, v108, v164\n\t" \
  "v_add_f32 v109, v109, v165\n\t" \
  "v_add_f32 v110, v110, v166\n\t" \
  "v_add_f32 v111, v111, v167\n\t" \
  "s_waitcnt lgkmcnt(4)\n\t" \
  "v_add_f32 v104, v104, v136\n\t" \
  "v_add_f32 v105, v105, v137\n\t" \
  "v_add_f32 v106, v106, v138\n\t" \
  "v_add_f32 v107, v107, v139\n\t" \
  "v_add_f32 v108, v108, v140\n\t" \
  "v_add_f32 v109, v109, v141\n\t" \
  "v_add_f32 v110, v110, v142\n\t" \
  "v_add_f32 v111, v111, v143\n\t" \
  "s_waitcnt lgkmcnt(2)\n\t" \
  "v_add_f32 v104, v104, v144\n\t" \
  "v_add_f32 v105, v105, v145\n\t" \
  "v_add_f32 v106, v106, v146\n\t" \
  "v_add_f32 v107, v107, v147\n\t" \
  "v_add_f32 v108, v108, v148\n\t" \
  "v_add_f32 v109, v109, v149\n\t" \
  "v_add_f32 v110, v110, v150\n\t" \
  "v_add_f32 v111, v111, v151\n\t" \
  "s_waitcnt lgkmcnt(0)\n\t" \
  "v_add_f32 v104, v104, v152\n\t" \
  "v_add_f32 v105, v105, v153\n\t" \
  "v_add_f32 v106, v106, v154\n\t" \
  "v_add_f32 v107, v107, v155\n\t" \
  "v_add_f32 v108, v108, v156\n\t" \
  "v_add_f32 v109, v109, v157\n\t" \
  "v_add_f32 v110, v110, v158\n\t" \
  "v_add_f32 v111, v111, v159\n\t" \
  "v_cvt_pk_bf16_f32 v24, v104, v105\n\t" \
  "v_cvt_pk_bf16_f32 v25, v106, v107\n\t" \
  "v_cvt_pk_bf16_f32 v26, v108, v109\n\t" \
  "v_cvt_pk_bf16_f32 v27, v110, v111\n\t" \
  "buffer_store_dwordx4 v[24:27], v32, s[4:7], s42 offen offset:0 sc1\n\t" \
  "v_dot2_f32_bf16 v136, v24, s53, 0\n\t" \
  "v_dot2_f32_bf16 v137, v24, v24, 0\n\t" \
  "v_dot2_f32_bf16 v136, v25, s53, v136\n\t" \
  "v_dot2_f32_bf16 v137, v25, v25, v137\n\t" \
  "v_dot2_f32_bf16 v136, v26, s53, v136\n\t" \
  "v_dot2_f32_bf16 v137, v26, v26, v137\n\t" \
  "v_dot2_f32_bf16 v136, v27, s53, v136\n\t" \
  "v_dot2_f32_bf16 v137, v27, v27, v137\n\t" \
  "s_nop 0\n\t" \
  "s_nop 0\n\t" \
  "v_mov_b32 v138, v136\n\t" \
  "v_mov_b32 v139, v137\n\t" \
  "s_nop 0\n\t" \
  "v_permlane16_swap_b32 v136, v138\n\t" \
  "v_permlane16_swap_b32 v137, v139\n\t" \
  "v_add_f32 v136, v136, v138\n\t" \
  "v_add_f32 v137, v137, v139\n\t" \
  "v_mov_b32 v138, v136\n\t" \
  "v_mov_b32 v139, v137\n\t" \
  "s_nop 0\n\t" \
  "v_permlane32_swap_b32 v136, v138\n\t" \
  "v_permlane32_swap_b32 v137, v139\n\t" \
  "v_add_f32 v136, v136, v138\n\t" \
  "v_add_f32 v137, v137, v139\n\t" \
  "buffer_store_dwordx2 v[136:137], v44, s[4:7], s43 offen offset:0 sc1\n\t" \
  "s_add_u32 s88, s41, 32768\n\t" \
  "buffer_load_dwordx4 v[64:67], v32, s[4:7], s88 offen offset:0 sc1\n\t" \
  "buffer_load_dwordx4 v[68:71], v32, s[4:7], s88 offen offset:1024 sc1\n\t" \
  "buffer_load_dwordx4 v[72:75], v32, s[4:7], s88 offen offset:2048 sc1\n\t" \
  "buffer_load_dwordx4 v[76:79], v32, s[4:7], s88 offen offset:3072 sc1\n\t" \
  "s_add_u32 s89, s40, 32768\n\t" \
  "buffer_load_dwordx4 v[80:83], v32, s[4:7], s89 offen offset:0 sc1\n\t" \
  "buffer_load_dwordx4 v[84:87], v32, s[4:7], s89 offen offset:1024 sc1\n\t" \
  "buffer_load_dwordx4 v[88:91], v32, s[4:7], s89 offen offset:2048 sc1\n\t" \
  "buffer_load_dwordx4 v[92:95], v32, s[4:7], s89 offen offset:3072 sc1\n\t" \
  "v_mov_b32 v180, s53\n\t" \
  "v_mov_b32 v181, s53\n\t" \
  "v_mov_b32 v182, s53\n\t" \
  "v_mov_b32 v183, s53\n\t" \
  "s_nop 1\n\t" \
  "v_mfma_f32_16x16x32_bf16 v[168:171], a[0:3], v[180:183], 0\n\t" \
  "s_add_u32 s89, s50, 24576\n\t" \
  "buffer_load_dwordx4 v[192:195], v33, s[4:7], s89 offen offset:0 sc1\n\t" \
  "v_mfma_f32_16x16x32_bf16 v[172:175], a[64:67], v[180:183], 0\n\t" \
  "v_mfma_f32_16x16x32_bf16 v[176:179], a[128:131], v[180:183], 0\n\t" \
  "v_mfma_f32_16x16x32_bf16 v[168:171], a[4:7], v[180:183], v[168:171]\n\t" \
  "buffer_load_dwordx4 v[196:199], v33, s[4:7], s89 offen offset:1024 sc1\n\t" \
  "v_mfma_f32_16x16x32_bf16 v[172:175], a[68:71], v[180:183], v[172:175]\n\t" \
  "v_mfma_f32_16x16x32_bf16 v[176:179], a[132:135], v[180:183], v[176:179]\n\t" \
  "v_mfma_f32_16x16x32_bf16 v[168:171], a[8:11], v[180:183], v[168:171]\n\t" \
  "buffer_load_dwordx4 v[200:203], v34, s[4:7], s89 offen offset:0 sc1\n\t" \
  "v_mfma_f32_16x16x32_bf16 v[172:175], a[72:75], v[180:183], v[172:175]\n\t" \
  "v_mfma_f32_16x16x32_bf16 v[176:179], a[136:139], v[180:183], v[176:179]\n\t" \
  "v_mfma_f32_16x16x32_bf16 v[168:171], a[12:15], v[180:183], v[168:171]\n\t" \
  "buffer_load_dwordx4 v[204:207], v34, s[4:7], s89 offen offset:1024 sc1\n\t" \
  "v_mfma_f32_16x16x32_bf16 v[172:175], a[76:79], v[180:183], v[172:175]\n\t" \
  "s_add_u32 s89, s50, 28672\n\t" \
  "v_mfma_f32_16x16x32_bf16 v[176:179], a[140:143], v[180:183], v[176:179]\n\t" \
  "v_mfma_f32_16x16x32_bf16 v[168:171], a[16:19], v[180:183], v[168:171]\n\t" \
  "buffer_load_dwordx4 v[208:211], v33, s[4:7], s89 offen offset:0 sc1\n\t" \
  "v_mfma_f32_16x16x32_bf16 v[172:175], a[80:83], v[180:183], v[172:175]\n\t" \
  "v_mfma_f32_16x16x32_bf16 v[176:179], a[144:147], v[180:183], v[176:179]\n\t" \
  "v_mfma_f32_16x16x32_bf16 v[168:171], a[20:23], v[180:183], v[168:171]\n\t" \
  "buffer_load_dwordx4 v[212:215], v33, s[4:7], s89 offen offset:1024 sc1\n\t" \
  "v_mfma_f32_16x16x32_bf16 v[172:175], a[84:87], v[180:183], v[172:175]\n\t" \
  "v_mfma_f32_16x16x32_bf16 v[176:179], a[148:151], v[180:183], v[176:179]\n\t" \
  "v_mfma_f32_16x16x32_bf16 v[168:171], a[24:27], v[180:183], v[168:171]\n\t" \
  "buffer_load_dwordx4 v[216:219], v34, s[4:7], s89 offen offset:0 sc1\n\t" \
  "v_mfma_f32_16x16x32_bf16 v[172:175], a[88:91], v[180:183], v[172:175]\n\t" \
  "v_mfma_f32_16x16x32_bf16 v[176:179], a[152:155], v[180:183], v[176:179]\n\t" \
  "v_mfma_f32_16x16x32_bf16 v[168:171], a[28:31], v[180:183], v[168:171]\n\t" \
  "buffer_load_dwordx4 v[220:223], v34, s[4:7], s89 offen offset:1024 sc1\n\t" \
  "v_mfma_f32_16x16x32_bf16 v[172:175], a[92:95], v[180:183], v[172:175]\n\t" \
  "v_mfma_f32_16x16x32_bf16 v[176:179], a[156:159], v[180:183], v[176:179]\n\t" \
  "v_mfma_f32_16x16x32_bf16 v[168:171], a[32:35], v[180:183], v[168:171]\n\t" \
  "v_mfma_f32_16x16x32_bf16 v[172:175], a[96:99], v[180:183], v[172:175]\n\t" \
  "v_mfma_f32_16x16x32_bf16 v[176:179], a[160:163], v[180:183], v[176:179]\n\t" \
  "v_mfma_f32_16x16x32_bf16 v[168:171], a[36:39], v[180:183], v[168:171]\n\t" \
  "v_mfma_f32_16x16x32_bf16 v[172:175], a[100:103], v[180:183], v[172:175]\n\t" \
  "v_mfma_f32_16x16x32_bf16 v[176:179], a[164:167], v[180:183], v[176:179]\n\t" \
  "v_mfma_f32_16x16x32_bf16 v[168:171], a[40:43], v[180:183], v[168:171]\n\t" \
  "v_mfma_f32_16x16x32_bf16 v[172:175], a[104:107], v[180:183], v[172:175]\n\t" \
  "v_mfma_f32_16x16x32_bf16 v[176:179], a[168:171], v[180:183], v[176:179]\n\t" \
  "v_mfma_f32_16x16x32_bf16 v[168:171], a[44:47], v[180:183], v[168:171]\n\t" \
  "v_mfma_f32_16x16x32_bf16 v[172:175], a[108:111], v[180:183], v[172:175]\n\t" \
  "v_mfma_f32_16x16x32_bf16 v[176:179], a[172:175], v[180:183], v[176:179]\n\t" \
  "v_mfma_f32_16x16x32_bf16 v[168:171], a[48:51], v[180:183], v[168:171]\n\t" \
  "v_mfma_f32_16x16x32_bf16 v[172:175], a[112:115], v[180:183], v[172:175]\n\t" \
  "v_mfma_f32_16x16x32_bf16 v[176:179], a[176:179], v[180:183], v[176:179]\n\t" \
  "v_mfma_f32_16x16x32_bf16 v[168:171], a[52:55], v[180:183], v[168:171]\n\t" \
  "v_mfma_f32_16x16x32_bf16 v[172:175], a[116:119], v[180:183], v[172:175]\n\t" \
  "v_mfma_f32_16x16x32_bf16 v[176:179], a[180:183], v[180:183], v[176:179]\n\t" \
  "v_mfma_f32_16x16x32_bf16 v[168:171], a[56:59], v[180:183], v[168:171]\n\t" \
  "v_mfma_f32_16x16x32_bf16 v[172:175], a[120:123], v[180:183], v[172:175]\n\t" \
  "v_mfma_f32_16x16x32_bf16 v[176:179], a[184:187], v[180:183], v[176:179]\n\t" \
  "v_mfma_f32_16x16x32_bf16 v[168:171], a[60:63], v[180:183], v[168:171]\n\t" \
  "v_mfma_f32_16x16x32_bf16 v[172:175], a[124:127], v[180:183], v[172:175]\n\t" \
  "v_mfma_f32_16x16x32_bf16 v[176:179], a[188:191], v[180:183], v[176:179]\n\t" \
  "s_waitcnt vmcnt(16)\n\t" \
  "buffer_store_dwordx2 v[56:57], v55, s[4:7], s44 offen offset:0 sc1\n\t" \
  "s_waitcnt vmcnt(13)\n\t" \
  "v_mfma_f32_16x16x32_bf16 v[120:123], a[192:195], v[64:67], 0\n\t" \
  "v_mfma_f32_16x16x32_bf16 v[124:127], a[196:199], v[64:67], 0\n\t" \
  "v_mfma_f32_16x16x32_bf16 v[128:131], a[200:203], v[64:67], 0\n\t" \
  "v_mfma_f32_16x16x32_bf16 v[132:135], a[204:207], v[64:67], 0\n\t" \
  "v_mfma_f32_16x16x32_bf16 v[120:123], a[208:211], v[68:71], v[120:123]\n\t" \
  "v_mfma_f32_16x16x32_bf16 v[124:127], a[212:215], v[68:71], v[124:127]\n\t" \
  "v_mfma_f32_16x16x32_bf16 v[128:131], a[216:219], v[68:71], v[128:131]\n\t" \
  "v_mfma_f32_16x16x32_bf16 v[132:135], a[220:223], v[68:71], v[132:135]\n\t" \
  "v_mfma_f32_16x16x32_bf16 v[120:123], a[224:227], v[72:75], v[120:123]\n\t" \
  "v_mfma_f32_16x16x32_bf16 v[124:127], a[228:231], v[72:75], v[124:127]\n\t" \
  "v_mfma_f32_16x16x32_bf16 v[128:131], a[232:235], v[72:75], v[128:131]\n\t" \
  "v_mfma_f32_16x16x32_bf16 v[132:135], a[236:239], v[72:75], v[132:135]\n\t" \
  "v_mfma_f32_16x16x32_bf16 v[120:123], a[240:243], v[76:79], v[120:123]\n\t" \
  "v_mfma_f32_16x16x32_bf16 v[124:127], a[244:247], v[76:79], v[124:127]\n\t" \
  "v_mfma_f32_16x16x32_bf16 v[128:131], a[248:251], v[76:79], v[128:131]\n\t" \
  "v_mfma_f32_16x16x32_bf16 v[132:135], a[252:255], v[76:79], v[132:135]\n\t" \
  "s_waitcnt vmcnt(9)\n\t" \
  "v_mfma_f32_16x16x32_bf16 v[112:115], a[200:203], v[80:83], 0\n\t" \
  "v_mfma_f32_16x16x32_bf16 v[116:119], a[204:207], v[80:83], 0\n\t" \
  "v_mfma_f32_16x16x32_bf16 v[112:115], a[216:219], v[84:87], v[112:115]\n\t" \
  "v_mfma_f32_16x16x32_bf16 v[116:119], a[220:223], v[84:87], v[116:119]\n\t" \
  "v_mfma_f32_16x16x32_bf16 v[112:115], a[232:235], v[88:91], v[112:115]\n\t" \
  "v_mfma_f32_16x16x32_bf16 v[116:119], a[236:239], v[88:91], v[116:119]\n\t" \
  "ds_write_b128 v40, v[120:123] offset:0\n\t" \
  "v_mfma_f32_16x16x32_bf16 v[112:115], a[248:251], v[92:95], v[112:115]\n\t" \
  "ds_write_b128 v40, v[124:127] offset:1024\n\t" \
  "v_mfma_f32_16x16x32_bf16 v[116:119], a[252:255], v[92:95], v[116:119]\n\t" \
  "ds_write_b128 v41, v[128:131] offset:0\n\t" \
  "v_mfma_f32_16x16x32_bf16 v[104:107], a[192:195], v[80:83], 0\n\t" \
  "ds_write_b128 v41, v[132:135] offset:1024\n\t" \
  "v_mfma_f32_16x16x32_bf16 v[108:111], a[196:199], v[80:83], 0\n\t" \
  "v_mfma_f32_16x16x32_bf16 v[104:107], a[208:211], v[84:87], v[104:107]\n\t" \
  "v_mfma_f32_16x16x32_bf16 v[108:111], a[212:215], v[84:87], v[108:111]\n\t" \
  "v_mfma_f32_16x16x32_bf16 v[104:107], a[224:227], v[88:91], v[104:107]\n\t" \
  "v_mfma_f32_16x16x32_bf16 v[108:111], a[228:231], v[88:91], v[108:111]\n\t" \
  "v_mfma_f32_16x16x32_bf16 v[104:107], a[240:243], v[92:95], v[104:107]\n\t" \
  "ds_write_b128 v39, v[112:115] offset:0\n\t" \
  "v_mfma_f32_16x16x32_bf16 v[108:111], a[244:247], v[92:95], v[108:111]\n\t" \
  "ds_write_b128 v39, v[116:119] offset:1024\n\t" \
  "v_cmp_u_f32 vcc, v132, v112\n\t" \
  "s_and_b64 vcc, exec, vcc\n\t" \
  "s_cbranch_scc1 Lroa_%=\n\t" \
  "Lrob_%=:\n\t" \
  "s_add_u32 m0, s46, 0\n\t" \
  "s_add_u32 s89, s45, 0\n\t" \
  "buffer_load_dwordx4 v32, s[4:7], s89 offen offset:0 sc1 lds\n\t" \
  "buffer_load_dwordx4 v32, s[4:7], s89 offen offset:1024 sc1 lds\n\t" \
  "buffer_load_dwordx4 v32, s[4:7], s89 offen offset:2048 sc1 lds\n\t" \
  "buffer_load_dwordx4 v32, s[4:7], s89 offen offset:3072 sc1 lds\n\t" \
  "s_waitcnt lgkmcnt(0)\n\t" \
  "s_barrier\n\t" \
  "buffer_load_dwordx2 v[184:185], v46, s[4:7], s47 offen offset:0 sc1\n\t" \
  "ds_read_b128 v[136:139], v42 offset:0\n\t" \
  "ds_read_b128 v[140:143], v42 offset:1024\n\t" \
  "ds_read_b128 v[144:147], v42 offset:2048\n\t" \
  "ds_read_b128 v[148:151], v42 offset:3072\n\t" \
  "ds_read_b128 v[152:155], v42 offset:4096\n\t" \
  "buffer_load_dwordx2 v[186:187], v46, s[4:7], s47 offen offset:128 sc1\n\t" \
  "ds_read_b128 v[156:159], v42 offset:5120\n\t" \
  "v_lshlrev_b32 v160, 16, v12\n\t" \
  "v_and_b32 v161, 0xffff0000, v12\n\t" \
  "v_lshlrev_b32 v162, 16, v13\n\t" \
  "v_and_b32 v163, 0xffff0000, v13\n\t" \
  "buffer_load_dwordx2 v[188:189], v46, s[4:7], s47 offen offset:256 sc1\n\t" \
  "v_lshlrev_b32 v164, 16, v14\n\t" \
  "v_and_b32 v165, 0xffff0000, v14\n\t" \
  "v_lshlrev_b32 v166, 16, v15\n\t" \
  "v_and_b32 v167, 0xffff0000, v15\n\t" \
  "v_add_f32 v104, v104, v160\n\t" \
  "buffer_load_dwordx2 v[190:191], v46, s[4:7], s47 offen offset:384 sc1\n\t" \
  "v_add_f32 v105, v105, v161\n\t" \
  "v_add_f32 v106, v106, v162\n\t" \
  "v_add_f32 v107, v107, v163\n\t" \
  "v_add_f32 v108, v108, v164\n\t" \
  "v_add_f32 v109, v109, v165\n\t" \
  "s_add_u32 m0, s46, 16384\n\t" \
  "s_add_u32 s89, s45, 65536\n\t" \
  "buffer_load_dwordx4 v32, s[4:7], s89 offen offset:0 sc1 lds\n\t" \
  "v_add_f32 v110, v110, v166\n\t" \
  "v_add_f32 v111, v111, v167\n\t" \
  "s_waitcnt lgkmcnt(4)\n\t" \
  "v_add_f32 v104, v104, v136\n\t" \
  "v_add_f32 v105, v105, v137\n\t" \
  "buffer_load_dwordx4 v32, s[4:7], s89 offen offset:1024 sc1 lds\n\t" \
  "v_add_f32 v106, v106, v138\n\t" \
  "v_add_f32 v107, v107, v139\n\t" \
  "v_add_f32 v108, v108, v140\n\t" \
  "v_add_f32 v109, v109, v141\n\t" \
  "v_add_f32 v110, v110, v142\n\t" \
  "buffer_load_dwordx4 v32, s[4:7], s89 offen offset:2048 sc1 lds\n\t" \
  "v_add_f32 v111, v111, v143\n\t" \
  "s_waitcnt lgkmcnt(2)\n\t" \
  "v_add_f32 v104, v104, v144\n\t" \
  "v_add_f32 v105, v105, v145\n\t" \
  "v_add_f32 v106, v106, v146\n\t" \
  "buffer_load_dwordx4 v32, s[4:7], s89 offen offset:3072 sc1 lds\n\t" \
  "v_add_f32 v107, v107, v147\n\t" \
  "v_add_f32 v108, v108, v148\n\t" \
  "v_add_f32 v109, v109, v149\n\t" \
  "v_add_f32 v110, v110, v150\n\t" \
  "v_add_f32 v111, v111, v151\n\t" \
  "s_add_u32 s89, s50, 32768\n\t" \
  "buffer_load_dwordx4 v[224:227], v33, s[4:7], s89 offen offset:0 sc1\n\t" \
  "s_waitcnt lgkmcnt(0)\n\t" \
  "v_add_f32 v104, v104, v152\n\t" \
  "v_add_f32 v105, v105, v153\n\t" \
  "v_add_f32 v106, v106, v154\n\t" \
  "v_add_f32 v107, v107, v155\n\t" \
  "buffer_load_dwordx4 v[228:231], v33, s[4:7], s89 offen offset:1024 sc1\n\t" \
  "v_add_f32 v108, v108, v156\n\t" \
  "v_add_f32 v109, v109, v157\n\t" \
  "v_add_f32 v110, v110, v158\n\t" \
  "v_add_f32 v111, v111, v159\n\t" \
  "v_cvt_pk_bf16_f32 v28, v104, v105\n\t" \
  "buffer_load_dwordx4 v[232:235], v34, s[4:7], s89 offen offset:0 sc1\n\t" \
  "v_cvt_pk_bf16_f32 v29, v106, v107\n\t" \
  "v_cvt_pk_bf16_f32 v30, v108, v109\n\t" \
  "v_cvt_pk_bf16_f32 v31, v110, v111\n\t" \
  "s_add_u32 s94, s42, 32768\n\t" \
  "buffer_store_dwordx4 v[28:31], v32, s[4:7], s94 offen offset:0 sc1\n\t" \
  "buffer_load_dwordx4 v[236:239], v34, s[4:7], s89 offen offset:1024 sc1\n\t" \
  "v_dot2_f32_bf16 v136, v28, s53, 0\n\t" \
  "v_dot2_f32_bf16 v137, v28, v28, 0\n\t" \
  "v_dot2_f32_bf16 v136, v29, s53, v136\n\t" \
  "v_dot2_f32_bf16 v137, v29, v29, v137\n\t" \
  "v_dot2_f32_bf16 v136, v30, s53, v136\n\t" \
  "s_add_u32 s89, s50, 36864\n\t" \
  "buffer_load_dwordx4 v[240:243], v33, s[4:7], s89 offen offset:0 sc1\n\t" \
  "v_dot2_f32_bf16 v137, v30, v30, v137\n\t" \
  "v_dot2_f32_bf16 v136, v31, s53, v136\n\t" \
  "v_dot2_f32_bf16 v137, v31, v31, v137\n\t" \
  "buffer_load_dwordx4 v[244:247], v33, s[4:7], s89 offen offset:1024 sc1\n\t" \
  "buffer_load_dwordx4 v[248:251], v34, s[4:7], s89 offen offset:0 sc1\n\t" \
  "v_mov_b32 v138, v136\n\t" \
  "v_mov_b32 v139, v137\n\t" \
  "buffer_load_dwordx4 v[252:255], v34, s[4:7], s89 offen offset:1024 sc1\n\t" \
  "v_permlane16_swap_b32 v136, v138\n\t" \
  "v_permlane16_swap_b32 v137, v139\n\t" \
  "v_add_f32 v136, v136, v138\n\t" \
  "v_add_f32 v137, v137, v139\n\t" \
  "v_mov_b32 v138, v136\n\t" \
  "v_mov_b32 v139, v137\n\t" \
  "s_nop 0\n\t" \
  "v_permlane32_swap_b32 v136, v138\n\t" \
  "v_permlane32_swap_b32 v137, v139\n\t" \
  "v_add_f32 v136, v136, v138\n\t" \
  "v_add_f32 v137, v137, v139\n\t" \
  "s_add_u32 s94, s43, 4096\n\t" \
  "buffer_store_dwordx2 v[136:137], v44, s[4:7], s94 offen offset:0 sc1\n\t" \
  "s_waitcnt vmcnt(10)\n\t" \
  "ds_read_b128 v[88:91], v62 offset:0\n\t" \
  "ds_read_b128 v[92:95], v62 offset:1024\n\t" \
  "ds_read_b128 v[96:99], v62 offset:2048\n\t" \
  "ds_read_b128 v[100:103], v62 offset:3072\n\t" \
  "ds_read_b128 v[104:107], v62 offset:16384\n\t" \
  "ds_read_b128 v[108:111], v62 offset:17408\n\t" \
  "ds_read_b128 v[112:115], v62 offset:18432\n\t" \
  "ds_read_b128 v[116:119], v62 offset:19456\n\t" \
  "s_waitcnt lgkmcnt(4)\n\t" \
  "v_max3_u32 v120, v88, v89, v88\n\t" \
  "v_max3_u32 v120, v90, v91, v120\n\t" \
  "v_max3_u32 v120, v92, v93, v120\n\t" \
  "v_max3_u32 v120, v94, v95, v120\n\t" \
  "v_max3_u32 v120, v96, v97, v120\n\t" \
  "v_max3_u32 v120, v98, v99, v120\n\t" \
  "v_max3_u32 v120, v100, v101, v120\n\t" \
  "v_max3_u32 v120, v102, v103, v120\n\t" \
  "s_waitcnt lgkmcnt(0)\n\t" \
  "v_max3_u32 v120, v104, v105, v120\n\t" \
  "v_max3_u32 v120, v106, v107, v120\n\t" \
  "v_max3_u32 v120, v108, v109, v120\n\t" \
  "v_max3_u32 v120, v110, v111, v120\n\t" \
  "v_max3_u32 v120, v112, v113, v120\n\t" \
  "v_max3_u32 v120, v114, v115, v120\n\t" \
  "v_max3_u32 v120, v116, v117, v120\n\t" \
  "v_max3_u32 v120, v118, v119, v120\n\t" \
  "v_add_f32 v184, v184, v186\n\t" \
  "v_add_f32 v185, v185, v187\n\t" \
  "v_add_f32 v188, v188, v190\n\t" \
  "v_add_f32 v189, v189, v191\n\t" \
  "v_add_f32 v184, v184, v188\n\t" \
  "v_add_f32 v185, v185, v189\n\t" \
  "v_cmp_eq_u32 vcc, -1, v120\n\t" \
  "v_cmp_u_f32 s[32:33], v184, v185\n\t" \
  "s_or_b64 vcc, vcc, s[32:33]\n\t" \
  "s_and_b64 vcc, exec, vcc\n\t" \
  "s_cbranch_scc1 Lvaa_%=\n\t" \
  "Lvab_%=:\n\t" \
  "ds_write_b64 v47, v[184:185] offset:0\n\t" \
  "s_waitcnt lgkmcnt(0)\n\t" \
  "s_barrier\n\t" \
  "ds_read_b128 v[88:91], v32 offset:0\n\t" \
  "ds_read_b128 v[92:95], v32 offset:1024\n\t" \
  "ds_read_b128 v[96:99], v32 offset:2048\n\t" \
  "ds_read_b128 v[100:103], v32 offset:3072\n\t" \
  "s_waitcnt lgkmcnt(0)\n\t" \
  "v_mfma_f32_16x16x32_bf16 v[64:67], a[0:3], v[88:91], 0\n\t" \
  "ds_read_b128 v[104:107], v32 offset:4096\n\t" \
  "s_add_u32 m0, s46, 32768\n\t" \
  "v_mfma_f32_16x16x32_bf16 v[68:71], a[64:67], v[88:91], 0\n\t" \
  "ds_read_b128 v[108:111], v32 offset:5120\n\t" \
  "s_add_u32 s89, s45, 16384\n\t" \
  "v_mfma_f32_16x16x32_bf16 v[72:75], a[128:131], v[88:91], 0\n\t" \
  "buffer_load_dwordx4 v32, s[4:7], s89 offen offset:0 sc1 lds\n\t" \
  "ds_read_b128 v[184:187], v48 offset:0\n\t" \
  "v_mfma_f32_16x16x32_bf16 v[64:67], a[4:7], v[92:95], v[64:67]\n\t" \
  "ds_read_b128 v[112:115], v32 offset:6144\n\t" \
  "ds_read_b128 v[188:191], v48 offset:16\n\t" \
  "v_mfma_f32_16x16x32_bf16 v[68:71], a[68:71], v[92:95], v[68:71]\n\t" \
  "ds_read_b128 v[116:119], v32 offset:7168\n\t" \
  "buffer_load_dwordx4 v32, s[4:7], s89 offen offset:1024 sc1 lds\n\t" \
  "v_mfma_f32_16x16x32_bf16 v[72:75], a[132:135], v[92:95], v[72:75]\n\t" \
  "v_mfma_f32_16x16x32_bf16 v[64:67], a[8:11], v[96:99], v[64:67]\n\t" \
  "buffer_load_dwordx4 v32, s[4:7], s89 offen offset:2048 sc1 lds\n\t" \
  "v_mfma_f32_16x16x32_bf16 v[68:71], a[72:75], v[96:99], v[68:71]\n\t" \
  "v_mfma_f32_16x16x32_bf16 v[72:75], a[136:139], v[96:99], v[72:75]\n\t" \
  "buffer_load_dwordx4 v32, s[4:7], s89 offen offset:3072 sc1 lds\n\t" \
  "s_waitcnt lgkmcnt(1)\n\t" \
  "v_mfma_f32_16x16x32_bf16 v[64:67], a[12:15], v[100:103], v[64:67]\n\t" \
  "s_add_u32 m0, s46, 49152\n\t" \
  "v_add_f32 v184, v184, v186\n\t" \
  "v_mfma_f32_16x16x32_bf16 v[68:71], a[76:79], v[100:103], v[68:71]\n\t" \
  "s_add_u32 s89, s45, 81920\n\t" \
  "v_add_f32 v185, v185, v187\n\t" \
  "v_mfma_f32_16x16x32_bf16 v[72:75], a[140:143], v[100:103], v[72:75]\n\t" \
  "buffer_load_dwordx4 v32, s[4:7], s89 offen offset:0 sc1 lds\n\t" \
  "s_waitcnt lgkmcnt(0)\n\t" \
  "v_mfma_f32_16x16x32_bf16 v[64:67], a[16:19], v[104:107], v[64:67]\n\t" \
  "ds_read_b128 v[88:91], v32 offset:8192\n\t" \
  "v_add_f32 v188, v188, v190\n\t" \
  "v_mfma_f32_16x16x32_bf16 v[68:71], a[80:83], v[104:107], v[68:71]\n\t" \
  "ds_read_b128 v[92:95], v32 offset:9216\n\t" \
  "buffer_load_dwordx4 v32, s[4:7], s89 offen offset:1024 sc1 lds\n\t" \
  "v_mfma_f32_16x16x32_bf16 v[72:75], a[144:147], v[104:107], v[72:75]\n\t" \
  "v_add_f32 v189, v189, v191\n\t" \
  "v_add_f32 v184, v184, v188\n\t" \
  "v_mfma_f32_16x16x32_bf16 v[64:67], a[20:23], v[108:111], v[64:67]\n\t" \
  "ds_read_b128 v[96:99], v32 offset:10240\n\t" \
  "buffer_load_dwordx4 v32, s[4:7], s89 offen offset:2048 sc1 lds\n\t" \
  "v_mfma_f32_16x16x32_bf16 v[68:71], a[84:87], v[108:111], v[68:71]\n\t" \
  "ds_read_b128 v[100:103], v32 offset:11264\n\t" \
  "v_add_f32 v185, v185, v189\n\t" \
  "v_mfma_f32_16x16x32_bf16 v[72:75], a[148:151], v[108:111], v[72:75]\n\t" \
  "buffer_load_dwordx4 v32, s[4:7], s89 offen offset:3072 sc1 lds\n\t" \
  "v_mul_f32 v184, 0xbb000000, v184\n\t" \
  "v_mfma_f32_16x16x32_bf16 v[64:67], a[24:27], v[112:115], v[64:67]\n\t" \
  "s_waitcnt vmcnt(8)\n\t" \
  "v_mul_f32 v187, 0x3b000000, v185\n\t" \
  "v_mfma_f32_16x16x32_bf16 v[68:71], a[88:91], v[112:115], v[68:71]\n\t" \
  "s_add_u32 s95, s44, 131072\n\t" \
  "v_fma_f32 v187, -v184, v184, v187\n\t" \
  "v_mfma_f32_16x16x32_bf16 v[72:75], a[152:155], v[112:115], v[72:75]\n\t" \
  "buffer_store_dwordx2 v[56:57], v55, s[4:7], s95 offen offset:0 sc1\n\t" \
  "v_max_f32 v187, 0, v187\n\t" \
  "v_mfma_f32_16x16x32_bf16 v[64:67], a[28:31], v[116:119], v[64:67]\n\t" \
  "v_add_f32 v187, 0x3727c5ac, v187\n\t" \
  "v_mfma_f32_16x16x32_bf16 v[68:71], a[92:95], v[116:119], v[68:71]\n\t" \
  "v_rsq_f32 v188, v187\n\t" \
  "v_mfma_f32_16x16x32_bf16 v[72:75], a[156:159], v[116:119], v[72:75]\n\t" \
  "s_nop 0\n\t" \
  "s_waitcnt lgkmcnt(0)\n\t" \
  "v_mfma_f32_16x16x32_bf16 v[64:67], a[32:35], v[88:91], v[64:67]\n\t" \
  "ds_read_b128 v[104:107], v32 offset:12288\n\t" \
  "v_mul_f32 v185, 0xc0135761, v188\n\t" \
  "v_mfma_f32_16x16x32_bf16 v[68:71], a[96:99], v[88:91], v[68:71]\n\t" \
  "ds_read_b128 v[108:111], v32 offset:13312\n\t" \
  "v_mul_f32 v188, v188, v188\n\t" \
  "v_mfma_f32_16x16x32_bf16 v[72:75], a[160:163], v[88:91], v[72:75]\n\t" \
  "v_mul_f32 v186, 0x3d372713, v185\n\t" \
  "v_mul_f32 v186, v186, v188\n\t" \
  "v_mfma_f32_16x16x32_bf16 v[64:67], a[36:39], v[92:95], v[64:67]\n\t" \
  "ds_read_b128 v[112:115], v32 offset:14336\n\t" \
  "ds_write_b128 v49, v[184:187] offset:0\n\t" \
  "v_mfma_f32_16x16x32_bf16 v[68:71], a[100:103], v[92:95], v[68:71]\n\t" \
  "ds_read_b128 v[116:119], v32 offset:15360\n\t" \
  "v_mfma_f32_16x16x32_bf16 v[72:75], a[164:167], v[92:95], v[72:75]\n\t" \
  "v_mfma_f32_16x16x32_bf16 v[64:67], a[40:43], v[96:99], v[64:67]\n\t" \
  "v_mfma_f32_16x16x32_bf16 v[68:71], a[104:107], v[96:99], v[68:71]\n\t" \
  "v_mfma_f32_16x16x32_bf16 v[72:75], a[168:171], v[96:99], v[72:75]\n\t" \
  "v_mfma_f32_16x16x32_bf16 v[64:67], a[44:47], v[100:103], v[64:67]\n\t" \
  "v_mfma_f32_16x16x32_bf16 v[68:71], a[108:111], v[100:103], v[68:71]\n\t" \
  "v_mfma_f32_16x16x32_bf16 v[72:75], a[172:175], v[100:103], v[72:75]\n\t" \
  "s_waitcnt lgkmcnt(0)\n\t" \
  "v_mfma_f32_16x16x32_bf16 v[64:67], a[48:51], v[104:107], v[64:67]\n\t" \
  "ds_read_b128 v[88:91], v32 offset:16384\n\t" \
  "s_waitcnt vmcnt(1)\n\t" \
  "ds_read_b128 v[128:131], v62 offset:32768\n\t" \
  "v_mfma_f32_16x16x32_bf16 v[68:71], a[112:115], v[104:107], v[68:71]\n\t" \
  "ds_read_b128 v[92:95], v32 offset:17408\n\t" \
  "ds_read_b128 v[132:135], v62 offset:33792\n\t" \
  "ds_read_b128 v[136:139], v62 offset:34816\n\t" \
  "v_mfma_f32_16x16x32_bf16 v[72:75], a[176:179], v[104:107], v[72:75]\n\t" \
  "ds_read_b128 v[140:143], v62 offset:35840\n\t" \
  "s_waitcnt lgkmcnt(0)\n\t" \
  "v_max3_u32 v87, v128, v129, v128\n\t" \
  "v_mfma_f32_16x16x32_bf16 v[64:67], a[52:55], v[108:111], v[64:67]\n\t" \
  "ds_read_b128 v[96:99], v32 offset:18432\n\t" \
  "v_max3_u32 v87, v130, v131, v87\n\t" \
  "v_max3_u32 v87, v132, v133, v87\n\t" \
  "v_mfma_f32_16x16x32_bf16 v[68:71], a[116:119], v[108:111], v[68:71]\n\t" \
  "ds_read_b128 v[100:103], v32 offset:19456\n\t" \
  "v_max3_u32 v87, v134, v135, v87\n\t" \
  "v_max3_u32 v87, v136, v137, v87\n\t" \
  "v_mfma_f32_16x16x32_bf16 v[72:75], a[180:183], v[108:111], v[72:75]\n\t" \
  "v_max3_u32 v87, v138, v139, v87\n\t" \
  "v_max3_u32 v87, v140, v141, v87\n\t" \
  "v_max3_u32 v87, v142, v143, v87\n\t" \
  "v_mfma_f32_16x16x32_bf16 v[64:67], a[56:59], v[112:115], v[64:67]\n\t" \
  "ds_read_b128 v[120:123], v50 offset:0\n\t" \
  "ds_read_b128 v[128:131], v62 offset:49152\n\t" \
  "ds_read_b128 v[132:135], v62 offset:50176\n\t" \
  "v_mfma_f32_16x16x32_bf16 v[68:71], a[120:123], v[112:115], v[68:71]\n\t" \
  "ds_read_b128 v[136:139], v62 offset:51200\n\t" \
  "ds_read_b128 v[140:143], v62 offset:52224\n\t" \
  "s_waitcnt lgkmcnt(0)\n\t" \
  "v_mfma_f32_16x16x32_bf16 v[72:75], a[184:187], v[112:115], v[72:75]\n\t" \
  "v_max3_u32 v87, v128, v129, v87\n\t" \
  "v_max3_u32 v87, v130, v131, v87\n\t" \
  "v_max3_u32 v87, v132, v133, v87\n\t" \
  "v_mfma_f32_16x16x32_bf16 v[64:67], a[60:63], v[116:119], v[64:67]\n\t" \
  "v_max3_u32 v87, v134, v135, v87\n\t" \
  "v_max3_u32 v87, v136, v137, v87\n\t" \
  "v_mfma_f32_16x16x32_bf16 v[68:71], a[124:127], v[116:119], v[68:71]\n\t" \
  "v_max3_u32 v87, v138, v139, v87\n\t" \
  "v_max3_u32 v87, v140, v141, v87\n\t" \
  "v_mfma_f32_16x16x32_bf16 v[72:75], a[188:191], v[116:119], v[72:75]\n\t" \
  "v_max3_u32 v87, v142, v143, v87\n\t" \
  "v_cmp_eq_u32 vcc, -1, v87\n\t" \
  "s_and_b64 vcc, exec, vcc\n\t" \
  "s_cbranch_scc1 Lvba_%=\n\t" \
  "Lvbb_%=:\n\t" \
  "v_mfma_f32_16x16x32_bf16 v[76:79], a[0:3], v[88:91], 0\n\t" \
  "ds_read_b128 v[104:107], v32 offset:20480\n\t" \
  "buffer_load_dwordx4 a[192:195], v33, s[4:7], s50 offen offset:0 sc1\n\t" \
  "v_fma_f32 v128, v168, v120, v64\n\t" \
  "v_mfma_f32_16x16x32_bf16 v[80:83], a[64:67], v[88:91], 0\n\t" \
  "ds_read_b128 v[108:111], v32 offset:21504\n\t" \
  "v_fma_f32 v129, v169, v120, v65\n\t" \
  "v_mul_f32 v132, v128, v128\n\t" \
  "v_mfma_f32_16x16x32_bf16 v[84:87], a[128:131], v[88:91], 0\n\t" \
  "v_mul_f32 v133, v129, v129\n\t" \
  "v_fma_f32 v132, v132, v122, v121\n\t" \
  "v_fma_f32 v133, v133, v122, v121\n\t" \
  "v_mfma_f32_16x16x32_bf16 v[76:79], a[4:7], v[92:95], v[76:79]\n\t" \
  "ds_read_b128 v[112:115], v32 offset:22528\n\t" \
  "v_mul_f32 v132, v132, v128\n\t" \
  "v_mul_f32 v133, v133, v129\n\t" \
  "v_mfma_f32_16x16x32_bf16 v[80:83], a[68:71], v[92:95], v[80:83]\n\t" \
  "ds_read_b128 v[116:119], v32 offset:23552\n\t" \
  "buffer_load_dwordx4 a[196:199], v33, s[4:7], s50 offen offset:1024 sc1\n\t" \
  "v_fma_f32 v136, v172, v120, v68\n\t" \
  "v_mfma_f32_16x16x32_bf16 v[84:87], a[132:135], v[92:95], v[84:87]\n\t" \
  "v_exp_f32 v134, v132\n\t" \
  "v_fma_f32 v137, v173, v120, v69\n\t" \
  "v_mfma_f32_16x16x32_bf16 v[76:79], a[8:11], v[96:99], v[76:79]\n\t" \
  "v_exp_f32 v135, v133\n\t" \
  "v_fma_f32 v130, v170, v120, v66\n\t" \
  "v_mfma_f32_16x16x32_bf16 v[80:83], a[72:75], v[96:99], v[80:83]\n\t" \
  "v_fma_f32 v131, v171, v120, v67\n\t" \
  "v_fma_f32 v134, v134, v123, v123\n\t" \
  "v_fma_f32 v135, v135, v123, v123\n\t" \
  "v_mfma_f32_16x16x32_bf16 v[84:87], a[136:139], v[96:99], v[84:87]\n\t" \
  "buffer_load_dwordx4 a[200:203], v34, s[4:7], s50 offen offset:0 sc1\n\t" \
  "v_rcp_f32 v134, v134\n\t" \
  "v_mfma_f32_16x16x32_bf16 v[76:79], a[12:15], v[100:103], v[76:79]\n\t" \
  "v_rcp_f32 v135, v135\n\t" \
  "v_mul_f32 v130, v130, v128\n\t" \
  "v_mfma_f32_16x16x32_bf16 v[80:83], a[76:79], v[100:103], v[80:83]\n\t" \
  "v_mul_f32 v131, v131, v129\n\t" \
  "v_mul_f32 v130, v130, v134\n\t" \
  "v_mul_f32 v131, v131, v135\n\t" \
  "v_mfma_f32_16x16x32_bf16 v[84:87], a[140:143], v[100:103], v[84:87]\n\t" \
  "v_cvt_pk_bf16_f32 v180, v130, v131\n\t" \
  "v_mul_f32 v140, v136, v136\n\t" \
  "s_waitcnt lgkmcnt(0)\n\t" \
  "v_mfma_f32_16x16x32_bf16 v[76:79], a[16:19], v[104:107], v[76:79]\n\t" \
  "ds_read_b128 v[88:91], v32 offset:24576\n\t" \
  "buffer_load_dwordx4 a[204:207], v34, s[4:7], s50 offen offset:1024 sc1\n\t" \
  "v_mul_f32 v141, v137, v137\n\t" \
  "v_mfma_f32_16x16x32_bf16 v[80:83], a[80:83], v[104:107], v[80:83]\n\t" \
  "ds_read_b128 v[92:95], v32 offset:25600\n\t" \
  "s_add_u32 s89, s50, 4096\n\t" \
  "v_fma_f32 v140, v140, v122, v121\n\t" \
  "v_mfma_f32_16x16x32_bf16 v[84:87], a[144:147], v[104:107], v[84:87]\n\t" \
  "v_fma_f32 v141, v141, v122, v121\n\t" \
  "v_mul_f32 v140, v140, v136\n\t" \
  "v_mfma_f32_16x16x32_bf16 v[76:79], a[20:23], v[108:111], v[76:79]\n\t" \
  "ds_read_b128 v[96:99], v32 offset:26624\n\t" \
  "v_mul_f32 v141, v141, v137\n\t" \
  "v_fma_f32 v128, v176, v120, v72\n\t" \
  "v_mfma_f32_16x16x32_bf16 v[80:83], a[84:87], v[108:111], v[80:83]\n\t" \
  "ds_read_b128 v[100:103], v32 offset:27648\n\t" \
  "buffer_load_dwordx4 a[208:211], v33, s[4:7], s89 offen offset:0 sc1\n\t" \
  "v_mfma_f32_16x16x32_bf16 v[84:87], a[148:151], v[108:111], v[84:87]\n\t" \
  "v_exp_f32 v142, v140\n\t" \
  "v_mfma_f32_16x16x32_bf16 v[76:79], a[24:27], v[112:115], v[76:79]\n\t" \
  "v_exp_f32 v143, v141\n\t" \
  "v_mfma_f32_16x16x32_bf16 v[80:83], a[88:91], v[112:115], v[80:83]\n\t" \
  "v_fma_f32 v138, v174, v120, v70\n\t" \
  "v_fma_f32 v139, v175, v120, v71\n\t" \
  "v_mfma_f32_16x16x32_bf16 v[84:87], a[152:155], v[112:115], v[84:87]\n\t" \
  "buffer_load_dwordx4 a[212:215], v33, s[4:7], s89 offen offset:1024 sc1\n\t" \
  "v_fma_f32 v142, v142, v123, v123\n\t" \
  "v_mfma_f32_16x16x32_bf16 v[76:79], a[28:31], v[116:119], v[76:79]\n\t" \
  "v_fma_f32 v143, v143, v123, v123\n\t" \
  "v_fma_f32 v129, v177, v120, v73\n\t" \
  "v_mfma_f32_16x16x32_bf16 v[80:83], a[92:95], v[116:119], v[80:83]\n\t" \
  "v_rcp_f32 v142, v142\n\t" \
  "v_mfma_f32_16x16x32_bf16 v[84:87], a[156:159], v[116:119], v[84:87]\n\t" \
  "v_mul_f32 v132, v128, v128\n\t" \
  "s_waitcnt lgkmcnt(0)\n\t" \
  "v_mfma_f32_16x16x32_bf16 v[76:79], a[32:35], v[88:91], v[76:79]\n\t" \
  "ds_read_b128 v[104:107], v32 offset:28672\n\t" \
  "buffer_load_dwordx4 a[216:219], v34, s[4:7], s89 offen offset:0 sc1\n\t" \
  "v_mfma_f32_16x16x32_bf16 v[80:83], a[96:99], v[88:91], v[80:83]\n\t" \
  "ds_read_b128 v[108:111], v32 offset:29696\n\t" \
  "v_mul_f32 v133, v129, v129\n\t" \
  "v_mfma_f32_16x16x32_bf16 v[84:87], a[160:163], v[88:91], v[84:87]\n\t" \
  "v_rcp_f32 v143, v143\n\t" \
  "v_mfma_f32_16x16x32_bf16 v[76:79], a[36:39], v[92:95], v[76:79]\n\t" \
  "ds_read_b128 v[112:115], v32 offset:30720\n\t" \
  "v_mul_f32 v138, v138, v136\n\t" \
  "v_mfma_f32_16x16x32_bf16 v[80:83], a[100:103], v[92:95], v[80:83]\n\t" \
  "ds_read_b128 v[116:119], v32 offset:31744\n\t" \
  "buffer_load_dwordx4 a[220:223], v34, s[4:7], s89 offen offset:1024 sc1\n\t" \
  "v_mul_f32 v139, v139, v137\n\t" \
  "v_mfma_f32_16x16x32_bf16 v[84:87], a[164:167], v[92:95], v[84:87]\n\t" \
  "s_mov_b32 s91, 131072\n\t" \
  "v_mul_f32 v138, v138, v142\n\t" \
  "v_mfma_f32_16x16x32_bf16 v[76:79], a[40:43], v[96:99], v[76:79]\n\t" \
  "v_mul_f32 v139, v139, v143\n\t" \
  "v_cvt_pk_bf16_f32 v181, v138, v139\n\t" \
  "v_mfma_f32_16x16x32_bf16 v[80:83], a[104:107], v[96:99], v[80:83]\n\t" \
  "v_fma_f32 v132, v132, v122, v121\n\t" \
  "v_fma_f32 v133, v133, v122, v121\n\t" \
  "v_mfma_f32_16x16x32_bf16 v[84:87], a[168:171], v[96:99], v[84:87]\n\t" \
  "v_mul_f32 v132, v132, v128\n\t" \
  "v_mul_f32 v133, v133, v129\n\t" \
  "v_mfma_f32_16x16x32_bf16 v[76:79], a[44:47], v[100:103], v[76:79]\n\t" \
  "v_exp_f32 v134, v132\n\t" \
  "v_mfma_f32_16x16x32_bf16 v[80:83], a[108:111], v[100:103], v[80:83]\n\t" \
  "v_exp_f32 v135, v133\n\t" \
  "v_mfma_f32_16x16x32_bf16 v[84:87], a[172:175], v[100:103], v[84:87]\n\t" \
  "v_fma_f32 v130, v178, v120, v74\n\t" \
  "s_waitcnt lgkmcnt(0)\n\t" \
  "v_mfma_f32_16x16x32_bf16 v[76:79], a[48:51], v[104:107], v[76:79]\n\t" \
  "s_barrier\n\t" \
  "ds_read_b128 v[88:91], v32 offset:32768\n\t" \
  "v_mfma_f32_16x16x32_bf16 v[80:83], a[112:115], v[104:107], v[80:83]\n\t" \
  "ds_read_b128 v[92:95], v32 offset:33792\n\t" \
  "v_fma_f32 v131, v179, v120, v75\n\t" \
  "v_mfma_f32_16x16x32_bf16 v[84:87], a[176:179], v[104:107], v[84:87]\n\t" \
  "v_fma_f32 v134, v134, v123, v123\n\t" \
  "v_fma_f32 v135, v135, v123, v123\n\t" \
  "v_mfma_f32_16x16x32_bf16 v[76:79], a[52:55], v[108:111], v[76:79]\n\t" \
  "ds_read_b128 v[96:99], v32 offset:34816\n\t" \
  "v_rcp_f32 v134, v134\n\t" \
  "v_mfma_f32_16x16x32_bf16 v[80:83], a[116:119], v[108:111], v[80:83]\n\t" \
  "ds_read_b128 v[100:103], v32 offset:35840\n\t" \
  "buffer_load_dwordx2 v[60:61], v45, s[4:7], s91 offen offset:0 sc1\n\t" \
  "v_mfma_f32_16x16x32_bf16 v[84:87], a[180:183], v[108:111], v[84:87]\n\t" \
  "v_rcp_f32 v135, v135\n\t" \
  "v_mfma_f32_16x16x32_bf16 v[76:79], a[56:59], v[112:115], v[76:79]\n\t" \
  "ds_read_b128 v[124:127], v50 offset:256\n\t" \
  "v_mul_f32 v130, v130, v128\n\t" \
  "v_mfma_f32_16x16x32_bf16 v[80:83], a[120:123], v[112:115], v[80:83]\n\t" \
  "v_mul_f32 v131, v131, v129\n\t" \
  "v_mul_f32 v130, v130, v134\n\t" \
  "v_mfma_f32_16x16x32_bf16 v[84:87], a[184:187], v[112:115], v[84:87]\n\t" \
  "v_mul_f32 v131, v131, v135\n\t" \
  "v_cvt_pk_bf16_f32 v182, v130, v131\n\t" \
  "v_mfma_f32_16x16x32_bf16 v[76:79], a[60:63], v[116:119], v[76:79]\n\t" \
  "v_permlane32_swap_b32 v180, v183\n\t" \
  "s_add_u32 s94, s48, 0\n\t" \
  "v_mfma_f32_16x16x32_bf16 v[80:83], a[124:127], v[116:119], v[80:83]\n\t" \
  "v_permlane16_swap_b32 v181, v180\n\t" \
  "v_permlane16_swap_b32 v182, v183\n\t" \
  "v_mfma_f32_16x16x32_bf16 v[84:87], a[188:191], v[116:119], v[84:87]\n\t" \
  "buffer_store_dwordx4 v[180:183], v52, s[4:7], s94 offen offset:0 sc1\n\t" \
  "s_waitcnt lgkmcnt(0)\n\t" \
  "v_mfma_f32_16x16x32_bf16 v[64:67], a[0:3], v[88:91], 0\n\t" \
  "ds_read_b128 v[104:107], v32 offset:36864\n\t" \
  "v_fma_f32 v128, v168, v124, v76\n\t" \
  "v_fma_f32 v129, v169, v124, v77\n\t" \
  "v_mfma_f32_16x16x32_bf16 v[68:71], a[64:67], v[88:91], 0\n\t" \
  "ds_read_b128 v[108:111], v32 offset:37888\n\t" \
  "v_mul_f32 v132, v128, v128\n\t" \
  "v_mul_f32 v133, v129, v129\n\t" \
  "v_mfma_f32_16x16x32_bf16 v[72:75], a[128:131], v[88:91], 0\n\t" \
  "v_fma_f32 v132, v132, v126, v125\n\t" \
  "v_fma_f32 v133, v133, v126, v125\n\t" \
  "v_mul_f32 v132, v132, v128\n\t" \
  "v_mfma_f32_16x16x32_bf16 v[64:67], a[4:7], v[92:95], v[64:67]\n\t" \
  "ds_read_b128 v[112:115], v32 offset:38912\n\t" \
  "v_mul_f32 v133, v133, v129\n\t" \
  "v_fma_f32 v136, v172, v124, v80\n\t" \
  "v_mfma_f32_16x16x32_bf16 v[68:71], a[68:71], v[92:95], v[68:71]\n\t" \
  "ds_read_b128 v[116:119], v32 offset:39936\n\t" \
  "v_exp_f32 v134, v132\n\t" \
  "v_mfma_f32_16x16x32_bf16 v[72:75], a[132:135], v[92:95], v[72:75]\n\t" \
  "v_exp_f32 v135, v133\n\t" \
  "v_fma_f32 v130, v170, v124, v78\n\t" \
  "v_mfma_f32_16x16x32_bf16 v[64:67], a[8:11], v[96:99], v[64:67]\n\t" \
  "v_fma_f32 v131, v171, v124, v79\n\t" \
  "v_fma_f32 v134, v134, v127, v127\n\t" \
  "v_fma_f32 v135, v135, v127, v127\n\t" \
  "v_mfma_f32_16x16x32_bf16 v[68:71], a[72:75], v[96:99], v[68:71]\n\t" \
  "v_rcp_f32 v134, v134\n\t" \
  "v_fma_f32 v137, v173, v124, v81\n\t" \
  "v_mfma_f32_16x16x32_bf16 v[72:75], a[136:139], v[96:99], v[72:75]\n\t" \
  "v_rcp_f32 v135, v135\n\t" \
  "v_mul_f32 v130, v130, v128\n\t" \
  "v_mfma_f32_16x16x32_bf16 v[64:67], a[12:15], v[100:103], v[64:67]\n\t" \
  "v_mul_f32 v131, v131, v129\n\t" \
  "v_mul_f32 v130, v130, v134\n\t" \
  "v_mul_f32 v131, v131, v135\n\t" \
  "v_mfma_f32_16x16x32_bf16 v[68:71], a[76:79], v[100:103], v[68:71]\n\t" \
  "v_cvt_pk_bf16_f32 v180, v130, v131\n\t" \
  "v_mul_f32 v140, v136, v136\n\t" \
  "v_mul_f32 v141, v137, v137\n\t" \
  "v_mfma_f32_16x16x32_bf16 v[72:75], a[140:143], v[100:103], v[72:75]\n\t" \
  "v_fma_f32 v140, v140, v126, v125\n\t" \
  "v_fma_f32 v141, v141, v126, v125\n\t" \
  "s_waitcnt lgkmcnt(0)\n\t" \
  "v_mfma_f32_16x16x32_bf16 v[64:67], a[16:19], v[104:107], v[64:67]\n\t" \
  "ds_read_b128 v[88:91], v32 offset:40960\n\t" \
  "s_waitcnt vmcnt(1)\n\t" \
  "s_movk_i32 s90, 0x7fff\n\t" \
  "Lp3_%=:\n\t" \
  "v_cmp_ne_u64 vcc, s[54:55], v[60:61]\n\t" \
  "s_and_b64 vcc, exec, vcc\n\t" \
  "s_cbranch_scc0 Lq3_%=\n\t" \
  "s_sub_u32 s90, s90, 1\n\t" \
  "s_cbranch_scc1 Lq3_%=\n\t" \
  "s_sleep 3\n\t" \
  "buffer_load_dwordx2 v[60:61], v45, s[4:7], s91 offen sc1\n\t" \
  "s_waitcnt vmcnt(0)\n\t" \
  "s_branch Lp3_%=\n\t" \
  "Lq3_%=:\n\t" \
  "v_mfma_f32_16x16x32_bf16 v[68:71], a[80:83], v[104:107], v[68:71]\n\t" \
  "ds_read_b128 v[92:95], v32 offset:41984\n\t" \
  "s_add_u32 m0, s46, 65536\n\t" \
  "v_mul_f32 v140, v140, v136\n\t" \
  "v_mfma_f32_16x16x32_bf16 v[72:75], a[144:147], v[104:107], v[72:75]\n\t" \
  "s_add_u32 s89, s45, 32768\n\t" \
  "v_mul_f32 v141, v141, v137\n\t" \
  "v_fma_f32 v128, v176, v124, v84\n\t" \
  "v_mfma_f32_16x16x32_bf16 v[64:67], a[20:23], v[108:111], v[64:67]\n\t" \
  "ds_read_b128 v[96:99], v32 offset:43008\n\t" \
  "buffer_load_dwordx4 v32, s[4:7], s89 offen offset:0 sc1 lds\n\t" \
  "v_fma_f32 v129, v177, v124, v85\n\t" \
  "v_mfma_f32_16x16x32_bf16 v[68:71], a[84:87], v[108:111], v[68:71]\n\t" \
  "ds_read_b128 v[100:103], v32 offset:44032\n\t" \
  "v_exp_f32 v142, v140\n\t" \
  "v_mfma_f32_16x16x32_bf16 v[72:75], a[148:151], v[108:111], v[72:75]\n\t" \
  "buffer_load_dwordx4 v32, s[4:7], s89 offen offset:1024 sc1 lds\n\t" \
  "v_exp_f32 v143, v141\n\t" \
  "v_mfma_f32_16x16x32_bf16 v[64:67], a[24:27], v[112:115], v[64:67]\n\t" \
  "v_fma_f32 v138, v174, v124, v82\n\t" \
  "v_fma_f32 v139, v175, v124, v83\n\t" \
  "v_fma_f32 v142, v142, v127, v127\n\t" \
  "v_mfma_f32_16x16x32_bf16 v[68:71], a[88:91], v[112:115], v[68:71]\n\t" \
  "buffer_load_dwordx4 v32, s[4:7], s89 offen offset:2048 sc1 lds\n\t" \
  "v_fma_f32 v143, v143, v127, v127\n\t" \
  "v_mul_f32 v132, v128, v128\n\t" \
  "v_mfma_f32_16x16x32_bf16 v[72:75], a[152:155], v[112:115], v[72:75]\n\t" \
  "v_rcp_f32 v142, v142\n\t" \
  "v_mul_f32 v133, v129, v129\n\t" \
  "v_mfma_f32_16x16x32_bf16 v[64:67], a[28:31], v[116:119], v[64:67]\n\t" \
  "buffer_load_dwordx4 v32, s[4:7], s89 offen offset:3072 sc1 lds\n\t" \
  "v_fma_f32 v132, v132, v126, v125\n\t" \
  "v_mfma_f32_16x16x32_bf16 v[68:71], a[92:95], v[116:119], v[68:71]\n\t" \
  "s_add_u32 s88, s47, 4096\n\t" \
  "v_fma_f32 v133, v133, v126, v125\n\t" \
  "v_mfma_f32_16x16x32_bf16 v[72:75], a[156:159], v[116:119], v[72:75]\n\t" \
  "buffer_load_dwordx2 v[184:185], v46, s[4:7], s88 offen offset:0 sc1\n\t" \
  "v_mul_f32 v132, v132, v128\n\t" \
  "s_waitcnt lgkmcnt(0)\n\t" \
  "v_mfma_f32_16x16x32_bf16 v[64:67], a[32:35], v[88:91], v[64:67]\n\t" \
  "ds_read_b128 v[104:107], v32 offset:45056\n\t" \
  "v_mul_f32 v133, v133, v129\n\t" \
  "v_mfma_f32_16x16x32_bf16 v[68:71], a[96:99], v[88:91], v[68:71]\n\t" \
  "ds_read_b128 v[108:111], v32 offset:46080\n\t" \
  "buffer_load_dwordx2 v[186:187], v46, s[4:7], s88 offen offset:128 sc1\n\t" \
  "v_permlane32_swap_b32 v180, v183\n\t" \
  "v_mfma_f32_16x16x32_bf16 v[72:75], a[160:163], v[88:91], v[72:75]\n\t" \
  "v_rcp_f32 v143, v143\n\t" \
  "v_mfma_f32_16x16x32_bf16 v[64:67], a[36:39], v[92:95], v[64:67]\n\t" \
  "ds_read_b128 v[112:115], v32 offset:47104\n\t" \
  "buffer_load_dwordx2 v[188:189], v46, s[4:7], s88 offen offset:256 sc1\n\t" \
  "v_mfma_f32_16x16x32_bf16 v[68:71], a[100:103], v[92:95], v[68:71]\n\t" \
  "ds_read_b128 v[116:119], v32 offset:48128\n\t" \
  "v_mul_f32 v138, v138, v136\n\t" \
  "v_mul_f32 v139, v139, v137\n\t" \
  "v_mfma_f32_16x16x32_bf16 v[72:75], a[164:167], v[92:95], v[72:75]\n\t" \
  "buffer_load_dwordx2 v[190:191], v46, s[4:7], s88 offen offset:384 sc1\n\t" \
  "v_mul_f32 v138, v138, v142\n\t" \
  "v_mfma_f32_16x16x32_bf16 v[64:67], a[40:43], v[96:99], v[64:67]\n\t" \
  "s_add_u32 m0, s46, 81920\n\t" \
  "v_mul_f32 v139, v139, v143\n\t" \
  "v_mfma_f32_16x16x32_bf16 v[68:71], a[104:107], v[96:99], v[68:71]\n\t" \
  "s_add_u32 s89, s45, 98304\n\t" \
  "v_cvt_pk_bf16_f32 v181, v138, v139\n\t" \
  "v_mfma_f32_16x16x32_bf16 v[72:75], a[168:171], v[96:99], v[72:75]\n\t" \
  "buffer_load_dwordx4 v32, s[4:7], s89 offen offset:0 sc1 lds\n\t" \
  "s_add_u32 s94, s48, 180224\n\t" \
  "v_mfma_f32_16x16x32_bf16 v[64:67], a[44:47], v[100:103], v[64:67]\n\t" \
  "v_exp_f32 v134, v132\n\t" \
  "v_mfma_f32_16x16x32_bf16 v[68:71], a[108:111], v[100:103], v[68:71]\n\t" \
  "buffer_load_dwordx4 v32, s[4:7], s89 offen offset:1024 sc1 lds\n\t" \
  "v_permlane16_swap_b32 v181, v180\n\t" \
  "v_mfma_f32_16x16x32_bf16 v[72:75], a[172:175], v[100:103], v[72:75]\n\t" \
  "s_waitcnt lgkmcnt(0)\n\t" \
  "v_mfma_f32_16x16x32_bf16 v[64:67], a[48:51], v[104:107], v[64:67]\n\t" \
  "ds_read_b128 v[88:91], v32 offset:49152\n\t" \
  "buffer_load_dwordx4 v32, s[4:7], s89 offen offset:2048 sc1 lds\n\t" \
  "v_mfma_f32_16x16x32_bf16 v[68:71], a[112:115], v[104:107], v[68:71]\n\t" \
  "ds_read_b128 v[92:95], v32 offset:50176\n\t" \
  "v_mfma_f32_16x16x32_bf16 v[72:75], a[176:179], v[104:107], v[72:75]\n\t" \
  "buffer_load_dwordx4 v32, s[4:7], s89 offen offset:3072 sc1 lds\n\t" \
  "v_exp_f32 v135, v133\n\t" \
  "v_mfma_f32_16x16x32_bf16 v[64:67], a[52:55], v[108:111], v[64:67]\n\t" \
  "ds_read_b128 v[96:99], v32 offset:51200\n\t" \
  "v_fma_f32 v130, v178, v124, v86\n\t" \
  "v_fma_f32 v131, v179, v124, v87\n\t" \
  "v_mfma_f32_16x16x32_bf16 v[68:71], a[116:119], v[108:111], v[68:71]\n\t" \
  "ds_read_b128 v[100:103], v32 offset:52224\n\t" \
  "v_fma_f32 v134, v134, v127, v127\n\t" \
  "v_mfma_f32_16x16x32_bf16 v[72:75], a[180:183], v[108:111], v[72:75]\n\t" \
  "v_fma_f32 v135, v135, v127, v127\n\t" \
  "v_mfma_f32_16x16x32_bf16 v[64:67], a[56:59], v[112:115], v[64:67]\n\t" \
  "ds_read_b128 v[120:123], v50 offset:512\n\t" \
  "v_rcp_f32 v134, v134\n\t" \
  "v_mfma_f32_16x16x32_bf16 v[68:71], a[120:123], v[112:115], v[68:71]\n\t" \
  "v_rcp_f32 v135, v135\n\t" \
  "v_mfma_f32_16x16x32_bf16 v[72:75], a[184:187], v[112:115], v[72:75]\n\t" \
  "v_mul_f32 v130, v130, v128\n\t" \
  "v_mul_f32 v131, v131, v129\n\t" \
  "v_mfma_f32_16x16x32_bf16 v[64:67], a[60:63], v[116:119], v[64:67]\n\t" \
  "v_mul_f32 v130, v130, v134\n\t" \
  "v_mul_f32 v131, v131, v135\n\t" \
  "v_mfma_f32_16x16x32_bf16 v[68:71], a[124:127], v[116:119], v[68:71]\n\t" \
  "v_cvt_pk_bf16_f32 v182, v130, v131\n\t" \
  "v_mfma_f32_16x16x32_bf16 v[72:75], a[188:191], v[116:119], v[72:75]\n\t" \
  "s_nop 0\n\t" \
  "v_permlane16_swap_b32 v182, v183\n\t" \
  "buffer_store_dwordx4 v[180:183], v52, s[4:7], s94 offen offset:0 sc1\n\t" \
  "s_waitcnt lgkmcnt(0)\n\t" \
  "v_mfma_f32_16x16x32_bf16 v[76:79], a[0:3], v[88:91], 0\n\t" \
  "ds_read_b128 v[104:107], v32 offset:53248\n\t" \
  "s_waitcnt vmcnt(5)\n\t" \
  "v_fma_f32 v128, v168, v120, v64\n\t" \
  "v_mfma_f32_16x16x32_bf16 v[80:83], a[64:67], v[88:91], 0\n\t" \
  "ds_read_b128 v[108:111], v32 offset:54272\n\t" \
  "v_add_f32 v184, v184, v186\n\t" \
  "v_fma_f32 v129, v169, v120, v65\n\t" \
  "v_mfma_f32_16x16x32_bf16 v[84:87], a[128:131], v[88:91], 0\n\t" \
  "v_add_f32 v185, v185, v187\n\t" \
  "v_mul_f32 v132, v128, v128\n\t" \
  "v_mul_f32 v133, v129, v129\n\t" \
  "v_mfma_f32_16x16x32_bf16 v[76:79], a[4:7], v[92:95], v[76:79]\n\t" \
  "ds_read_b128 v[112:115], v32 offset:55296\n\t" \
  "v_add_f32 v188, v188, v190\n\t" \
  "v_fma_f32 v132, v132, v122, v121\n\t" \
  "v_mfma_f32_16x16x32_bf16 v[80:83], a[68:71], v[92:95], v[80:83]\n\t" \
  "ds_read_b128 v[116:119], v32 offset:56320\n\t" \
  "v_add_f32 v189, v189, v191\n\t" \
  "v_fma_f32 v133, v133, v122, v121\n\t" \
  "v_mfma_f32_16x16x32_bf16 v[84:87], a[132:135], v[92:95], v[84:87]\n\t" \
  "v_add_f32 v184, v184, v188\n\t" \
  "v_mul_f32 v132, v132, v128\n\t" \
  "v_mul_f32 v133, v133, v129\n\t" \
  "v_mfma_f32_16x16x32_bf16 v[76:79], a[8:11], v[96:99], v[76:79]\n\t" \
  "v_add_f32 v185, v185, v189\n\t" \
  "v_exp_f32 v134, v132\n\t" \
  "v_mfma_f32_16x16x32_bf16 v[80:83], a[72:75], v[96:99], v[80:83]\n\t" \
  "ds_write_b64 v47, v[184:185] offset:2048\n\t" \
  "v_exp_f32 v135, v133\n\t" \
  "v_mfma_f32_16x16x32_bf16 v[84:87], a[136:139], v[96:99], v[84:87]\n\t" \
  "s_add_u32 m0, s46, 98304\n\t" \
  "v_fma_f32 v130, v170, v120, v66\n\t" \
  "v_fma_f32 v131, v171, v120, v67\n\t" \
  "v_mfma_f32_16x16x32_bf16 v[76:79], a[12:15], v[100:103], v[76:79]\n\t" \
  "s_add_u32 s89, s45, 49152\n\t" \
  "v_fma_f32 v134, v134, v123, v123\n\t" \
  "v_fma_f32 v135, v135, v123, v123\n\t" \
  "v_mfma_f32_16x16x32_bf16 v[80:83], a[76:79], v[100:103], v[80:83]\n\t" \
  "buffer_load_dwordx4 v32, s[4:7], s89 offen offset:0 sc1 lds\n\t" \
  "v_rcp_f32 v134, v134\n\t" \
  "v_mfma_f32_16x16x32_bf16 v[84:87], a[140:143], v[100:103], v[84:87]\n\t" \
  "v_rcp_f32 v135, v135\n\t" \
  "s_waitcnt lgkmcnt(1)\n\t" \
  "v_mfma_f32_16x16x32_bf16 v[76:79], a[16:19], v[104:107], v[76:79]\n\t" \
  "ds_read_b128 v[88:91], v32 offset:57344\n\t" \
  "v_mul_f32 v130, v130, v128\n\t" \
  "v_mul_f32 v131, v131, v129\n\t" \
  "v_mfma_f32_16x16x32_bf16 v[80:83], a[80:83], v[104:107], v[80:83]\n\t" \
  "ds_read_b128 v[92:95], v32 offset:58368\n\t" \
  "buffer_load_dwordx4 v32, s[4:7], s89 offen offset:1024 sc1 lds\n\t" \
  "v_mul_f32 v130, v130, v134\n\t" \
  "v_mfma_f32_16x16x32_bf16 v[84:87], a[144:147], v[104:107], v[84:87]\n\t" \
  "v_mul_f32 v131, v131, v135\n\t" \
  "v_cvt_pk_bf16_f32 v180, v130, v131\n\t" \
  "v_fma_f32 v136, v172, v120, v68\n\t" \
  "v_mfma_f32_16x16x32_bf16 v[76:79], a[20:23], v[108:111], v[76:79]\n\t" \
  "ds_read_b128 v[96:99], v32 offset:59392\n\t" \
  "v_fma_f32 v137, v173, v120, v69\n\t" \
  "v_mul_f32 v140, v136, v136\n\t" \
  "v_mfma_f32_16x16x32_bf16 v[80:83], a[84:87], v[108:111], v[80:83]\n\t" \
  "ds_read_b128 v[100:103], v32 offset:60416\n\t" \
  "buffer_load_dwordx4 v32, s[4:7], s89 offen offset:2048 sc1 lds\n\t" \
  "v_mul_f32 v141, v137, v137\n\t" \
  "v_mfma_f32_16x16x32_bf16 v[84:87], a[148:151], v[108:111], v[84:87]\n\t" \
  "v_fma_f32 v140, v140, v122, v121\n\t" \
  "v_fma_f32 v141, v141, v122, v121\n\t" \
  "v_mul_f32 v140, v140, v136\n\t" \
  "v_mfma_f32_16x16x32_bf16 v[76:79], a[24:27], v[112:115], v[76:79]\n\t" \
  "v_mul_f32 v141, v141, v137\n\t" \
  "v_exp_f32 v142, v140\n\t" \
  "v_mfma_f32_16x16x32_bf16 v[80:83], a[88:91], v[112:115], v[80:83]\n\t" \
  "buffer_load_dwordx4 v32, s[4:7], s89 offen offset:3072 sc1 lds\n\t" \
  "v_exp_f32 v143, v141\n\t" \
  "v_mfma_f32_16x16x32_bf16 v[84:87], a[152:155], v[112:115], v[84:87]\n\t" \
  "s_add_u32 m0, s46, 114688\n\t" \
  "v_fma_f32 v138, v174, v120, v70\n\t" \
  "v_fma_f32 v139, v175, v120, v71\n\t" \
  "v_mfma_f32_16x16x32_bf16 v[76:79], a[28:31], v[116:119], v[76:79]\n\t" \
  "s_add_u32 s89, s45, 114688\n\t" \
  "v_fma_f32 v142, v142, v123, v123\n\t" \
  "v_fma_f32 v143, v143, v123, v123\n\t" \
  "v_mfma_f32_16x16x32_bf16 v[80:83], a[92:95], v[116:119], v[80:83]\n\t" \
  "buffer_load_dwordx4 v32, s[4:7], s89 offen offset:0 sc1 lds\n\t" \
  "v_fma_f32 v128, v176, v120, v72\n\t" \
  "v_mfma_f32_16x16x32_bf16 v[84:87], a[156:159], v[116:119], v[84:87]\n\t" \
  "v_rcp_f32 v142, v142\n\t" \
  "s_waitcnt lgkmcnt(0)\n\t" \
  "v_mfma_f32_16x16x32_bf16 v[76:79], a[32:35], v[88:91], v[76:79]\n\t" \
  "ds_read_b128 v[104:107], v32 offset:61440\n\t" \
  "v_fma_f32 v129, v177, v120, v73\n\t" \
  "v_mfma_f32_16x16x32_bf16 v[80:83], a[96:99], v[88:91], v[80:83]\n\t" \
  "ds_read_b128 v[108:111], v32 offset:62464\n\t" \
  "buffer_load_dwordx4 v32, s[4:7], s89 offen offset:1024 sc1 lds\n\t" \
  "v_mul_f32 v132, v128, v128\n\t" \
  "v_mfma_f32_16x16x32_bf16 v[84:87], a[160:163], v[88:91], v[84:87]\n\t" \
  "v_rcp_f32 v143, v143\n\t" \
  "v_mfma_f32_16x16x32_bf16 v[76:79], a[36:39], v[92:95], v[76:79]\n\t" \
  "ds_read_b128 v[112:115], v32 offset:63488\n\t" \
  "v_mul_f32 v138, v138, v136\n\t" \
  "v_mfma_f32_16x16x32_bf16 v[80:83], a[100:103], v[92:95], v[80:83]\n\t" \
  "ds_read_b128 v[116:119], v32 offset:64512\n\t" \
  "buffer_load_dwordx4 v32, s[4:7], s89 offen offset:2048 sc1 lds\n\t" \
  "v_mul_f32 v139, v139, v137\n\t" \
  "v_mfma_f32_16x16x32_bf16 v[84:87], a[164:167], v[92:95], v[84:87]\n\t" \
  "v_mul_f32 v138, v138, v142\n\t" \
  "v_mul_f32 v139, v139, v143\n\t" \
  "v_mfma_f32_16x16x32_bf16 v[76:79], a[40:43], v[96:99], v[76:79]\n\t" \
  "v_cvt_pk_bf16_f32 v181, v138, v139\n\t" \
  "v_mul_f32 v133, v129, v129\n\t" \
  "v_mfma_f32_16x16x32_bf16 v[80:83], a[104:107], v[96:99], v[80:83]\n\t" \
  "buffer_load_dwordx4 v32, s[4:7], s89 offen offset:3072 sc1 lds\n\t" \
  "v_fma_f32 v132, v132, v122, v121\n\t" \
  "v_mfma_f32_16x16x32_bf16 v[84:87], a[168:171], v[96:99], v[84:87]\n\t" \
  "v_fma_f32 v133, v133, v122, v121\n\t" \
  "v_mul_f32 v132, v132, v128\n\t" \
  "v_mfma_f32_16x16x32_bf16 v[76:79], a[44:47], v[100:103], v[76:79]\n\t" \
  "v_mul_f32 v133, v133, v129\n\t" \
  "v_permlane32_swap_b32 v180, v183\n\t" \
  "v_mfma_f32_16x16x32_bf16 v[80:83], a[108:111], v[100:103], v[80:83]\n\t" \
  "v_exp_f32 v134, v132\n\t" \
  "v_mfma_f32_16x16x32_bf16 v[84:87], a[172:175], v[100:103], v[84:87]\n\t" \
  "s_add_u32 s94, s48, 45056\n\t" \
  "s_waitcnt lgkmcnt(0)\n\t" \
  "v_mfma_f32_16x16x32_bf16 v[76:79], a[48:51], v[104:107], v[76:79]\n\t" \
  "s_waitcnt vmcnt(9)\n\t" \
  "s_barrier\n\t" \
  "ds_read_b128 v[88:91], v51 offset:0\n\t" \
  "v_mfma_f32_16x16x32_bf16 v[80:83], a[112:115], v[104:107], v[80:83]\n\t" \
  "ds_read_b128 v[92:95], v51 offset:1024\n\t" \
  "v_permlane16_swap_b32 v181, v180\n\t" \
  "v_mfma_f32_16x16x32_bf16 v[84:87], a[176:179], v[104:107], v[84:87]\n\t" \
  "v_exp_f32 v135, v133\n\t" \
  "v_mfma_f32_16x16x32_bf16 v[76:79], a[52:55], v[108:111], v[76:79]\n\t" \
  "ds_read_b128 v[96:99], v51 offset:2048\n\t" \
  "v_fma_f32 v130, v178, v120, v74\n\t" \
  "v_fma_f32 v131, v179, v120, v75\n\t" \
  "v_mfma_f32_16x16x32_bf16 v[80:83], a[116:119], v[108:111], v[80:83]\n\t" \
  "ds_read_b128 v[100:103], v51 offset:3072\n\t" \
  "v_fma_f32 v134, v134, v123, v123\n\t" \
  "v_mfma_f32_16x16x32_bf16 v[84:87], a[180:183], v[108:111], v[84:87]\n\t" \
  "v_fma_f32 v135, v135, v123, v123\n\t" \
  "v_mfma_f32_16x16x32_bf16 v[76:79], a[56:59], v[112:115], v[76:79]\n\t" \
  "ds_read_b128 v[124:127], v50 offset:768\n\t" \
  "v_rcp_f32 v134, v134\n\t" \
  "v_mfma_f32_16x16x32_bf16 v[80:83], a[120:123], v[112:115], v[80:83]\n\t" \
  "v_rcp_f32 v135, v135\n\t" \
  "v_mfma_f32_16x16x32_bf16 v[84:87], a[184:187], v[112:115], v[84:87]\n\t" \
  "v_mul_f32 v130, v130, v128\n\t" \
  "v_mul_f32 v131, v131, v129\n\t" \
  "v_mfma_f32_16x16x32_bf16 v[76:79], a[60:63], v[116:119], v[76:79]\n\t" \
  "v_mul_f32 v130, v130, v134\n\t" \
  "v_mul_f32 v131, v131, v135\n\t" \
  "v_mfma_f32_16x16x32_bf16 v[80:83], a[124:127], v[116:119], v[80:83]\n\t" \
  "v_cvt_pk_bf16_f32 v182, v130, v131\n\t" \
  "v_mfma_f32_16x16x32_bf16 v[84:87], a[188:191], v[116:119], v[84:87]\n\t" \
  "s_nop 0\n\t" \
  "v_permlane16_swap_b32 v182, v183\n\t" \
  "buffer_store_dwordx4 v[180:183], v52, s[4:7], s94 offen offset:0 sc1\n\t" \
  "s_waitcnt lgkmcnt(0)\n\t" \
  "v_mfma_f32_16x16x32_bf16 v[64:67], a[0:3], v[88:91], 0\n\t" \
  "ds_read_b128 v[104:107], v51 offset:4096\n\t" \
  "v_mov_b32 v60, -1\n\t" \
  "ds_read_b128 v[184:187], v48 offset:2048\n\t" \
  "v_mfma_f32_16x16x32_bf16 v[68:71], a[64:67], v[88:91], 0\n\t" \
  "ds_read_b128 v[108:111], v51 offset:5120\n\t" \
  "v_mov_b32 v61, -1\n\t" \
  "ds_read_b128 v[188:191], v48 offset:2064\n\t" \
  "v_mfma_f32_16x16x32_bf16 v[72:75], a[128:131], v[88:91], 0\n\t" \
  "v_mov_b32 v62, -1\n\t" \
  "v_fma_f32 v128, v168, v124, v76\n\t" \
  "v_fma_f32 v129, v169, v124, v77\n\t" \
  "v_mfma_f32_16x16x32_bf16 v[64:67], a[4:7], v[92:95], v[64:67]\n\t" \
  "ds_read_b128 v[112:115], v51 offset:6144\n\t" \
  "v_mov_b32 v63, -1\n\t" \
  "v_mul_f32 v132, v128, v128\n\t" \
  "v_mfma_f32_16x16x32_bf16 v[68:71], a[68:71], v[92:95], v[68:71]\n\t" \
  "ds_read_b128 v[116:119], v51 offset:7168\n\t" \
  "s_add_u32 s95, s48, 90112\n\t" \
  "v_mul_f32 v133, v129, v129\n\t" \
  "v_mfma_f32_16x16x32_bf16 v[72:75], a[132:135], v[92:95], v[72:75]\n\t" \
  "buffer_store_dwordx4 v[60:63], v52, s[4:7], s95 offen offset:0 sc1\n\t" \
  "v_fma_f32 v132, v132, v126, v125\n\t" \
  "v_fma_f32 v133, v133, v126, v125\n\t" \
  "v_mfma_f32_16x16x32_bf16 v[64:67], a[8:11], v[96:99], v[64:67]\n\t" \
  "s_add_u32 s95, s48, 270336\n\t" \
  "v_mul_f32 v132, v132, v128\n\t" \
  "v_mul_f32 v133, v133, v129\n\t" \
  "v_mfma_f32_16x16x32_bf16 v[68:71], a[72:75], v[96:99], v[68:71]\n\t" \
  "v_exp_f32 v134, v132\n\t" \
  "v_fma_f32 v136, v172, v124, v80\n\t" \
  "v_mfma_f32_16x16x32_bf16 v[72:75], a[136:139], v[96:99], v[72:75]\n\t" \
  "s_waitcnt lgkmcnt(2)\n\t" \
  "v_add_f32 v184, v184, v186\n\t" \
  "v_add_f32 v185, v185, v187\n\t" \
  "v_mfma_f32_16x16x32_bf16 v[64:67], a[12:15], v[100:103], v[64:67]\n\t" \
  "buffer_store_dwordx4 v[60:63], v52, s[4:7], s95 offen offset:0 sc1\n\t" \
  "v_add_f32 v188, v188, v190\n\t" \
  "v_add_f32 v189, v189, v191\n\t" \
  "v_mfma_f32_16x16x32_bf16 v[68:71], a[76:79], v[100:103], v[68:71]\n\t" \
  "s_add_u32 s95, s48, 135168\n\t" \
  "v_add_f32 v184, v184, v188\n\t" \
  "v_add_f32 v185, v185, v189\n\t" \
  "v_mfma_f32_16x16x32_bf16 v[72:75], a[140:143], v[100:103], v[72:75]\n\t" \
  "v_mul_f32 v184, 0xbb000000, v184\n\t" \
  "v_mul_f32 v187, 0x3b000000, v185\n\t" \
  "s_waitcnt lgkmcnt(0)\n\t" \
  "v_mfma_f32_16x16x32_bf16 v[64:67], a[16:19], v[104:107], v[64:67]\n\t" \
  "ds_read_b128 v[88:91], v51 offset:8192\n\t" \
  "v_fma_f32 v187, -v184, v184, v187\n\t" \
  "v_max_f32 v187, 0, v187\n\t" \
  "v_mfma_f32_16x16x32_bf16 v[68:71], a[80:83], v[104:107], v[68:71]\n\t" \
  "ds_read_b128 v[92:95], v51 offset:9216\n\t" \
  "buffer_store_dwordx4 v[60:63], v52, s[4:7], s95 offen offset:0 sc1\n\t" \
  "v_add_f32 v187, 0x3727c5ac, v187\n\t" \
  "v_mfma_f32_16x16x32_bf16 v[72:75], a[144:147], v[104:107], v[72:75]\n\t" \
  "s_add_u32 s95, s48, 315392\n\t" \
  "v_rsq_f32 v188, v187\n\t" \
  "v_mfma_f32_16x16x32_bf16 v[64:67], a[20:23], v[108:111], v[64:67]\n\t" \
  "ds_read_b128 v[96:99], v51 offset:10240\n\t" \
  "s_nop 0\n\t" \
  "v_mul_f32 v185, 0xc0135761, v188\n\t" \
  "v_mfma_f32_16x16x32_bf16 v[68:71], a[84:87], v[108:111], v[68:71]\n\t" \
  "ds_read_b128 v[100:103], v51 offset:11264\n\t" \
  "v_mul_f32 v188, v188, v188\n\t" \
  "v_mul_f32 v186, 0x3d372713, v185\n\t" \
  "v_mfma_f32_16x16x32_bf16 v[72:75], a[148:151], v[108:111], v[72:75]\n\t" \
  "buffer_store_dwordx4 v[60:63], v52, s[4:7], s95 offen offset:0 sc1\n\t" \
  "v_mul_f32 v186, v186, v188\n\t" \
  "ds_write_b128 v49, v[184:187] offset:1024\n\t" \
  "v_mfma_f32_16x16x32_bf16 v[64:67], a[24:27], v[112:115], v[64:67]\n\t" \
  "s_add_u32 s89, s50, 8192\n\t" \
  "v_exp_f32 v135, v133\n\t" \
  "v_mfma_f32_16x16x32_bf16 v[68:71], a[88:91], v[112:115], v[68:71]\n\t" \
  "v_fma_f32 v130, v170, v124, v78\n\t" \
  "v_fma_f32 v131, v171, v124, v79\n\t" \
  "v_fma_f32 v134, v134, v127, v127\n\t" \
  "v_mfma_f32_16x16x32_bf16 v[72:75], a[152:155], v[112:115], v[72:75]\n\t" \
  "v_fma_f32 v135, v135, v127, v127\n\t" \
  "v_rcp_f32 v134, v134\n\t" \
  "v_mfma_f32_16x16x32_bf16 v[64:67], a[28:31], v[116:119], v[64:67]\n\t" \
  "buffer_load_dwordx4 a[224:227], v33, s[4:7], s89 offen offset:0 sc1\n\t" \
  "v_rcp_f32 v135, v135\n\t" \
  "v_mfma_f32_16x16x32_bf16 v[68:71], a[92:95], v[116:119], v[68:71]\n\t" \
  "v_mul_f32 v130, v130, v128\n\t" \
  "v_mul_f32 v131, v131, v129\n\t" \
  "v_mul_f32 v130, v130, v134\n\t" \
  "v_mfma_f32_16x16x32_bf16 v[72:75], a[156:159], v[116:119], v[72:75]\n\t" \
  "v_mul_f32 v131, v131, v135\n\t" \
  "v_cvt_pk_bf16_f32 v180, v130, v131\n\t" \
  "s_waitcnt lgkmcnt(1)\n\t" \
  "v_mfma_f32_16x16x32_bf16 v[64:67], a[32:35], v[88:91], v[64:67]\n\t" \
  "ds_read_b128 v[104:107], v51 offset:12288\n\t" \
  "v_fma_f32 v137, v173, v124, v81\n\t" \
  "v_mul_f32 v140, v136, v136\n\t" \
  "v_mfma_f32_16x16x32_bf16 v[68:71], a[96:99], v[88:91], v[68:71]\n\t" \
  "ds_read_b128 v[108:111], v51 offset:13312\n\t" \
  "buffer_load_dwordx4 a[228:231], v33, s[4:7], s89 offen offset:1024 sc1\n\t" \
  "v_mul_f32 v141, v137, v137\n\t" \
  "v_mfma_f32_16x16x32_bf16 v[72:75], a[160:163], v[88:91], v[72:75]\n\t" \
  "v_fma_f32 v140, v140, v126, v125\n\t" \
  "v_fma_f32 v141, v141, v126, v125\n\t" \
  "v_mul_f32 v140, v140, v136\n\t" \
  "v_mfma_f32_16x16x32_bf16 v[64:67], a[36:39], v[92:95], v[64:67]\n\t" \
  "ds_read_b128 v[112:115], v51 offset:14336\n\t" \
  "v_mul_f32 v141, v141, v137\n\t" \
  "v_fma_f32 v128, v176, v124, v84\n\t" \
  "v_mfma_f32_16x16x32_bf16 v[68:71], a[100:103], v[92:95], v[68:71]\n\t" \
  "ds_read_b128 v[116:119], v51 offset:15360\n\t" \
  "v_exp_f32 v142, v140\n\t" \
  "v_mfma_f32_16x16x32_bf16 v[72:75], a[164:167], v[92:95], v[72:75]\n\t" \
  "buffer_load_dwordx4 a[232:235], v34, s[4:7], s89 offen offset:0 sc1\n\t" \
  "v_exp_f32 v143, v141\n\t" \
  "v_mfma_f32_16x16x32_bf16 v[64:67], a[40:43], v[96:99], v[64:67]\n\t" \
  "v_fma_f32 v138, v174, v124, v82\n\t" \
  "v_fma_f32 v139, v175, v124, v83\n\t" \
  "v_fma_f32 v142, v142, v127, v127\n\t" \
  "v_mfma_f32_16x16x32_bf16 v[68:71], a[104:107], v[96:99], v[68:71]\n\t" \
  "v_fma_f32 v143, v143, v127, v127\n\t" \
  "v_rcp_f32 v142, v142\n\t" \
  "v_mfma_f32_16x16x32_bf16 v[72:75], a[168:171], v[96:99], v[72:75]\n\t" \
  "v_rcp_f32 v143, v143\n\t" \
  "v_mul_f32 v138, v138, v136\n\t" \
  "v_mfma_f32_16x16x32_bf16 v[64:67], a[44:47], v[100:103], v[64:67]\n\t" \
  "buffer_load_dwordx4 a[236:239], v34, s[4:7], s89 offen offset:1024 sc1\n\t" \
  "v_mul_f32 v139, v139, v137\n\t" \
  "v_mul_f32 v138, v138, v142\n\t" \
  "v_mfma_f32_16x16x32_bf16 v[68:71], a[108:111], v[100:103], v[68:71]\n\t" \
  "s_add_u32 s89, s50, 12288\n\t" \
  "v_mul_f32 v139, v139, v143\n\t" \
  "v_cvt_pk_bf16_f32 v181, v138, v139\n\t" \
  "v_mfma_f32_16x16x32_bf16 v[72:75], a[172:175], v[100:103], v[72:75]\n\t" \
  "v_fma_f32 v129, v177, v124, v85\n\t" \
  "v_mul_f32 v132, v128, v128\n\t" \
  "s_waitcnt lgkmcnt(0)\n\t" \
  "v_mfma_f32_16x16x32_bf16 v[64:67], a[48:51], v[104:107], v[64:67]\n\t" \
  "s_waitcnt vmcnt(9)\n\t" \
  "s_barrier\n\t" \
  "ds_read_b128 v[88:91], v51 offset:16384\n\t" \
  "v_mul_f32 v133, v129, v129\n\t" \
  "v_mfma_f32_16x16x32_bf16 v[68:71], a[112:115], v[104:107], v[68:71]\n\t" \
  "ds_read_b128 v[92:95], v51 offset:17408\n\t" \
  "buffer_load_dwordx4 a[240:243], v33, s[4:7], s89 offen offset:0 sc1\n\t" \
  "v_fma_f32 v132, v132, v126, v125\n\t" \
  "v_mfma_f32_16x16x32_bf16 v[72:75], a[176:179], v[104:107], v[72:75]\n\t" \
  "v_fma_f32 v133, v133, v126, v125\n\t" \
  "v_mul_f32 v132, v132, v128\n\t" \
  "v_mul_f32 v133, v133, v129\n\t" \
  "v_mfma_f32_16x16x32_bf16 v[64:67], a[52:55], v[108:111], v[64:67]\n\t" \
  "ds_read_b128 v[96:99], v51 offset:18432\n\t" \
  "v_exp_f32 v134, v132\n\t" \
  "v_mfma_f32_16x16x32_bf16 v[68:71], a[116:119], v[108:111], v[68:71]\n\t" \
  "ds_read_b128 v[100:103], v51 offset:19456\n\t" \
  "v_exp_f32 v135, v133\n\t" \
  "v_mfma_f32_16x16x32_bf16 v[72:75], a[180:183], v[108:111], v[72:75]\n\t" \
  "buffer_load_dwordx4 a[244:247], v33, s[4:7], s89 offen offset:1024 sc1\n\t" \
  "v_fma_f32 v130, v178, v124, v86\n\t" \
  "v_fma_f32 v131, v179, v124, v87\n\t" \
  "v_mfma_f32_16x16x32_bf16 v[64:67], a[56:59], v[112:115], v[64:67]\n\t" \
  "ds_read_b128 v[120:123], v50 offset:1024\n\t" \
  "v_fma_f32 v134, v134, v127, v127\n\t" \
  "v_fma_f32 v135, v135, v127, v127\n\t" \
  "v_mfma_f32_16x16x32_bf16 v[68:71], a[120:123], v[112:115], v[68:71]\n\t" \
  "v_rcp_f32 v134, v134\n\t" \
  "v_permlane32_swap_b32 v180, v183\n\t" \
  "v_mfma_f32_16x16x32_bf16 v[72:75], a[184:187], v[112:115], v[72:75]\n\t" \
  "v_rcp_f32 v135, v135\n\t" \
  "v_mul_f32 v130, v130, v128\n\t" \
  "v_mfma_f32_16x16x32_bf16 v[64:67], a[60:63], v[116:119], v[64:67]\n\t" \
  "buffer_load_dwordx4 a[248:251], v34, s[4:7], s89 offen offset:0 sc1\n\t" \
  "v_mul_f32 v131, v131, v129\n\t" \
  "v_mul_f32 v130, v130, v134\n\t" \
  "v_mfma_f32_16x16x32_bf16 v[68:71], a[124:127], v[116:119], v[68:71]\n\t" \
  "v_mul_f32 v131, v131, v135\n\t" \
  "v_cvt_pk_bf16_f32 v182, v130, v131\n\t" \
  "s_add_u32 s94, s48, 225280\n\t" \
  "v_mfma_f32_16x16x32_bf16 v[72:75], a[188:191], v[116:119], v[72:75]\n\t" \
  "v_permlane16_swap_b32 v181, v180\n\t" \
  "v_permlane16_swap_b32 v182, v183\n\t" \
  "buffer_store_dwordx4 v[180:183], v52, s[4:7], s94 offen offset:0 sc1\n\t" \
  "buffer_load_dwordx4 a[252:255], v34, s[4:7], s89 offen offset:1024 sc1\n\t" \
  "s_waitcnt lgkmcnt(0)\n\t" \
  "v_mfma_f32_16x16x32_bf16 v[76:79], a[0:3], v[88:91], 0\n\t" \
  "ds_read_b128 v[104:107], v51 offset:20480\n\t" \
  "s_add_u32 s89, s50, 16384\n\t" \
  "v_fma_f32 v128, v168, v120, v64\n\t" \
  "v_mfma_f32_16x16x32_bf16 v[80:83], a[64:67], v[88:91], 0\n\t" \
  "ds_read_b128 v[108:111], v51 offset:21504\n\t" \
  "buffer_load_dwordx4 v[144:147], v33, s[4:7], s89 offen offset:0 sc1\n\t" \
  "v_fma_f32 v129, v169, v120, v65\n\t" \
  "v_mfma_f32_16x16x32_bf16 v[84:87], a[128:131], v[88:91], 0\n\t" \
  "v_mul_f32 v132, v128, v128\n\t" \
  "v_mul_f32 v133, v129, v129\n\t" \
  "v_fma_f32 v132, v132, v122, v121\n\t" \
  "v_mfma_f32_16x16x32_bf16 v[76:79], a[4:7], v[92:95], v[76:79]\n\t" \
  "ds_read_b128 v[112:115], v51 offset:22528\n\t" \
  "v_fma_f32 v133, v133, v122, v121\n\t" \
  "v_mul_f32 v132, v132, v128\n\t" \
  "v_mfma_f32_16x16x32_bf16 v[80:83], a[68:71], v[92:95], v[80:83]\n\t" \
  "ds_read_b128 v[116:119], v51 offset:23552\n\t" \
  "v_mul_f32 v133, v133, v129\n\t" \
  "v_fma_f32 v136, v172, v120, v68\n\t" \
  "v_mfma_f32_16x16x32_bf16 v[84:87], a[132:135], v[92:95], v[84:87]\n\t" \
  "buffer_load_dwordx4 v[148:151], v33, s[4:7], s89 offen offset:1024 sc1\n\t" \
  "v_exp_f32 v134, v132\n\t" \
  "v_mfma_f32_16x16x32_bf16 v[76:79], a[8:11], v[96:99], v[76:79]\n\t" \
  "v_exp_f32 v135, v133\n\t" \
  "v_fma_f32 v130, v170, v120, v66\n\t" \
  "v_mfma_f32_16x16x32_bf16 v[80:83], a[72:75], v[96:99], v[80:83]\n\t" \
  "v_fma_f32 v131, v171, v120, v67\n\t" \
  "v_fma_f32 v134, v134, v123, v123\n\t" \
  "v_fma_f32 v135, v135, v123, v123\n\t" \
  "v_mfma_f32_16x16x32_bf16 v[84:87], a[136:139], v[96:99], v[84:87]\n\t" \
  "v_rcp_f32 v134, v134\n\t" \
  "v_fma_f32 v137, v173, v120, v69\n\t" \
  "v_mfma_f32_16x16x32_bf16 v[76:79], a[12:15], v[100:103], v[76:79]\n\t" \
  "buffer_load_dwordx4 v[152:155], v34, s[4:7], s89 offen offset:0 sc1\n\t" \
  "v_rcp_f32 v135, v135\n\t" \
  "v_mfma_f32_16x16x32_bf16 v[80:83], a[76:79], v[100:103], v[80:83]\n\t" \
  "v_mul_f32 v130, v130, v128\n\t" \
  "v_mul_f32 v131, v131, v129\n\t" \
  "v_mul_f32 v130, v130, v134\n\t" \
  "v_mfma_f32_16x16x32_bf16 v[84:87], a[140:143], v[100:103], v[84:87]\n\t" \
  "v_mul_f32 v131, v131, v135\n\t" \
  "v_cvt_pk_bf16_f32 v180, v130, v131\n\t" \
  "s_waitcnt lgkmcnt(0)\n\t" \
  "v_mfma_f32_16x16x32_bf16 v[76:79], a[16:19], v[104:107], v[76:79]\n\t" \
  "ds_read_b128 v[88:91], v51 offset:24576\n\t" \
  "v_mul_f32 v140, v136, v136\n\t" \
  "v_mul_f32 v141, v137, v137\n\t" \
  "v_mfma_f32_16x16x32_bf16 v[80:83], a[80:83], v[104:107], v[80:83]\n\t" \
  "ds_read_b128 v[92:95], v51 offset:25600\n\t" \
  "buffer_load_dwordx4 v[156:159], v34, s[4:7], s89 offen offset:1024 sc1\n\t" \
  "v_fma_f32 v140, v140, v122, v121\n\t" \
  "v_mfma_f32_16x16x32_bf16 v[84:87], a[144:147], v[104:107], v[84:87]\n\t" \
  "s_add_u32 s89, s50, 20480\n\t" \
  "v_fma_f32 v141, v141, v122, v121\n\t" \
  "v_mul_f32 v140, v140, v136\n\t" \
  "v_mfma_f32_16x16x32_bf16 v[76:79], a[20:23], v[108:111], v[76:79]\n\t" \
  "ds_read_b128 v[96:99], v51 offset:26624\n\t" \
  "v_mul_f32 v141, v141, v137\n\t" \
  "v_fma_f32 v128, v176, v120, v72\n\t" \
  "v_mfma_f32_16x16x32_bf16 v[80:83], a[84:87], v[108:111], v[80:83]\n\t" \
  "ds_read_b128 v[100:103], v51 offset:27648\n\t" \
  "v_fma_f32 v129, v177, v120, v73\n\t" \
  "v_mfma_f32_16x16x32_bf16 v[84:87], a[148:151], v[108:111], v[84:87]\n\t" \
  "buffer_load_dwordx4 v[160:163], v33, s[4:7], s89 offen offset:0 sc1\n\t" \
  "v_mul_f32 v132, v128, v128\n\t" \
  "v_mfma_f32_16x16x32_bf16 v[76:79], a[24:27], v[112:115], v[76:79]\n\t" \
  "v_exp_f32 v142, v140\n\t" \
  "v_mfma_f32_16x16x32_bf16 v[80:83], a[88:91], v[112:115], v[80:83]\n\t" \
  "v_exp_f32 v143, v141\n\t" \
  "v_mfma_f32_16x16x32_bf16 v[84:87], a[152:155], v[112:115], v[84:87]\n\t" \
  "v_fma_f32 v138, v174, v120, v70\n\t" \
  "v_fma_f32 v139, v175, v120, v71\n\t" \
  "v_mfma_f32_16x16x32_bf16 v[76:79], a[28:31], v[116:119], v[76:79]\n\t" \
  "buffer_load_dwordx4 v[164:167], v33, s[4:7], s89 offen offset:1024 sc1\n\t" \
  "v_fma_f32 v142, v142, v123, v123\n\t" \
  "v_mfma_f32_16x16x32_bf16 v[80:83], a[92:95], v[116:119], v[80:83]\n\t" \
  "v_fma_f32 v143, v143, v123, v123\n\t" \
  "v_mul_f32 v133, v129, v129\n\t" \
  "v_mfma_f32_16x16x32_bf16 v[84:87], a[156:159], v[116:119], v[84:87]\n\t" \
  "v_fma_f32 v132, v132, v122, v121\n\t" \
  "s_waitcnt lgkmcnt(0)\n\t" \
  "v_mfma_f32_16x16x32_bf16 v[76:79], a[32:35], v[88:91], v[76:79]\n\t" \
  "ds_read_b128 v[104:107], v51 offset:28672\n\t" \
  "v_fma_f32 v133, v133, v122, v121\n\t" \
  "v_mfma_f32_16x16x32_bf16 v[80:83], a[96:99], v[88:91], v[80:83]\n\t" \
  "ds_read_b128 v[108:111], v51 offset:29696\n\t" \
  "buffer_load_dwordx4 v[0:3], v34, s[4:7], s89 offen offset:0 sc1\n\t" \
  "v_mfma_f32_16x16x32_bf16 v[84:87], a[160:163], v[88:91], v[84:87]\n\t" \
  "v_rcp_f32 v142, v142\n\t" \
  "v_mfma_f32_16x16x32_bf16 v[76:79], a[36:39], v[92:95], v[76:79]\n\t" \
  "ds_read_b128 v[112:115], v51 offset:30720\n\t" \
  "v_mul_f32 v132, v132, v128\n\t" \
  "v_mfma_f32_16x16x32_bf16 v[80:83], a[100:103], v[92:95], v[80:83]\n\t" \
  "ds_read_b128 v[116:119], v51 offset:31744\n\t" \
  "v_rcp_f32 v143, v143\n\t" \
  "v_mfma_f32_16x16x32_bf16 v[84:87], a[164:167], v[92:95], v[84:87]\n\t" \
  "buffer_load_dwordx4 v[4:7], v34, s[4:7], s89 offen offset:1024 sc1\n\t" \
  "v_mul_f32 v138, v138, v136\n\t" \
  "v_mfma_f32_16x16x32_bf16 v[76:79], a[40:43], v[96:99], v[76:79]\n\t" \
  "s_waitcnt vmcnt(9)\n\t" \
  "v_mul_f32 v139, v139, v137\n\t" \
  "v_mfma_f32_16x16x32_bf16 v[80:83], a[104:107], v[96:99], v[80:83]\n\t" \
  "v_mul_f32 v138, v138, v142\n\t" \
  "v_mul_f32 v139, v139, v143\n\t" \
  "v_mfma_f32_16x16x32_bf16 v[84:87], a[168:171], v[96:99], v[84:87]\n\t" \
  "v_cvt_pk_bf16_f32 v181, v138, v139\n\t" \
  "v_mul_f32 v133, v133, v129\n\t" \
  "v_mfma_f32_16x16x32_bf16 v[76:79], a[44:47], v[100:103], v[76:79]\n\t" \
  "buffer_store_dwordx2 v[56:57], v55, s[4:7], s49 offen offset:0 sc1\n\t" \
  "v_permlane32_swap_b32 v180, v183\n\t" \
  "v_mfma_f32_16x16x32_bf16 v[80:83], a[108:111], v[100:103], v[80:83]\n\t" \
  "v_exp_f32 v134, v132\n\t" \
  "v_mfma_f32_16x16x32_bf16 v[84:87], a[172:175], v[100:103], v[84:87]\n\t" \
  "s_add_u32 s94, s48, 90112\n\t" \
  "s_waitcnt lgkmcnt(0)\n\t" \
  "v_mfma_f32_16x16x32_bf16 v[76:79], a[48:51], v[104:107], v[76:79]\n\t" \
  "ds_read_b128 v[88:91], v51 offset:32768\n\t" \
  "v_permlane16_swap_b32 v181, v180\n\t" \
  "v_mfma_f32_16x16x32_bf16 v[80:83], a[112:115], v[104:107], v[80:83]\n\t" \
  "ds_read_b128 v[92:95], v51 offset:33792\n\t" \
  "v_mfma_f32_16x16x32_bf16 v[84:87], a[176:179], v[104:107], v[84:87]\n\t" \
  "v_exp_f32 v135, v133\n\t" \
  "v_mfma_f32_16x16x32_bf16 v[76:79], a[52:55], v[108:111], v[76:79]\n\t" \
  "ds_read_b128 v[96:99], v51 offset:34816\n\t" \
  "v_fma_f32 v130, v178, v120, v74\n\t" \
  "v_fma_f32 v131, v179, v120, v75\n\t" \
  "v_mfma_f32_16x16x32_bf16 v[80:83], a[116:119], v[108:111], v[80:83]\n\t" \
  "ds_read_b128 v[100:103], v51 offset:35840\n\t" \
  "v_fma_f32 v134, v134, v123, v123\n\t" \
  "v_mfma_f32_16x16x32_bf16 v[84:87], a[180:183], v[108:111], v[84:87]\n\t" \
  "v_fma_f32 v135, v135, v123, v123\n\t" \
  "v_mfma_f32_16x16x32_bf16 v[76:79], a[56:59], v[112:115], v[76:79]\n\t" \
  "ds_read_b128 v[124:127], v50 offset:1280\n\t" \
  "v_rcp_f32 v134, v134\n\t" \
  "v_mfma_f32_16x16x32_bf16 v[80:83], a[120:123], v[112:115], v[80:83]\n\t" \
  "v_rcp_f32 v135, v135\n\t" \
  "v_mfma_f32_16x16x32_bf16 v[84:87], a[184:187], v[112:115], v[84:87]\n\t" \
  "v_mul_f32 v130, v130, v128\n\t" \
  "v_mul_f32 v131, v131, v129\n\t" \
  "v_mfma_f32_16x16x32_bf16 v[76:79], a[60:63], v[116:119], v[76:79]\n\t" \
  "v_mul_f32 v130, v130, v134\n\t" \
  "v_mul_f32 v131, v131, v135\n\t" \
  "v_mfma_f32_16x16x32_bf16 v[80:83], a[124:127], v[116:119], v[80:83]\n\t" \
  "v_cvt_pk_bf16_f32 v182, v130, v131\n\t" \
  "v_mfma_f32_16x16x32_bf16 v[84:87], a[188:191], v[116:119], v[84:87]\n\t" \
  "s_nop 0\n\t" \
  "v_permlane16_swap_b32 v182, v183\n\t" \
  "buffer_store_dwordx4 v[180:183], v52, s[4:7], s94 offen offset:0 sc1\n\t" \
  "s_waitcnt lgkmcnt(0)\n\t" \
  "v_mfma_f32_16x16x32_bf16 v[64:67], a[0:3], v[88:91], 0\n\t" \
  "ds_read_b128 v[104:107], v51 offset:36864\n\t" \
  "s_add_u32 s89, s50, 40960\n\t" \
  "v_fma_f32 v128, v168, v124, v76\n\t" \
  "v_mfma_f32_16x16x32_bf16 v[68:71], a[64:67], v[88:91], 0\n\t" \
  "ds_read_b128 v[108:111], v51 offset:37888\n\t" \
  "buffer_load_dwordx4 v[8:11], v33, s[4:7], s89 offen offset:0 sc1\n\t" \
  "v_fma_f32 v129, v169, v124, v77\n\t" \
  "v_mfma_f32_16x16x32_bf16 v[72:75], a[128:131], v[88:91], 0\n\t" \
  "v_mul_f32 v132, v128, v128\n\t" \
  "v_mul_f32 v133, v129, v129\n\t" \
  "v_fma_f32 v132, v132, v126, v125\n\t" \
  "v_mfma_f32_16x16x32_bf16 v[64:67], a[4:7], v[92:95], v[64:67]\n\t" \
  "ds_read_b128 v[112:115], v51 offset:38912\n\t" \
  "v_fma_f32 v133, v133, v126, v125\n\t" \
  "v_mul_f32 v132, v132, v128\n\t" \
  "v_mfma_f32_16x16x32_bf16 v[68:71], a[68:71], v[92:95], v[68:71]\n\t" \
  "ds_read_b128 v[116:119], v51 offset:39936\n\t" \
  "buffer_load_dwordx4 v[12:15], v33, s[4:7], s89 offen offset:1024 sc1\n\t" \
  "v_mul_f32 v133, v133, v129\n\t" \
  "v_mfma_f32_16x16x32_bf16 v[72:75], a[132:135], v[92:95], v[72:75]\n\t" \
  "v_exp_f32 v134, v132\n\t" \
  "v_fma_f32 v136, v172, v124, v80\n\t" \
  "v_mfma_f32_16x16x32_bf16 v[64:67], a[8:11], v[96:99], v[64:67]\n\t" \
  "v_exp_f32 v135, v133\n\t" \
  "v_fma_f32 v130, v170, v124, v78\n\t" \
  "v_mfma_f32_16x16x32_bf16 v[68:71], a[72:75], v[96:99], v[68:71]\n\t" \
  "buffer_load_dwordx4 v[16:19], v34, s[4:7], s89 offen offset:0 sc1\n\t" \
  "v_fma_f32 v131, v171, v124, v79\n\t" \
  "v_fma_f32 v134, v134, v127, v127\n\t" \
  "v_mfma_f32_16x16x32_bf16 v[72:75], a[136:139], v[96:99], v[72:75]\n\t" \
  "v_fma_f32 v135, v135, v127, v127\n\t" \
  "v_fma_f32 v137, v173, v124, v81\n\t" \
  "v_mfma_f32_16x16x32_bf16 v[64:67], a[12:15], v[100:103], v[64:67]\n\t" \
  "v_rcp_f32 v134, v134\n\t" \
  "v_mfma_f32_16x16x32_bf16 v[68:71], a[76:79], v[100:103], v[68:71]\n\t" \
  "buffer_load_dwordx4 v[20:23], v34, s[4:7], s89 offen offset:1024 sc1\n\t" \
  "v_mul_f32 v140, v136, v136\n\t" \
  "v_mfma_f32_16x16x32_bf16 v[72:75], a[140:143], v[100:103], v[72:75]\n\t" \
  "v_mul_f32 v141, v137, v137\n\t" \
  "s_waitcnt lgkmcnt(0)\n\t" \
  "v_mfma_f32_16x16x32_bf16 v[64:67], a[16:19], v[104:107], v[64:67]\n\t" \
  "ds_read_b128 v[88:91], v51 offset:40960\n\t" \
  "v_rcp_f32 v135, v135\n\t" \
  "v_mfma_f32_16x16x32_bf16 v[68:71], a[80:83], v[104:107], v[68:71]\n\t" \
  "ds_read_b128 v[92:95], v51 offset:41984\n\t" \
  "v_mul_f32 v130, v130, v128\n\t" \
  "v_mfma_f32_16x16x32_bf16 v[72:75], a[144:147], v[104:107], v[72:75]\n\t" \
  "v_mul_f32 v131, v131, v129\n\t" \
  "v_mul_f32 v130, v130, v134\n\t" \
  "v_mfma_f32_16x16x32_bf16 v[64:67], a[20:23], v[108:111], v[64:67]\n\t" \
  "ds_read_b128 v[96:99], v51 offset:43008\n\t" \
  "v_mul_f32 v131, v131, v135\n\t" \
  "v_cvt_pk_bf16_f32 v180, v130, v131\n\t" \
  "v_mfma_f32_16x16x32_bf16 v[68:71], a[84:87], v[108:111], v[68:71]\n\t" \
  "ds_read_b128 v[100:103], v51 offset:44032\n\t" \
  "v_fma_f32 v140, v140, v126, v125\n\t" \
  "v_mfma_f32_16x16x32_bf16 v[72:75], a[148:151], v[108:111], v[72:75]\n\t" \
  "v_fma_f32 v141, v141, v126, v125\n\t" \
  "v_mul_f32 v140, v140, v136\n\t" \
  "v_mfma_f32_16x16x32_bf16 v[64:67], a[24:27], v[112:115], v[64:67]\n\t" \
  "v_mul_f32 v141, v141, v137\n\t" \
  "v_fma_f32 v128, v176, v124, v84\n\t" \
  "v_mfma_f32_16x16x32_bf16 v[68:71], a[88:91], v[112:115], v[68:71]\n\t" \
  "v_exp_f32 v142, v140\n\t" \
  "v_mfma_f32_16x16x32_bf16 v[72:75], a[152:155], v[112:115], v[72:75]\n\t" \
  "v_exp_f32 v143, v141\n\t" \
  "v_mfma_f32_16x16x32_bf16 v[64:67], a[28:31], v[116:119], v[64:67]\n\t" \
  "v_fma_f32 v138, v174, v124, v82\n\t" \
  "v_fma_f32 v139, v175, v124, v83\n\t" \
  "v_mfma_f32_16x16x32_bf16 v[68:71], a[92:95], v[116:119], v[68:71]\n\t" \
  "v_fma_f32 v142, v142, v127, v127\n\t" \
  "v_fma_f32 v143, v143, v127, v127\n\t" \
  "v_mfma_f32_16x16x32_bf16 v[72:75], a[156:159], v[116:119], v[72:75]\n\t" \
  "v_fma_f32 v129, v177, v124, v85\n\t" \
  "s_waitcnt lgkmcnt(0)\n\t" \
  "v_mfma_f32_16x16x32_bf16 v[64:67], a[32:35], v[88:91], v[64:67]\n\t" \
  "ds_read_b128 v[104:107], v51 offset:45056\n\t" \
  "v_mul_f32 v132, v128, v128\n\t" \
  "v_mfma_f32_16x16x32_bf16 v[68:71], a[96:99], v[88:91], v[68:71]\n\t" \
  "ds_read_b128 v[108:111], v51 offset:46080\n\t" \
  "v_mul_f32 v133, v129, v129\n\t" \
  "v_mfma_f32_16x16x32_bf16 v[72:75], a[160:163], v[88:91], v[72:75]\n\t" \
  "v_rcp_f32 v142, v142\n\t" \
  "v_mfma_f32_16x16x32_bf16 v[64:67], a[36:39], v[92:95], v[64:67]\n\t" \
  "ds_read_b128 v[112:115], v51 offset:47104\n\t" \
  "v_fma_f32 v132, v132, v126, v125\n\t" \
  "v_mfma_f32_16x16x32_bf16 v[68:71], a[100:103], v[92:95], v[68:71]\n\t" \
  "ds_read_b128 v[116:119], v51 offset:48128\n\t" \
  "v_rcp_f32 v143, v143\n\t" \
  "v_mfma_f32_16x16x32_bf16 v[72:75], a[164:167], v[92:95], v[72:75]\n\t" \
  "v_mul_f32 v138, v138, v136\n\t" \
  "v_mul_f32 v139, v139, v137\n\t" \
  "v_mfma_f32_16x16x32_bf16 v[64:67], a[40:43], v[96:99], v[64:67]\n\t" \
  "v_mul_f32 v138, v138, v142\n\t" \
  "v_mul_f32 v139, v139, v143\n\t" \
  "v_mfma_f32_16x16x32_bf16 v[68:71], a[104:107], v[96:99], v[68:71]\n\t" \
  "v_cvt_pk_bf16_f32 v181, v138, v139\n\t" \
  "v_fma_f32 v133, v133, v126, v125\n\t" \
  "v_mfma_f32_16x16x32_bf16 v[72:75], a[168:171], v[96:99], v[72:75]\n\t" \
  "v_mul_f32 v132, v132, v128\n\t" \
  "v_mul_f32 v133, v133, v129\n\t" \
  "v_mfma_f32_16x16x32_bf16 v[64:67], a[44:47], v[100:103], v[64:67]\n\t" \
  "v_exp_f32 v134, v132\n\t" \
  "v_mfma_f32_16x16x32_bf16 v[68:71], a[108:111], v[100:103], v[68:71]\n\t" \
  "v_exp_f32 v135, v133\n\t" \
  "v_mfma_f32_16x16x32_bf16 v[72:75], a[172:175], v[100:103], v[72:75]\n\t" \
  "v_fma_f32 v130, v178, v124, v86\n\t" \
  "s_waitcnt lgkmcnt(0)\n\t" \
  "v_mfma_f32_16x16x32_bf16 v[64:67], a[48:51], v[104:107], v[64:67]\n\t" \
  "ds_read_b128 v[88:91], v51 offset:49152\n\t" \
  "v_fma_f32 v131, v179, v124, v87\n\t" \
  "v_mfma_f32_16x16x32_bf16 v[68:71], a[112:115], v[104:107], v[68:71]\n\t" \
  "ds_read_b128 v[92:95], v51 offset:50176\n\t" \
  "v_fma_f32 v134, v134, v127, v127\n\t" \
  "v_mfma_f32_16x16x32_bf16 v[72:75], a[176:179], v[104:107], v[72:75]\n\t" \
  "v_fma_f32 v135, v135, v127, v127\n\t" \
  "v_permlane32_swap_b32 v180, v183\n\t" \
  "v_mfma_f32_16x16x32_bf16 v[64:67], a[52:55], v[108:111], v[64:67]\n\t" \
  "ds_read_b128 v[96:99], v51 offset:51200\n\t" \
  "s_add_u32 s94, s48, 270336\n\t" \
  "v_mfma_f32_16x16x32_bf16 v[68:71], a[116:119], v[108:111], v[68:71]\n\t" \
  "ds_read_b128 v[100:103], v51 offset:52224\n\t" \
  "buffer_load_dwordx2 v[60:61], v53, s[4:7], 0 offen offset:0 sc1\n\t" \
  "v_mfma_f32_16x16x32_bf16 v[72:75], a[180:183], v[108:111], v[72:75]\n\t" \
  "v_rcp_f32 v134, v134\n\t" \
  "v_mfma_f32_16x16x32_bf16 v[64:67], a[56:59], v[112:115], v[64:67]\n\t" \
  "ds_read_b128 v[120:123], v50 offset:1536\n\t" \
  "v_permlane16_swap_b32 v181, v180\n\t" \
  "v_mfma_f32_16x16x32_bf16 v[68:71], a[120:123], v[112:115], v[68:71]\n\t" \
  "v_rcp_f32 v135, v135\n\t" \
  "v_mfma_f32_16x16x32_bf16 v[72:75], a[184:187], v[112:115], v[72:75]\n\t" \
  "v_mul_f32 v130, v130, v128\n\t" \
  "v_mul_f32 v131, v131, v129\n\t" \
  "v_mfma_f32_16x16x32_bf16 v[64:67], a[60:63], v[116:119], v[64:67]\n\t" \
  "v_mul_f32 v130, v130, v134\n\t" \
  "v_mul_f32 v131, v131, v135\n\t" \
  "v_mfma_f32_16x16x32_bf16 v[68:71], a[124:127], v[116:119], v[68:71]\n\t" \
  "v_cvt_pk_bf16_f32 v182, v130, v131\n\t" \
  "v_mfma_f32_16x16x32_bf16 v[72:75], a[188:191], v[116:119], v[72:75]\n\t" \
  "s_nop 0\n\t" \
  "v_permlane16_swap_b32 v182, v183\n\t" \
  "buffer_store_dwordx4 v[180:183], v52, s[4:7], s94 offen offset:0 sc1\n\t" \
  "s_waitcnt lgkmcnt(0)\n\t" \
  "v_mfma_f32_16x16x32_bf16 v[76:79], a[0:3], v[88:91], 0\n\t" \
  "ds_read_b128 v[104:107], v51 offset:53248\n\t" \
  "v_fma_f32 v128, v168, v120, v64\n\t" \
  "v_fma_f32 v129, v169, v120, v65\n\t" \
  "v_mfma_f32_16x16x32_bf16 v[80:83], a[64:67], v[88:91], 0\n\t" \
  "ds_read_b128 v[108:111], v51 offset:54272\n\t" \
  "v_mul_f32 v132, v128, v128\n\t" \
  "v_mul_f32 v133, v129, v129\n\t" \
  "v_mfma_f32_16x16x32_bf16 v[84:87], a[128:131], v[88:91], 0\n\t" \
  "v_fma_f32 v132, v132, v122, v121\n\t" \
  "v_fma_f32 v133, v133, v122, v121\n\t" \
  "v_mul_f32 v132, v132, v128\n\t" \
  "v_mfma_f32_16x16x32_bf16 v[76:79], a[4:7], v[92:95], v[76:79]\n\t" \
  "ds_read_b128 v[112:115], v51 offset:55296\n\t" \
  "v_mul_f32 v133, v133, v129\n\t" \
  "v_fma_f32 v136, v172, v120, v68\n\t" \
  "v_mfma_f32_16x16x32_bf16 v[80:83], a[68:71], v[92:95], v[80:83]\n\t" \
  "ds_read_b128 v[116:119], v51 offset:56320\n\t" \
  "v_exp_f32 v134, v132\n\t" \
  "v_mfma_f32_16x16x32_bf16 v[84:87], a[132:135], v[92:95], v[84:87]\n\t" \
  "v_exp_f32 v135, v133\n\t" \
  "v_fma_f32 v130, v170, v120, v66\n\t" \
  "v_mfma_f32_16x16x32_bf16 v[76:79], a[8:11], v[96:99], v[76:79]\n\t" \
  "v_fma_f32 v131, v171, v120, v67\n\t" \
  "v_fma_f32 v134, v134, v123, v123\n\t" \
  "v_fma_f32 v135, v135, v123, v123\n\t" \
  "v_mfma_f32_16x16x32_bf16 v[80:83], a[72:75], v[96:99], v[80:83]\n\t" \
  "v_rcp_f32 v134, v134\n\t" \
  "v_fma_f32 v137, v173, v120, v69\n\t" \
  "v_mfma_f32_16x16x32_bf16 v[84:87], a[136:139], v[96:99], v[84:87]\n\t" \
  "v_rcp_f32 v135, v135\n\t" \
  "v_mul_f32 v130, v130, v128\n\t" \
  "v_mfma_f32_16x16x32_bf16 v[76:79], a[12:15], v[100:103], v[76:79]\n\t" \
  "v_mul_f32 v131, v131, v129\n\t" \
  "v_mul_f32 v130, v130, v134\n\t" \
  "v_mul_f32 v131, v131, v135\n\t" \
  "v_mfma_f32_16x16x32_bf16 v[80:83], a[76:79], v[100:103], v[80:83]\n\t" \
  "v_cvt_pk_bf16_f32 v180, v130, v131\n\t" \
  "v_mul_f32 v140, v136, v136\n\t" \
  "v_mul_f32 v141, v137, v137\n\t" \
  "v_mfma_f32_16x16x32_bf16 v[84:87], a[140:143], v[100:103], v[84:87]\n\t" \
  "v_fma_f32 v140, v140, v122, v121\n\t" \
  "v_fma_f32 v141, v141, v122, v121\n\t" \
  "s_waitcnt lgkmcnt(0)\n\t" \
  "v_mfma_f32_16x16x32_bf16 v[76:79], a[16:19], v[104:107], v[76:79]\n\t" \
  "ds_read_b128 v[88:91], v51 offset:57344\n\t" \
  "s_waitcnt vmcnt(1)\n\t" \
  "s_movk_i32 s90, 0x7fff\n\t" \
  "Lp4_%=:\n\t" \
  "v_cmp_ne_u64 vcc, s[54:55], v[60:61]\n\t" \
  "s_and_b64 vcc, exec, vcc\n\t" \
  "s_cbranch_scc0 Lq4_%=\n\t" \
  "s_sub_u32 s90, s90, 1\n\t" \
  "s_cbranch_scc1 Lq4_%=\n\t" \
  "s_sleep 3\n\t" \
  "buffer_load_dwordx2 v[60:61], v53, s[4:7], 0 offen sc1\n\t" \
  "s_waitcnt vmcnt(0)\n\t" \
  "s_branch Lp4_%=\n\t" \
  "Lq4_%=:\n\t" \
  "v_mfma_f32_16x16x32_bf16 v[80:83], a[80:83], v[104:107], v[80:83]\n\t" \
  "ds_read_b128 v[92:95], v51 offset:58368\n\t" \
  "buffer_load_dwordx4 a[0:3], v32, s[4:7], s52 offen offset:0 sc1\n\t" \
  "v_mul_f32 v140, v140, v136\n\t" \
  "v_mfma_f32_16x16x32_bf16 v[84:87], a[144:147], v[104:107], v[84:87]\n\t" \
  "v_mul_f32 v141, v141, v137\n\t" \
  "v_exp_f32 v142, v140\n\t" \
  "v_mfma_f32_16x16x32_bf16 v[76:79], a[20:23], v[108:111], v[76:79]\n\t" \
  "ds_read_b128 v[96:99], v51 offset:59392\n\t" \
  "v_exp_f32 v143, v141\n\t" \
  "v_mfma_f32_16x16x32_bf16 v[80:83], a[84:87], v[108:111], v[80:83]\n\t" \
  "ds_read_b128 v[100:103], v51 offset:60416\n\t" \
  "buffer_load_dwordx4 a[64:67], v32, s[4:7], s52 offen offset:1024 sc1\n\t" \
  "v_fma_f32 v138, v174, v120, v70\n\t" \
  "v_mfma_f32_16x16x32_bf16 v[84:87], a[148:151], v[108:111], v[84:87]\n\t" \
  "v_fma_f32 v139, v175, v120, v71\n\t" \
  "v_fma_f32 v142, v142, v123, v123\n\t" \
  "v_fma_f32 v143, v143, v123, v123\n\t" \
  "v_mfma_f32_16x16x32_bf16 v[76:79], a[24:27], v[112:115], v[76:79]\n\t" \
  "v_rcp_f32 v142, v142\n\t" \
  "v_fma_f32 v128, v176, v120, v72\n\t" \
  "v_mfma_f32_16x16x32_bf16 v[80:83], a[88:91], v[112:115], v[80:83]\n\t" \
  "buffer_load_dwordx4 a[128:131], v32, s[4:7], s52 offen offset:2048 sc1\n\t" \
  "v_fma_f32 v129, v177, v120, v73\n\t" \
  "v_mfma_f32_16x16x32_bf16 v[84:87], a[152:155], v[112:115], v[84:87]\n\t" \
  "v_rcp_f32 v143, v143\n\t" \
  "v_mfma_f32_16x16x32_bf16 v[76:79], a[28:31], v[116:119], v[76:79]\n\t" \
  "v_mul_f32 v138, v138, v136\n\t" \
  "v_mul_f32 v139, v139, v137\n\t" \
  "v_mfma_f32_16x16x32_bf16 v[80:83], a[92:95], v[116:119], v[80:83]\n\t" \
  "buffer_load_dwordx4 a[4:7], v32, s[4:7], s52 offen offset:3072 sc1\n\t" \
  "v_mul_f32 v138, v138, v142\n\t" \
  "v_mfma_f32_16x16x32_bf16 v[84:87], a[156:159], v[116:119], v[84:87]\n\t" \
  "s_add_u32 s88, s52, 4096\n\t" \
  "s_waitcnt lgkmcnt(0)\n\t" \
  "v_mfma_f32_16x16x32_bf16 v[76:79], a[32:35], v[88:91], v[76:79]\n\t" \
  "ds_read_b128 v[104:107], v51 offset:61440\n\t" \
  "v_mul_f32 v139, v139, v143\n\t" \
  "v_mfma_f32_16x16x32_bf16 v[80:83], a[96:99], v[88:91], v[80:83]\n\t" \
  "ds_read_b128 v[108:111], v51 offset:62464\n\t" \
  "buffer_load_dwordx4 a[68:71], v32, s[4:7], s88 offen offset:0 sc1\n\t" \
  "v_mfma_f32_16x16x32_bf16 v[84:87], a[160:163], v[88:91], v[84:87]\n\t" \
  "v_cvt_pk_bf16_f32 v181, v138, v139\n\t" \
  "v_mul_f32 v132, v128, v128\n\t" \
  "v_mfma_f32_16x16x32_bf16 v[76:79], a[36:39], v[92:95], v[76:79]\n\t" \
  "ds_read_b128 v[112:115], v51 offset:63488\n\t" \
  "v_mul_f32 v133, v129, v129\n\t" \
  "v_mfma_f32_16x16x32_bf16 v[80:83], a[100:103], v[92:95], v[80:83]\n\t" \
  "ds_read_b128 v[116:119], v51 offset:64512\n\t" \
  "buffer_load_dwordx4 a[132:135], v32, s[4:7], s88 offen offset:1024 sc1\n\t" \
  "v_fma_f32 v132, v132, v122, v121\n\t" \
  "v_mfma_f32_16x16x32_bf16 v[84:87], a[164:167], v[92:95], v[84:87]\n\t" \
  "v_fma_f32 v133, v133, v122, v121\n\t" \
  "v_mul_f32 v132, v132, v128\n\t" \
  "v_mfma_f32_16x16x32_bf16 v[76:79], a[40:43], v[96:99], v[76:79]\n\t" \
  "v_mul_f32 v133, v133, v129\n\t" \
  "v_permlane32_swap_b32 v180, v183\n\t" \
  "v_mfma_f32_16x16x32_bf16 v[80:83], a[104:107], v[96:99], v[80:83]\n\t" \
  "buffer_load_dwordx4 a[8:11], v32, s[4:7], s88 offen offset:2048 sc1\n\t" \
  "s_add_u32 s94, s48, 135168\n\t" \
  "v_mfma_f32_16x16x32_bf16 v[84:87], a[168:171], v[96:99], v[84:87]\n\t" \
  "v_exp_f32 v134, v132\n\t" \
  "v_mfma_f32_16x16x32_bf16 v[76:79], a[44:47], v[100:103], v[76:79]\n\t" \
  "v_exp_f32 v135, v133\n\t" \
  "v_mfma_f32_16x16x32_bf16 v[80:83], a[108:111], v[100:103], v[80:83]\n\t" \
  "buffer_load_dwordx4 a[72:75], v32, s[4:7], s88 offen offset:3072 sc1\n\t" \
  "v_fma_f32 v130, v178, v120, v74\n\t" \
  "v_mfma_f32_16x16x32_bf16 v[84:87], a[172:175], v[100:103], v[84:87]\n\t" \
  "s_add_u32 s89, s52, 8192\n\t" \
  "s_waitcnt lgkmcnt(0)\n\t" \
  "v_mfma_f32_16x16x32_bf16 v[76:79], a[48:51], v[104:107], v[76:79]\n\t" \
  "v_fma_f32 v131, v179, v120, v75\n\t" \
  "v_fma_f32 v134, v134, v123, v123\n\t" \
  "v_mfma_f32_16x16x32_bf16 v[80:83], a[112:115], v[104:107], v[80:83]\n\t" \
  "buffer_load_dwordx4 a[136:139], v32, s[4:7], s89 offen offset:0 sc1\n\t" \
  "v_fma_f32 v135, v135, v123, v123\n\t" \
  "v_mfma_f32_16x16x32_bf16 v[84:87], a[176:179], v[104:107], v[84:87]\n\t" \
  "v_rcp_f32 v134, v134\n\t" \
  "v_mfma_f32_16x16x32_bf16 v[76:79], a[52:55], v[108:111], v[76:79]\n\t" \
  "v_rcp_f32 v135, v135\n\t" \
  "v_mfma_f32_16x16x32_bf16 v[80:83], a[116:119], v[108:111], v[80:83]\n\t" \
  "buffer_load_dwordx4 a[12:15], v32, s[4:7], s89 offen offset:1024 sc1\n\t" \
  "v_mul_f32 v130, v130, v128\n\t" \
  "v_mfma_f32_16x16x32_bf16 v[84:87], a[180:183], v[108:111], v[84:87]\n\t" \
  "v_mul_f32 v131, v131, v129\n\t" \
  "v_mul_f32 v130, v130, v134\n\t" \
  "v_mfma_f32_16x16x32_bf16 v[76:79], a[56:59], v[112:115], v[76:79]\n\t" \
  "ds_read_b128 v[124:127], v50 offset:1792\n\t" \
  "v_mul_f32 v131, v131, v135\n\t" \
  "v_mfma_f32_16x16x32_bf16 v[80:83], a[120:123], v[112:115], v[80:83]\n\t" \
  "buffer_load_dwordx4 a[76:79], v54, s[4:7], s89 offen offset:2048 sc1\n\t" \
  "v_cvt_pk_bf16_f32 v182, v130, v131\n\t" \
  "v_mfma_f32_16x16x32_bf16 v[84:87], a[184:187], v[112:115], v[84:87]\n\t" \
  "v_permlane16_swap_b32 v181, v180\n\t" \
  "v_permlane16_swap_b32 v182, v183\n\t" \
  "v_mfma_f32_16x16x32_bf16 v[76:79], a[60:63], v[116:119], v[76:79]\n\t" \
  "buffer_store_dwordx4 v[180:183], v52, s[4:7], s94 offen offset:0 sc1\n\t" \
  "v_mfma_f32_16x16x32_bf16 v[80:83], a[124:127], v[116:119], v[80:83]\n\t" \
  "v_mfma_f32_16x16x32_bf16 v[84:87], a[188:191], v[116:119], v[84:87]\n\t" \
  "s_waitcnt lgkmcnt(0)\n\t" \
  "s_nop 0\n\t" \
  "s_nop 0\n\t" \
  "s_nop 0\n\t" \
  "s_nop 0\n\t" \
  "v_fma_f32 v128, v168, v124, v76\n\t" \
  "v_fma_f32 v129, v169, v124, v77\n\t" \
  "v_mul_f32 v132, v128, v128\n\t" \
  "v_mul_f32 v133, v129, v129\n\t" \
  "v_fma_f32 v132, v132, v126, v125\n\t" \
  "v_fma_f32 v133, v133, v126, v125\n\t" \
  "v_mul_f32 v132, v132, v128\n\t" \
  "v_mul_f32 v133, v133, v129\n\t" \
  "v_exp_f32 v134, v132\n\t" \
  "v_exp_f32 v135, v133\n\t" \
  "v_fma_f32 v130, v170, v124, v78\n\t" \
  "v_fma_f32 v131, v171, v124, v79\n\t" \
  "v_fma_f32 v134, v134, v127, v127\n\t" \
  "v_fma_f32 v135, v135, v127, v127\n\t" \
  "v_rcp_f32 v134, v134\n\t" \
  "v_rcp_f32 v135, v135\n\t" \
  "v_mul_f32 v130, v130, v128\n\t" \
  "v_mul_f32 v131, v131, v129\n\t" \
  "v_mul_f32 v130, v130, v134\n\t" \
  "v_mul_f32 v131, v131, v135\n\t" \
  "v_cvt_pk_bf16_f32 v180, v130, v131\n\t" \
  "v_fma_f32 v136, v172, v124, v80\n\t" \
  "v_fma_f32 v137, v173, v124, v81\n\t" \
  "v_mul_f32 v140, v136, v136\n\t" \
  "v_mul_f32 v141, v137, v137\n\t" \
  "v_fma_f32 v140, v140, v126, v125\n\t" \
  "v_fma_f32 v141, v141, v126, v125\n\t" \
  "v_mul_f32 v140, v140, v136\n\t" \
  "v_mul_f32 v141, v141, v137\n\t" \
  "v_exp_f32 v142, v140\n\t" \
  "v_exp_f32 v143, v141\n\t" \
  "v_fma_f32 v138, v174, v124, v82\n\t" \
  "v_fma_f32 v139, v175, v124, v83\n\t" \
  "v_fma_f32 v142, v142, v127, v127\n\t" \
  "v_fma_f32 v143, v143, v127, v127\n\t" \
  "v_rcp_f32 v142, v142\n\t" \
  "v_rcp_f32 v143, v143\n\t" \
  "v_mul_f32 v138, v138, v136\n\t" \
  "v_mul_f32 v139, v139, v137\n\t" \
  "v_mul_f32 v138, v138, v142\n\t" \
  "v_mul_f32 v139, v139, v143\n\t" \
  "v_cvt_pk_bf16_f32 v181, v138, v139\n\t" \
  "v_fma_f32 v128, v176, v124, v84\n\t" \
  "v_fma_f32 v129, v177, v124, v85\n\t" \
  "v_mul_f32 v132, v128, v128\n\t" \
  "v_mul_f32 v133, v129, v129\n\t" \
  "v_fma_f32 v132, v132, v126, v125\n\t" \
  "v_fma_f32 v133, v133, v126, v125\n\t" \
  "v_mul_f32 v132, v132, v128\n\t" \
  "v_mul_f32 v133, v133, v129\n\t" \
  "v_exp_f32 v134, v132\n\t" \
  "v_exp_f32 v135, v133\n\t" \
  "v_fma_f32 v130, v178, v124, v86\n\t" \
  "v_fma_f32 v131, v179, v124, v87\n\t" \
  "v_fma_f32 v134, v134, v127, v127\n\t" \
  "v_fma_f32 v135, v135, v127, v127\n\t" \
  "v_rcp_f32 v134, v134\n\t" \
  "v_rcp_f32 v135, v135\n\t" \
  "v_mul_f32 v130, v130, v128\n\t" \
  "v_mul_f32 v131, v131, v129\n\t" \
  "v_mul_f32 v130, v130, v134\n\t" \
  "v_mul_f32 v131, v131, v135\n\t" \
  "v_cvt_pk_bf16_f32 v182, v130, v131\n\t" \
  "v_permlane32_swap_b32 v180, v183\n\t" \
  "s_add_u32 s94, s48, 315392\n\t" \
  "s_nop 0\n\t" \
  "v_permlane16_swap_b32 v181, v180\n\t" \
  "v_permlane16_swap_b32 v182, v183\n\t" \
  "buffer_store_dwordx4 v[180:183], v52, s[4:7], s94 offen offset:0 sc1\n\t" \
  "s_waitcnt vmcnt(12)\n\t" \
  "v_mfma_f32_16x16x32_bf16 v[104:107], a[192:195], a[0:3], 0\n\t" \
  "buffer_load_dwordx4 a[140:143], v32, s[4:7], s51 offen offset:0 sc1\n\t" \
  "s_and_b32 s32, s24, 1\n\t" \
  "v_mfma_f32_16x16x32_bf16 v[108:111], a[196:199], a[0:3], 0\n\t" \
  "s_lshl_b32 s32, s32, 13\n\t" \
  "s_lshl_b32 s33, s24, 11\n\t" \
  "v_mfma_f32_16x16x32_bf16 v[112:115], a[200:203], a[0:3], 0\n\t" \
  "s_add_u32 s32, s32, s33\n\t" \
  "v_add_u32 v62, s32, v32\n\t" \
  "v_mfma_f32_16x16x32_bf16 v[116:119], a[204:207], a[0:3], 0\n\t" \
  "buffer_load_dwordx4 a[16:19], v32, s[4:7], s51 offen offset:1024 sc1\n\t" \
  "s_waitcnt vmcnt(13)\n\t" \
  "v_mfma_f32_16x16x32_bf16 v[104:107], a[208:211], a[64:67], v[104:107]\n\t" \
  "v_xor_b32 v63, 0x2000, v62\n\t" \
  "s_lshl_b32 s32, s24, 13\n\t" \
  "v_mfma_f32_16x16x32_bf16 v[108:111], a[212:215], a[64:67], v[108:111]\n\t" \
  "v_add_u32 v59, s32, v32\n\t" \
  "v_mfma_f32_16x16x32_bf16 v[112:115], a[216:219], a[64:67], v[112:115]\n\t" \
  "buffer_load_dwordx4 a[80:83], v32, s[4:7], s51 offen offset:2048 sc1\n\t" \
  "v_mfma_f32_16x16x32_bf16 v[116:119], a[220:223], a[64:67], v[116:119]\n\t" \
  "s_waitcnt vmcnt(13)\n\t" \
  "v_mfma_f32_16x16x32_bf16 v[104:107], a[224:227], a[128:131], v[104:107]\n\t" \
  "v_mfma_f32_16x16x32_bf16 v[108:111], a[228:231], a[128:131], v[108:111]\n\t" \
  "buffer_load_dwordx4 a[144:147], v32, s[4:7], s51 offen offset:3072 sc1\n\t" \
  "s_add_u32 s88, s51, 4096\n\t" \
  "v_mfma_f32_16x16x32_bf16 v[112:115], a[232:235], a[128:131], v[112:115]\n\t" \
  "v_mfma_f32_16x16x32_bf16 v[116:119], a[236:239], a[128:131], v[116:119]\n\t" \
  "s_waitcnt vmcnt(13)\n\t" \
  "v_mfma_f32_16x16x32_bf16 v[104:107], a[240:243], a[4:7], v[104:107]\n\t" \
  "buffer_load_dwordx4 a[20:23], v32, s[4:7], s88 offen offset:0 sc1\n\t" \
  "v_mfma_f32_16x16x32_bf16 v[108:111], a[244:247], a[4:7], v[108:111]\n\t" \
  "v_mfma_f32_16x16x32_bf16 v[112:115], a[248:251], a[4:7], v[112:115]\n\t" \
  "v_mfma_f32_16x16x32_bf16 v[116:119], a[252:255], a[4:7], v[116:119]\n\t" \
  "buffer_load_dwordx4 a[84:87], v32, s[4:7], s88 offen offset:1024 sc1\n\t" \
  "s_waitcnt vmcnt(14)\n\t" \
  "v_mfma_f32_16x16x32_bf16 v[104:107], v[144:147], a[68:71], v[104:107]\n\t" \
  "v_mfma_f32_16x16x32_bf16 v[108:111], v[148:151], a[68:71], v[108:111]\n\t" \
  "v_mfma_f32_16x16x32_bf16 v[112:115], v[152:155], a[68:71], v[112:115]\n\t" \
  "buffer_load_dwordx4 a[148:151], v32, s[4:7], s88 offen offset:2048 sc1\n\t" \
  "v_mfma_f32_16x16x32_bf16 v[116:119], v[156:159], a[68:71], v[116:119]\n\t" \
  "s_waitcnt vmcnt(14)\n\t" \
  "v_mfma_f32_16x16x32_bf16 v[104:107], v[160:163], a[132:135], v[104:107]\n\t" \
  "v_mfma_f32_16x16x32_bf16 v[108:111], v[164:167], a[132:135], v[108:111]\n\t" \
  "buffer_load_dwordx4 a[24:27], v32, s[4:7], s88 offen offset:3072 sc1\n\t" \
  "s_add_u32 s89, s51, 8192\n\t" \
  "v_mfma_f32_16x16x32_bf16 v[112:115], v[0:3], a[132:135], v[112:115]\n\t" \
  "v_mfma_f32_16x16x32_bf16 v[116:119], v[4:7], a[132:135], v[116:119]\n\t" \
  "s_waitcnt vmcnt(14)\n\t" \
  "v_mfma_f32_16x16x32_bf16 v[104:107], v[192:195], a[8:11], v[104:107]\n\t" \
  "buffer_load_dwordx4 a[88:91], v32, s[4:7], s89 offen offset:0 sc1\n\t" \
  "v_mfma_f32_16x16x32_bf16 v[108:111], v[196:199], a[8:11], v[108:111]\n\t" \
  "v_mfma_f32_16x16x32_bf16 v[112:115], v[200:203], a[8:11], v[112:115]\n\t" \
  "v_mfma_f32_16x16x32_bf16 v[116:119], v[204:207], a[8:11], v[116:119]\n\t" \
  "buffer_load_dwordx4 a[152:155], v32, s[4:7], s89 offen offset:1024 sc1\n\t" \
  "s_waitcnt vmcnt(15)\n\t" \
  "v_mfma_f32_16x16x32_bf16 v[104:107], v[208:211], a[72:75], v[104:107]\n\t" \
  "v_mfma_f32_16x16x32_bf16 v[108:111], v[212:215], a[72:75], v[108:111]\n\t" \
  "v_mfma_f32_16x16x32_bf16 v[112:115], v[216:219], a[72:75], v[112:115]\n\t" \
  "buffer_load_dwordx4 a[28:31], v54, s[4:7], s89 offen offset:2048 sc1\n\t" \
  "s_min_u32 s51, s51, s52\n\t" \
  "v_mfma_f32_16x16x32_bf16 v[116:119], v[220:223], a[72:75], v[116:119]\n\t" \
  "s_add_u32 s89, s51, 90112\n\t" \
  "s_waitcnt vmcnt(15)\n\t" \
  "v_mfma_f32_16x16x32_bf16 v[104:107], v[224:227], a[136:139], v[104:107]\n\t" \
  "v_mfma_f32_16x16x32_bf16 v[108:111], v[228:231], a[136:139], v[108:111]\n\t" \
  "buffer_load_dwordx4 a[92:95], v32, s[4:7], s89 offen offset:0 sc1\n\t" \
  "v_mfma_f32_16x16x32_bf16 v[112:115], v[232:235], a[136:139], v[112:115]\n\t" \
  "v_mfma_f32_16x16x32_bf16 v[116:119], v[236:239], a[136:139], v[116:119]\n\t" \
  "s_waitcnt vmcnt(15)\n\t" \
  "v_mfma_f32_16x16x32_bf16 v[104:107], v[240:243], a[12:15], v[104:107]\n\t" \
  "buffer_load_dwordx4 a[156:159], v32, s[4:7], s89 offen offset:1024 sc1\n\t" \
  "v_mfma_f32_16x16x32_bf16 v[108:111], v[244:247], a[12:15], v[108:111]\n\t" \
  "v_mfma_f32_16x16x32_bf16 v[112:115], v[248:251], a[12:15], v[112:115]\n\t" \
  "v_mfma_f32_16x16x32_bf16 v[116:119], v[252:255], a[12:15], v[116:119]\n\t" \
  "buffer_load_dwordx4 a[32:35], v32, s[4:7], s89 offen offset:2048 sc1\n\t" \
  "s_waitcnt vmcnt(16)\n\t" \
  "v_mfma_f32_16x16x32_bf16 v[104:107], v[8:11], a[76:79], v[104:107]\n\t" \
  "v_mfma_f32_16x16x32_bf16 v[108:111], v[12:15], a[76:79], v[108:111]\n\t" \
  "v_mfma_f32_16x16x32_bf16 v[112:115], v[16:19], a[76:79], v[112:115]\n\t" \
  "buffer_load_dwordx4 a[96:99], v32, s[4:7], s89 offen offset:3072 sc1\n\t" \
  "s_add_u32 s88, s51, 94208\n\t" \
  "v_mfma_f32_16x16x32_bf16 v[116:119], v[20:23], a[76:79], v[116:119]\n\t" \
  "s_waitcnt vmcnt(14)\n\t" \
  "v_mfma_f32_16x16x32_bf16 v[96:99], a[200:203], a[140:143], 0\n\t" \
  "v_mfma_f32_16x16x32_bf16 v[100:103], a[204:207], a[140:143], 0\n\t" \
  "buffer_load_dwordx4 a[160:163], v32, s[4:7], s88 offen offset:0 sc1\n\t" \
  "s_waitcnt vmcnt(14)\n\t" \
  "v_mfma_f32_16x16x32_bf16 v[96:99], a[216:219], a[16:19], v[96:99]\n\t" \
  "v_mfma_f32_16x16x32_bf16 v[100:103], a[220:223], a[16:19], v[100:103]\n\t" \
  "s_waitcnt vmcnt(13)\n\t" \
  "v_mfma_f32_16x16x32_bf16 v[96:99], a[232:235], a[80:83], v[96:99]\n\t" \
  "buffer_load_dwordx4 a[36:39], v32, s[4:7], s88 offen offset:1024 sc1\n\t" \
  "v_mfma_f32_16x16x32_bf16 v[100:103], a[236:239], a[80:83], v[100:103]\n\t" \
  "ds_write_b128 v40, v[104:107] offset:49152\n\t" \
  "s_waitcnt vmcnt(13)\n\t" \
  "v_mfma_f32_16x16x32_bf16 v[96:99], a[248:251], a[144:147], v[96:99]\n\t" \
  "ds_write_b128 v40, v[108:111] offset:50176\n\t" \
  "v_mfma_f32_16x16x32_bf16 v[100:103], a[252:255], a[144:147], v[100:103]\n\t" \
  "ds_write_b128 v41, v[112:115] offset:49152\n\t" \
  "s_waitcnt vmcnt(12)\n\t" \
  "v_mfma_f32_16x16x32_bf16 v[96:99], v[152:155], a[20:23], v[96:99]\n\t" \
  "ds_write_b128 v41, v[116:119] offset:50176\n\t" \
  "buffer_load_dwordx4 a[100:103], v32, s[4:7], s88 offen offset:2048 sc1\n\t" \
  "v_mfma_f32_16x16x32_bf16 v[100:103], v[156:159], a[20:23], v[100:103]\n\t" \
  "s_waitcnt vmcnt(12)\n\t" \
  "v_mfma_f32_16x16x32_bf16 v[96:99], v[0:3], a[84:87], v[96:99]\n\t" \
  "v_mfma_f32_16x16x32_bf16 v[100:103], v[4:7], a[84:87], v[100:103]\n\t" \
  "buffer_load_dwordx4 a[164:167], v32, s[4:7], s88 offen offset:3072 sc1\n\t" \
  "s_waitcnt vmcnt(12)\n\t" \
  "v_mfma_f32_16x16x32_bf16 v[96:99], v[200:203], a[148:151], v[96:99]\n\t" \
  "s_add_u32 s89, s51, 98304\n\t" \
  "v_mfma_f32_16x16x32_bf16 v[100:103], v[204:207], a[148:151], v[100:103]\n\t" \
  "s_waitcnt vmcnt(11)\n\t" \
  "v_mfma_f32_16x16x32_bf16 v[96:99], v[216:219], a[24:27], v[96:99]\n\t" \
  "buffer_load_dwordx4 a[40:43], v32, s[4:7], s89 offen offset:0 sc1\n\t" \
  "v_mfma_f32_16x16x32_bf16 v[100:103], v[220:223], a[24:27], v[100:103]\n\t" \
  "s_waitcnt vmcnt(11)\n\t" \
  "v_mfma_f32_16x16x32_bf16 v[96:99], v[232:235], a[88:91], v[96:99]\n\t" \
  "v_mfma_f32_16x16x32_bf16 v[100:103], v[236:239], a[88:91], v[100:103]\n\t" \
  "buffer_load_dwordx4 a[104:107], v32, s[4:7], s89 offen offset:1024 sc1\n\t" \
  "s_waitcnt vmcnt(11)\n\t" \
  "v_mfma_f32_16x16x32_bf16 v[96:99], v[248:251], a[152:155], v[96:99]\n\t" \
  "v_mfma_f32_16x16x32_bf16 v[100:103], v[252:255], a[152:155], v[100:103]\n\t" \
  "s_waitcnt vmcnt(10)\n\t" \
  "v_mfma_f32_16x16x32_bf16 v[96:99], v[16:19], a[28:31], v[96:99]\n\t" \
  "buffer_load_dwordx4 a[168:171], v54, s[4:7], s89 offen offset:2048 sc1\n\t" \
  "v_mfma_f32_16x16x32_bf16 v[100:103], v[20:23], a[28:31], v[100:103]\n\t" \
  "v_mfma_f32_16x16x32_bf16 v[88:91], a[192:195], a[140:143], 0\n\t" \
  "v_mfma_f32_16x16x32_bf16 v[92:95], a[196:199], a[140:143], 0\n\t" \
  "v_mfma_f32_16x16x32_bf16 v[88:91], a[208:211], a[16:19], v[88:91]\n\t" \
  "v_mfma_f32_16x16x32_bf16 v[92:95], a[212:215], a[16:19], v[92:95]\n\t" \
  "v_mfma_f32_16x16x32_bf16 v[88:91], a[224:227], a[80:83], v[88:91]\n\t" \
  "v_mfma_f32_16x16x32_bf16 v[92:95], a[228:231], a[80:83], v[92:95]\n\t" \
  "v_mfma_f32_16x16x32_bf16 v[88:91], a[240:243], a[144:147], v[88:91]\n\t" \
  "ds_write_b128 v39, v[96:99] offset:49152\n\t" \
  "v_mfma_f32_16x16x32_bf16 v[92:95], a[244:247], a[144:147], v[92:95]\n\t" \
  "ds_write_b128 v39, v[100:103] offset:50176\n\t" \
  "v_mfma_f32_16x16x32_bf16 v[88:91], v[144:147], a[20:23], v[88:91]\n\t" \
  "v_mfma_f32_16x16x32_bf16 v[92:95], v[148:151], a[20:23], v[92:95]\n\t" \
  "v_mfma_f32_16x16x32_bf16 v[88:91], v[160:163], a[84:87], v[88:91]\n\t" \
  "v_mfma_f32_16x16x32_bf16 v[92:95], v[164:167], a[84:87], v[92:95]\n\t" \
  "v_mfma_f32_16x16x32_bf16 v[88:91], v[192:195], a[148:151], v[88:91]\n\t" \
  "v_mfma_f32_16x16x32_bf16 v[92:95], v[196:199], a[148:151], v[92:95]\n\t" \
  "v_mfma_f32_16x16x32_bf16 v[88:91], v[208:211], a[24:27], v[88:91]\n\t" \
  "v_mfma_f32_16x16x32_bf16 v[92:95], v[212:215], a[24:27], v[92:95]\n\t" \
  "v_mfma_f32_16x16x32_bf16 v[88:91], v[224:227], a[88:91], v[88:91]\n\t" \
  "v_mfma_f32_16x16x32_bf16 v[92:95], v[228:231], a[88:91], v[92:95]\n\t" \
  "v_mfma_f32_16x16x32_bf16 v[88:91], v[240:243], a[152:155], v[88:91]\n\t" \
  "v_mfma_f32_16x16x32_bf16 v[92:95], v[244:247], a[152:155], v[92:95]\n\t" \
  "v_mfma_f32_16x16x32_bf16 v[88:91], v[8:11], a[28:31], v[88:91]\n\t" \
  "v_mfma_f32_16x16x32_bf16 v[92:95], v[12:15], a[28:31], v[92:95]\n\t" \
  "s_waitcnt lgkmcnt(0)\n\t" \
  "s_barrier\n\t" \
  "s_waitcnt vmcnt(10)\n\t" \
  "v_mfma_f32_16x16x32_bf16 v[128:131], a[192:195], a[92:95], 0\n\t" \
  "s_add_u32 s89, s51, 135168\n\t" \
  "buffer_load_dwordx4 a[44:47], v32, s[4:7], s89 offen offset:0 sc1\n\t" \
  "v_mfma_f32_16x16x32_bf16 v[132:135], a[196:199], a[92:95], 0\n\t" \
  "ds_read_b128 v[64:67], v42 offset:49152\n\t" \
  "ds_read_b128 v[68:71], v42 offset:50176\n\t" \
  "v_mfma_f32_16x16x32_bf16 v[136:139], a[200:203], a[92:95], 0\n\t" \
  "buffer_load_dwordx4 a[108:111], v32, s[4:7], s89 offen offset:1024 sc1\n\t" \
  "ds_read_b128 v[72:75], v42 offset:51200\n\t" \
  "v_mfma_f32_16x16x32_bf16 v[140:143], a[204:207], a[92:95], 0\n\t" \
  "ds_read_b128 v[76:79], v42 offset:52224\n\t" \
  "ds_read_b128 v[80:83], v42 offset:53248\n\t" \
  "s_waitcnt vmcnt(11)\n\t" \
  "v_mfma_f32_16x16x32_bf16 v[128:131], a[208:211], a[156:159], v[128:131]\n\t" \
  "buffer_load_dwordx4 a[172:175], v32, s[4:7], s89 offen offset:2048 sc1\n\t" \
  "ds_read_b128 v[84:87], v42 offset:54272\n\t" \
  "v_mfma_f32_16x16x32_bf16 v[132:135], a[212:215], a[156:159], v[132:135]\n\t" \
  "v_lshlrev_b32 v120, 16, v24\n\t" \
  "v_and_b32 v121, 0xffff0000, v24\n\t" \
  "v_mfma_f32_16x16x32_bf16 v[136:139], a[216:219], a[156:159], v[136:139]\n\t" \
  "buffer_load_dwordx4 a[48:51], v32, s[4:7], s89 offen offset:3072 sc1\n\t" \
  "s_add_u32 s88, s51, 139264\n\t" \
  "v_mfma_f32_16x16x32_bf16 v[140:143], a[220:223], a[156:159], v[140:143]\n\t" \
  "v_lshlrev_b32 v122, 16, v25\n\t" \
  "v_and_b32 v123, 0xffff0000, v25\n\t" \
  "s_waitcnt vmcnt(12)\n\t" \
  "v_mfma_f32_16x16x32_bf16 v[128:131], a[224:227], a[32:35], v[128:131]\n\t" \
  "buffer_load_dwordx4 a[112:115], v32, s[4:7], s88 offen offset:0 sc1\n\t" \
  "v_lshlrev_b32 v124, 16, v26\n\t" \
  "v_mfma_f32_16x16x32_bf16 v[132:135], a[228:231], a[32:35], v[132:135]\n\t" \
  "v_and_b32 v125, 0xffff0000, v26\n\t" \
  "v_lshlrev_b32 v126, 16, v27\n\t" \
  "v_mfma_f32_16x16x32_bf16 v[136:139], a[232:235], a[32:35], v[136:139]\n\t" \
  "buffer_load_dwordx4 a[176:179], v32, s[4:7], s88 offen offset:1024 sc1\n\t" \
  "v_and_b32 v127, 0xffff0000, v27\n\t" \
  "v_mfma_f32_16x16x32_bf16 v[140:143], a[236:239], a[32:35], v[140:143]\n\t" \
  "v_add_f32 v88, v88, v120\n\t" \
  "v_add_f32 v89, v89, v121\n\t" \
  "s_waitcnt vmcnt(13)\n\t" \
  "v_mfma_f32_16x16x32_bf16 v[128:131], a[240:243], a[96:99], v[128:131]\n\t" \
  "buffer_load_dwordx4 a[52:55], v32, s[4:7], s88 offen offset:2048 sc1\n\t" \
  "v_add_f32 v90, v90, v122\n\t" \
  "v_mfma_f32_16x16x32_bf16 v[132:135], a[244:247], a[96:99], v[132:135]\n\t" \
  "v_add_f32 v91, v91, v123\n\t" \
  "v_add_f32 v92, v92, v124\n\t" \
  "v_mfma_f32_16x16x32_bf16 v[136:139], a[248:251], a[96:99], v[136:139]\n\t" \
  "buffer_load_dwordx4 a[116:119], v32, s[4:7], s88 offen offset:3072 sc1\n\t" \
  "s_add_u32 s89, s51, 143360\n\t" \
  "v_mfma_f32_16x16x32_bf16 v[140:143], a[252:255], a[96:99], v[140:143]\n\t" \
  "v_add_f32 v93, v93, v125\n\t" \
  "v_add_f32 v94, v94, v126\n\t" \
  "s_waitcnt vmcnt(14)\n\t" \
  "v_mfma_f32_16x16x32_bf16 v[128:131], v[144:147], a[160:163], v[128:131]\n\t" \
  "buffer_load_dwordx4 a[180:183], v32, s[4:7], s89 offen offset:0 sc1\n\t" \
  "v_add_f32 v95, v95, v127\n\t" \
  "v_mfma_f32_16x16x32_bf16 v[132:135], v[148:151], a[160:163], v[132:135]\n\t" \
  "s_waitcnt lgkmcnt(4)\n\t" \
  "v_add_f32 v88, v88, v64\n\t" \
  "v_mfma_f32_16x16x32_bf16 v[136:139], v[152:155], a[160:163], v[136:139]\n\t" \
  "buffer_load_dwordx4 a[56:59], v32, s[4:7], s89 offen offset:1024 sc1\n\t" \
  "v_add_f32 v89, v89, v65\n\t" \
  "v_mfma_f32_16x16x32_bf16 v[140:143], v[156:159], a[160:163], v[140:143]\n\t" \
  "v_add_f32 v90, v90, v66\n\t" \
  "v_add_f32 v91, v91, v67\n\t" \
  "s_waitcnt vmcnt(15)\n\t" \
  "v_mfma_f32_16x16x32_bf16 v[128:131], v[160:163], a[36:39], v[128:131]\n\t" \
  "buffer_load_dwordx4 a[120:123], v54, s[4:7], s89 offen offset:2048 sc1\n\t" \
  "v_add_f32 v92, v92, v68\n\t" \
  "v_mfma_f32_16x16x32_bf16 v[132:135], v[164:167], a[36:39], v[132:135]\n\t" \
  "v_add_f32 v93, v93, v69\n\t" \
  "v_add_f32 v94, v94, v70\n\t" \
  "v_mfma_f32_16x16x32_bf16 v[136:139], v[0:3], a[36:39], v[136:139]\n\t" \
  "v_add_f32 v95, v95, v71\n\t" \
  "s_waitcnt lgkmcnt(2)\n\t" \
  "v_mfma_f32_16x16x32_bf16 v[140:143], v[4:7], a[36:39], v[140:143]\n\t" \
  "v_add_f32 v88, v88, v72\n\t" \
  "v_add_f32 v89, v89, v73\n\t" \
  "s_waitcnt vmcnt(15)\n\t" \
  "v_mfma_f32_16x16x32_bf16 v[128:131], v[192:195], a[100:103], v[128:131]\n\t" \
  "s_add_u32 s95, s49, 131072\n\t" \
  "buffer_store_dwordx2 v[56:57], v55, s[4:7], s95 offen offset:0 sc1\n\t" \
  "v_mfma_f32_16x16x32_bf16 v[132:135], v[196:199], a[100:103], v[132:135]\n\t" \
  "v_add_f32 v90, v90, v74\n\t" \
  "v_add_f32 v91, v91, v75\n\t" \
  "v_mfma_f32_16x16x32_bf16 v[136:139], v[200:203], a[100:103], v[136:139]\n\t" \
  "v_add_f32 v92, v92, v76\n\t" \
  "v_add_f32 v93, v93, v77\n\t" \
  "v_mfma_f32_16x16x32_bf16 v[140:143], v[204:207], a[100:103], v[140:143]\n\t" \
  "v_add_f32 v94, v94, v78\n\t" \
  "v_add_f32 v95, v95, v79\n\t" \
  "s_waitcnt vmcnt(15)\n\t" \
  "v_mfma_f32_16x16x32_bf16 v[128:131], v[208:211], a[164:167], v[128:131]\n\t" \
  "s_waitcnt lgkmcnt(0)\n\t" \
  "v_add_f32 v88, v88, v80\n\t" \
  "v_mfma_f32_16x16x32_bf16 v[132:135], v[212:215], a[164:167], v[132:135]\n\t" \
  "v_add_f32 v89, v89, v81\n\t" \
  "v_add_f32 v90, v90, v82\n\t" \
  "v_mfma_f32_16x16x32_bf16 v[136:139], v[216:219], a[164:167], v[136:139]\n\t" \
  "v_add_f32 v91, v91, v83\n\t" \
  "v_add_f32 v92, v92, v84\n\t" \
  "v_mfma_f32_16x16x32_bf16 v[140:143], v[220:223], a[164:167], v[140:143]\n\t" \
  "v_add_f32 v93, v93, v85\n\t" \
  "v_add_f32 v94, v94, v86\n\t" \
  "s_waitcnt vmcnt(14)\n\t" \
  "v_mfma_f32_16x16x32_bf16 v[128:131], v[224:227], a[40:43], v[128:131]\n\t" \
  "v_add_f32 v95, v95, v87\n\t" \
  "v_cvt_pk_bf16_f32 v64, v88, v89\n\t" \
  "v_mfma_f32_16x16x32_bf16 v[132:135], v[228:231], a[40:43], v[132:135]\n\t" \
  "v_cvt_pk_bf16_f32 v65, v90, v91\n\t" \
  "v_cvt_pk_bf16_f32 v66, v92, v93\n\t" \
  "v_mfma_f32_16x16x32_bf16 v[136:139], v[232:235], a[40:43], v[136:139]\n\t" \
  "v_cvt_pk_bf16_f32 v67, v94, v95\n\t" \
  "buffer_store_dwordx4 v[64:67], v43, s[12:15], 0 offen offset:0 sc1\n\t" \
  "v_mfma_f32_16x16x32_bf16 v[140:143], v[236:239], a[40:43], v[140:143]\n\t" \
  "s_waitcnt vmcnt(14)\n\t" \
  "v_mfma_f32_16x16x32_bf16 v[128:131], v[240:243], a[104:107], v[128:131]\n\t" \
  "v_mfma_f32_16x16x32_bf16 v[132:135], v[244:247], a[104:107], v[132:135]\n\t" \
  "v_mfma_f32_16x16x32_bf16 v[136:139], v[248:251], a[104:107], v[136:139]\n\t" \
  "v_mfma_f32_16x16x32_bf16 v[140:143], v[252:255], a[104:107], v[140:143]\n\t" \
  "s_waitcnt vmcnt(13)\n\t" \
  "v_mfma_f32_16x16x32_bf16 v[128:131], v[8:11], a[168:171], v[128:131]\n\t" \
  "v_mfma_f32_16x16x32_bf16 v[132:135], v[12:15], a[168:171], v[132:135]\n\t" \
  "v_mfma_f32_16x16x32_bf16 v[136:139], v[16:19], a[168:171], v[136:139]\n\t" \
  "v_mfma_f32_16x16x32_bf16 v[140:143], v[20:23], a[168:171], v[140:143]\n\t" \
  "s_waitcnt vmcnt(12)\n\t" \
  "v_mfma_f32_16x16x32_bf16 v[176:179], a[200:203], a[44:47], 0\n\t" \
  "v_mfma_f32_16x16x32_bf16 v[180:183], a[204:207], a[44:47], 0\n\t" \
  "s_waitcnt vmcnt(11)\n\t" \
  "v_mfma_f32_16x16x32_bf16 v[176:179], a[216:219], a[108:111], v[176:179]\n\t" \
  "v_mfma_f32_16x16x32_bf16 v[180:183], a[220:223], a[108:111], v[180:183]\n\t" \
  "s_waitcnt vmcnt(10)\n\t" \
  "v_mfma_f32_16x16x32_bf16 v[176:179], a[232:235], a[172:175], v[176:179]\n\t" \
  "v_cmp_u_f32 vcc, v128, v128\n\t" \
  "s_and_b64 vcc, exec, vcc\n\t" \
  "s_cbranch_scc1 Lrfa_%=\n\t" \
  "Lrfb_%=:\n\t" \
  "v_mfma_f32_16x16x32_bf16 v[180:183], a[236:239], a[172:175], v[180:183]\n\t" \
  "ds_write_b128 v62, v[128:131] offset:0\n\t" \
  "s_waitcnt vmcnt(9)\n\t" \
  "v_mfma_f32_16x16x32_bf16 v[176:179], a[248:251], a[48:51], v[176:179]\n\t" \
  "ds_write_b128 v62, v[132:135] offset:1024\n\t" \
  "v_mfma_f32_16x16x32_bf16 v[180:183], a[252:255], a[48:51], v[180:183]\n\t" \
  "ds_write_b128 v63, v[136:139] offset:0\n\t" \
  "s_waitcnt vmcnt(8)\n\t" \
  "v_mfma_f32_16x16x32_bf16 v[176:179], v[152:155], a[112:115], v[176:179]\n\t" \
  "ds_write_b128 v63, v[140:143] offset:1024\n\t" \
  "v_mfma_f32_16x16x32_bf16 v[180:183], v[156:159], a[112:115], v[180:183]\n\t" \
  "s_waitcnt vmcnt(7)\n\t" \
  "v_mfma_f32_16x16x32_bf16 v[176:179], v[0:3], a[176:179], v[176:179]\n\t" \
  "v_mfma_f32_16x16x32_bf16 v[180:183], v[4:7], a[176:179], v[180:183]\n\t" \
  "s_waitcnt vmcnt(6)\n\t" \
  "v_mfma_f32_16x16x32_bf16 v[176:179], v[200:203], a[52:55], v[176:179]\n\t" \
  "v_mfma_f32_16x16x32_bf16 v[180:183], v[204:207], a[52:55], v[180:183]\n\t" \
  "s_waitcnt vmcnt(5)\n\t" \
  "v_mfma_f32_16x16x32_bf16 v[176:179], v[216:219], a[116:119], v[176:179]\n\t" \
  "v_mfma_f32_16x16x32_bf16 v[180:183], v[220:223], a[116:119], v[180:183]\n\t" \
  "s_waitcnt vmcnt(4)\n\t" \
  "v_mfma_f32_16x16x32_bf16 v[176:179], v[232:235], a[180:183], v[176:179]\n\t" \
  "v_mfma_f32_16x16x32_bf16 v[180:183], v[236:239], a[180:183], v[180:183]\n\t" \
  "s_waitcnt vmcnt(3)\n\t" \
  "v_mfma_f32_16x16x32_bf16 v[176:179], v[248:251], a[56:59], v[176:179]\n\t" \
  "v_mfma_f32_16x16x32_bf16 v[180:183], v[252:255], a[56:59], v[180:183]\n\t" \
  "s_waitcnt vmcnt(2)\n\t" \
  "v_mfma_f32_16x16x32_bf16 v[176:179], v[16:19], a[120:123], v[176:179]\n\t" \
  "v_mfma_f32_16x16x32_bf16 v[180:183], v[20:23], a[120:123], v[180:183]\n\t" \
  "v_mfma_f32_16x16x32_bf16 v[168:171], a[192:195], a[44:47], 0\n\t" \
  "v_mfma_f32_16x16x32_bf16 v[172:175], a[196:199], a[44:47], 0\n\t" \
  "v_mfma_f32_16x16x32_bf16 v[168:171], a[208:211], a[108:111], v[168:171]\n\t" \
  "v_mfma_f32_16x16x32_bf16 v[172:175], a[212:215], a[108:111], v[172:175]\n\t" \
  "v_mfma_f32_16x16x32_bf16 v[168:171], a[224:227], a[172:175], v[168:171]\n\t" \
  "v_mfma_f32_16x16x32_bf16 v[172:175], a[228:231], a[172:175], v[172:175]\n\t" \
  "v_mfma_f32_16x16x32_bf16 v[168:171], a[240:243], a[48:51], v[168:171]\n\t" \
  "ds_write_b128 v63, v[176:179] offset:16384\n\t" \
  "v_mfma_f32_16x16x32_bf16 v[172:175], a[244:247], a[48:51], v[172:175]\n\t" \
  "ds_write_b128 v63, v[180:183] offset:17408\n\t" \
  "v_mfma_f32_16x16x32_bf16 v[168:171], v[144:147], a[112:115], v[168:171]\n\t" \
  "v_mfma_f32_16x16x32_bf16 v[172:175], v[148:151], a[112:115], v[172:175]\n\t" \
  "v_mfma_f32_16x16x32_bf16 v[168:171], v[160:163], a[176:179], v[168:171]\n\t" \
  "v_mfma_f32_16x16x32_bf16 v[172:175], v[164:167], a[176:179], v[172:175]\n\t" \
  "v_mfma_f32_16x16x32_bf16 v[168:171], v[192:195], a[52:55], v[168:171]\n\t" \
  "v_mfma_f32_16x16x32_bf16 v[172:175], v[196:199], a[52:55], v[172:175]\n\t" \
  "v_mfma_f32_16x16x32_bf16 v[168:171], v[208:211], a[116:119], v[168:171]\n\t" \
  "v_mfma_f32_16x16x32_bf16 v[172:175], v[212:215], a[116:119], v[172:175]\n\t" \
  "v_mfma_f32_16x16x32_bf16 v[168:171], v[224:227], a[180:183], v[168:171]\n\t" \
  "v_mfma_f32_16x16x32_bf16 v[172:175], v[228:231], a[180:183], v[172:175]\n\t" \
  "v_mfma_f32_16x16x32_bf16 v[168:171], v[240:243], a[56:59], v[168:171]\n\t" \
  "v_mfma_f32_16x16x32_bf16 v[172:175], v[244:247], a[56:59], v[172:175]\n\t" \
  "v_mfma_f32_16x16x32_bf16 v[168:171], v[8:11], a[120:123], v[168:171]\n\t" \
  "v_mfma_f32_16x16x32_bf16 v[172:175], v[12:15], a[120:123], v[172:175]\n\t" \
  "v_cmp_u_f32 vcc, v176, v176\n\t" \
  "s_and_b64 vcc, exec, vcc\n\t" \
  "s_cbranch_scc1 Lrsa_%=\n\t" \
  "Lrsb_%=:\n\t" \
  "v_lshlrev_b32 v120, 16, v28\n\t" \
  "v_and_b32 v121, 0xffff0000, v28\n\t" \
  "v_lshlrev_b32 v122, 16, v29\n\t" \
  "v_and_b32 v123, 0xffff0000, v29\n\t" \
  "v_lshlrev_b32 v124, 16, v30\n\t" \
  "v_and_b32 v125, 0xffff0000, v30\n\t" \
  "v_lshlrev_b32 v126, 16, v31\n\t" \
  "v_and_b32 v127, 0xffff0000, v31\n\t" \
  "ds_write_b128 v62, v[168:171] offset:16384\n\t" \
  "ds_write_b128 v62, v[172:175] offset:17408\n\t" \
  "s_waitcnt lgkmcnt(0)\n\t" \
  "s_barrier\n\t" \
  "ds_read_b128 v[88:91], v59 offset:0\n\t" \
  "ds_read_b128 v[92:95], v59 offset:1024\n\t" \
  "ds_read_b128 v[96:99], v59 offset:2048\n\t" \
  "ds_read_b128 v[100:103], v59 offset:3072\n\t" \
  "ds_read_b128 v[104:107], v59 offset:4096\n\t" \
  "ds_read_b128 v[108:111], v59 offset:5120\n\t" \
  "ds_read_b128 v[112:115], v59 offset:6144\n\t" \
  "ds_read_b128 v[116:119], v59 offset:7168\n\t" \
  "s_waitcnt lgkmcnt(6)\n\t" \
  "v_add_f32 v120, v120, v88\n\t" \
  "v_add_f32 v121, v121, v89\n\t" \
  "v_add_f32 v122, v122, v90\n\t" \
  "v_add_f32 v123, v123, v91\n\t" \
  "v_add_f32 v124, v124, v92\n\t" \
  "v_add_f32 v125, v125, v93\n\t" \
  "v_add_f32 v126, v126, v94\n\t" \
  "v_add_f32 v127, v127, v95\n\t" \
  "s_waitcnt lgkmcnt(4)\n\t" \
  "v_add_f32 v120, v120, v96\n\t" \
  "v_add_f32 v121, v121, v97\n\t" \
  "v_add_f32 v122, v122, v98\n\t" \
  "v_add_f32 v123, v123, v99\n\t" \
  "v_add_f32 v124, v124, v100\n\t" \
  "v_add_f32 v125, v125, v101\n\t" \
  "v_add_f32 v126, v126, v102\n\t" \
  "v_add_f32 v127, v127, v103\n\t" \
  "s_waitcnt lgkmcnt(2)\n\t" \
  "v_add_f32 v120, v120, v104\n\t" \
  "v_add_f32 v121, v121, v105\n\t" \
  "v_add_f32 v122, v122, v106\n\t" \
  "v_add_f32 v123, v123, v107\n\t" \
  "v_add_f32 v124, v124, v108\n\t" \
  "v_add_f32 v125, v125, v109\n\t" \
  "v_add_f32 v126, v126, v110\n\t" \
  "v_add_f32 v127, v127, v111\n\t" \
  "s_waitcnt lgkmcnt(0)\n\t" \
  "v_add_f32 v120, v120, v112\n\t" \
  "v_add_f32 v121, v121, v113\n\t" \
  "v_add_f32 v122, v122, v114\n\t" \
  "v_add_f32 v123, v123, v115\n\t" \
  "v_add_f32 v124, v124, v116\n\t" \
  "v_add_f32 v125, v125, v117\n\t" \
  "v_add_f32 v126, v126, v118\n\t" \
  "v_add_f32 v127, v127, v119\n\t" \
  "v_cvt_pk_bf16_f32 v64, v120, v121\n\t" \
  "v_cvt_pk_bf16_f32 v65, v122, v123\n\t" \
  "v_cvt_pk_bf16_f32 v66, v124, v125\n\t" \
  "v_cvt_pk_bf16_f32 v67, v126, v127\n\t" \
  "s_mov_b32 s94, 32768\n\t" \
  "buffer_store_dwordx4 v[64:67], v43, s[12:15], s94 offen offset:0 sc1\n\t" \
  "s_nop 1\n\t" \
  "s_branch Lend_%=\n\t" \
  "Lroa_%=:\n\t" \
  "s_mov_b32 s95, 131072\n\t" \
  "s_movk_i32 s90, 0x7fff\n\t" \
  "s_waitcnt lgkmcnt(0)\n\t" \
  "s_mov_b64 s[34:35], -1\n\t" \
  "Lroc_%=:\n\t" \
  "buffer_load_dwordx2 v[60:61], v38, s[4:7], s95 offen sc1\n\t" \
  "s_add_u32 s88, s41, 32768\n\t" \
  "buffer_load_dwordx4 v[64:67], v32, s[4:7], s88 offen offset:0 sc1\n\t" \
  "buffer_load_dwordx4 v[68:71], v32, s[4:7], s88 offen offset:1024 sc1\n\t" \
  "buffer_load_dwordx4 v[72:75], v32, s[4:7], s88 offen offset:2048 sc1\n\t" \
  "buffer_load_dwordx4 v[76:79], v32, s[4:7], s88 offen offset:3072 sc1\n\t" \
  "s_add_u32 s89, s40, 32768\n\t" \
  "buffer_load_dwordx4 v[80:83], v32, s[4:7], s89 offen offset:0 sc1\n\t" \
  "buffer_load_dwordx4 v[84:87], v32, s[4:7], s89 offen offset:1024 sc1\n\t" \
  "buffer_load_dwordx4 v[88:91], v32, s[4:7], s89 offen offset:2048 sc1\n\t" \
  "buffer_load_dwordx4 v[92:95], v32, s[4:7], s89 offen offset:3072 sc1\n\t" \
  "s_waitcnt vmcnt(0)\n\t" \
  "v_mfma_f32_16x16x32_bf16 v[104:107], a[192:195], v[80:83], 0\n\t" \
  "v_mfma_f32_16x16x32_bf16 v[104:107], a[208:211], v[84:87], v[104:107]\n\t" \
  "v_mfma_f32_16x16x32_bf16 v[104:107], a[224:227], v[88:91], v[104:107]\n\t" \
  "v_mfma_f32_16x16x32_bf16 v[104:107], a[240:243], v[92:95], v[104:107]\n\t" \
  "v_mfma_f32_16x16x32_bf16 v[104:107], a[192:195], v[64:67], v[104:107]\n\t" \
  "v_mfma_f32_16x16x32_bf16 v[104:107], a[208:211], v[68:71], v[104:107]\n\t" \
  "v_mfma_f32_16x16x32_bf16 v[104:107], a[224:227], v[72:75], v[104:107]\n\t" \
  "v_mfma_f32_16x16x32_bf16 v[104:107], a[240:243], v[76:79], v[104:107]\n\t" \
  "s_cmp_eq_u64 s[34:35], 0\n\t" \
  "s_cbranch_scc1 Lrod_%=\n\t" \
  "v_cmp_ne_u64 s[34:35], s[54:55], v[60:61]\n\t" \
  "s_nop 4\n\t" \
  "v_cmp_u_f32 vcc, v104, v104\n\t" \
  "s_and_b64 vcc, exec, vcc\n\t" \
  "s_cbranch_scc0 Lrod_%=\n\t" \
  "s_sub_u32 s90, s90, 1\n\t" \
  "s_cbranch_scc0 Lroc_%=\n\t" \
  "Lrod_%=:\n\t" \
  "s_nop 7\n\t" \
  "v_mfma_f32_16x16x32_bf16 v[120:123], a[192:195], v[64:67], 0\n\t" \
  "v_mfma_f32_16x16x32_bf16 v[124:127], a[196:199], v[64:67], 0\n\t" \
  "v_mfma_f32_16x16x32_bf16 v[128:131], a[200:203], v[64:67], 0\n\t" \
  "v_mfma_f32_16x16x32_bf16 v[132:135], a[204:207], v[64:67], 0\n\t" \
  "v_mfma_f32_16x16x32_bf16 v[120:123], a[208:211], v[68:71], v[120:123]\n\t" \
  "v_mfma_f32_16x16x32_bf16 v[124:127], a[212:215], v[68:71], v[124:127]\n\t" \
  "v_mfma_f32_16x16x32_bf16 v[128:131], a[216:219], v[68:71], v[128:131]\n\t" \
  "v_mfma_f32_16x16x32_bf16 v[132:135], a[220:223], v[68:71], v[132:135]\n\t" \
  "v_mfma_f32_16x16x32_bf16 v[120:123], a[224:227], v[72:75], v[120:123]\n\t" \
  "v_mfma_f32_16x16x32_bf16 v[124:127], a[228:231], v[72:75], v[124:127]\n\t" \
  "v_mfma_f32_16x16x32_bf16 v[128:131], a[232:235], v[72:75], v[128:131]\n\t" \
  "v_mfma_f32_16x16x32_bf16 v[132:135], a[236:239], v[72:75], v[132:135]\n\t" \
  "v_mfma_f32_16x16x32_bf16 v[120:123], a[240:243], v[76:79], v[120:123]\n\t" \
  "v_mfma_f32_16x16x32_bf16 v[124:127], a[244:247], v[76:79], v[124:127]\n\t" \
  "v_mfma_f32_16x16x32_bf16 v[128:131], a[248:251], v[76:79], v[128:131]\n\t" \
  "v_mfma_f32_16x16x32_bf16 v[132:135], a[252:255], v[76:79], v[132:135]\n\t" \
  "v_mfma_f32_16x16x32_bf16 v[104:107], a[192:195], v[80:83], 0\n\t" \
  "v_mfma_f32_16x16x32_bf16 v[108:111], a[196:199], v[80:83], 0\n\t" \
  "v_mfma_f32_16x16x32_bf16 v[112:115], a[200:203], v[80:83], 0\n\t" \
  "v_mfma_f32_16x16x32_bf16 v[116:119], a[204:207], v[80:83], 0\n\t" \
  "v_mfma_f32_16x16x32_bf16 v[104:107], a[208:211], v[84:87], v[104:107]\n\t" \
  "v_mfma_f32_16x16x32_bf16 v[108:111], a[212:215], v[84:87], v[108:111]\n\t" \
  "v_mfma_f32_16x16x32_bf16 v[112:115], a[216:219], v[84:87], v[112:115]\n\t" \
  "v_mfma_f32_16x16x32_bf16 v[116:119], a[220:223], v[84:87], v[116:119]\n\t" \
  "v_mfma_f32_16x16x32_bf16 v[104:107], a[224:227], v[88:91], v[104:107]\n\t" \
  "v_mfma_f32_16x16x32_bf16 v[108:111], a[228:231], v[88:91], v[108:111]\n\t" \
  "v_mfma_f32_16x16x32_bf16 v[112:115], a[232:235], v[88:91], v[112:115]\n\t" \
  "v_mfma_f32_16x16x32_bf16 v[116:119], a[236:239], v[88:91], v[116:119]\n\t" \
  "v_mfma_f32_16x16x32_bf16 v[104:107], a[240:243], v[92:95], v[104:107]\n\t" \
  "v_mfma_f32_16x16x32_bf16 v[108:111], a[244:247], v[92:95], v[108:111]\n\t" \
  "v_mfma_f32_16x16x32_bf16 v[112:115], a[248:251], v[92:95], v[112:115]\n\t" \
  "v_mfma_f32_16x16x32_bf16 v[116:119], a[252:255], v[92:95], v[116:119]\n\t" \
  "ds_write_b128 v40, v[120:123] offset:0\n\t" \
  "ds_write_b128 v40, v[124:127] offset:1024\n\t" \
  "ds_write_b128 v41, v[128:131] offset:0\n\t" \
  "ds_write_b128 v41, v[132:135] offset:1024\n\t" \
  "s_nop 2\n\t" \
  "ds_write_b128 v39, v[112:115] offset:0\n\t" \
  "ds_write_b128 v39, v[116:119] offset:1024\n\t" \
  "s_nop 7\n\t" \
  "s_branch Lrob_%=\n\t" \
  "Lvaa_%=:\n\t" \
  "s_movk_i32 s90, 0x7fff\n\t" \
  "s_mov_b64 s[34:35], -1\n\t" \
  "s_mov_b32 s95, m0\n\t" \
  "s_mov_b32 s88, s89\n\t" \
  "Lvac_%=:\n\t" \
  "buffer_load_dwordx2 v[60:61], v45, s[4:7], 0 offen sc1\n\t" \
  "buffer_load_dwordx2 v[184:185], v46, s[4:7], s47 offen offset:0 sc1\n\t" \
  "buffer_load_dwordx2 v[186:187], v46, s[4:7], s47 offen offset:128 sc1\n\t" \
  "buffer_load_dwordx2 v[188:189], v46, s[4:7], s47 offen offset:256 sc1\n\t" \
  "buffer_load_dwordx2 v[190:191], v46, s[4:7], s47 offen offset:384 sc1\n\t" \
  "s_add_u32 m0, s46, 0\n\t" \
  "s_add_u32 s89, s45, 0\n\t" \
  "buffer_load_dwordx4 v32, s[4:7], s89 offen offset:0 sc1 lds\n\t" \
  "buffer_load_dwordx4 v32, s[4:7], s89 offen offset:1024 sc1 lds\n\t" \
  "buffer_load_dwordx4 v32, s[4:7], s89 offen offset:2048 sc1 lds\n\t" \
  "buffer_load_dwordx4 v32, s[4:7], s89 offen offset:3072 sc1 lds\n\t" \
  "s_add_u32 m0, s46, 16384\n\t" \
  "s_add_u32 s89, s45, 65536\n\t" \
  "buffer_load_dwordx4 v32, s[4:7], s89 offen offset:0 sc1 lds\n\t" \
  "buffer_load_dwordx4 v32, s[4:7], s89 offen offset:1024 sc1 lds\n\t" \
  "buffer_load_dwordx4 v32, s[4:7], s89 offen offset:2048 sc1 lds\n\t" \
  "buffer_load_dwordx4 v32, s[4:7], s89 offen offset:3072 sc1 lds\n\t" \
  "s_waitcnt vmcnt(0) lgkmcnt(0)\n\t" \
  "ds_read_b128 v[88:91], v62 offset:0\n\t" \
  "ds_read_b128 v[92:95], v62 offset:1024\n\t" \
  "ds_read_b128 v[96:99], v62 offset:2048\n\t" \
  "ds_read_b128 v[100:103], v62 offset:3072\n\t" \
  "ds_read_b128 v[104:107], v62 offset:16384\n\t" \
  "ds_read_b128 v[108:111], v62 offset:17408\n\t" \
  "ds_read_b128 v[112:115], v62 offset:18432\n\t" \
  "ds_read_b128 v[116:119], v62 offset:19456\n\t" \
  "s_waitcnt lgkmcnt(4)\n\t" \
  "v_max3_u32 v120, v88, v89, v88\n\t" \
  "v_max3_u32 v120, v90, v91, v120\n\t" \
  "v_max3_u32 v120, v92, v93, v120\n\t" \
  "v_max3_u32 v120, v94, v95, v120\n\t" \
  "v_max3_u32 v120, v96, v97, v120\n\t" \
  "v_max3_u32 v120, v98, v99, v120\n\t" \
  "v_max3_u32 v120, v100, v101, v120\n\t" \
  "v_max3_u32 v120, v102, v103, v120\n\t" \
  "s_waitcnt lgkmcnt(0)\n\t" \
  "v_max3_u32 v120, v104, v105, v120\n\t" \
  "v_max3_u32 v120, v106, v107, v120\n\t" \
  "v_max3_u32 v120, v108, v109, v120\n\t" \
  "v_max3_u32 v120, v110, v111, v120\n\t" \
  "v_max3_u32 v120, v112, v113, v120\n\t" \
  "v_max3_u32 v120, v114, v115, v120\n\t" \
  "v_max3_u32 v120, v116, v117, v120\n\t" \
  "v_max3_u32 v120, v118, v119, v120\n\t" \
  "v_add_f32 v184, v184, v186\n\t" \
  "v_add_f32 v185, v185, v187\n\t" \
  "v_add_f32 v188, v188, v190\n\t" \
  "v_add_f32 v189, v189, v191\n\t" \
  "v_add_f32 v184, v184, v188\n\t" \
  "v_add_f32 v185, v185, v189\n\t" \
  "s_cmp_eq_u64 s[34:35], 0\n\t" \
  "s_cbranch_scc1 Lvad_%=\n\t" \
  "v_cmp_ne_u64 s[34:35], s[54:55], v[60:61]\n\t" \
  "v_cmp_eq_u32 vcc, -1, v120\n\t" \
  "v_cmp_u_f32 s[32:33], v184, v185\n\t" \
  "s_or_b64 vcc, vcc, s[32:33]\n\t" \
  "s_and_b64 vcc, exec, vcc\n\t" \
  "s_cbranch_scc0 Lvad_%=\n\t" \
  "s_sub_u32 s90, s90, 1\n\t" \
  "s_cbranch_scc0 Lvac_%=\n\t" \
  "Lvad_%=:\n\t" \
  "s_mov_b32 m0, s95\n\t" \
  "s_mov_b32 s89, s88\n\t" \
  "s_branch Lvab_%=\n\t" \
  "Lvba_%=:\n\t" \
  "s_movk_i32 s90, 0x7fff\n\t" \
  "s_mov_b64 s[34:35], -1\n\t" \
  "s_mov_b32 s95, m0\n\t" \
  "s_mov_b32 s88, s89\n\t" \
  "Lvbc_%=:\n\t" \
  "buffer_load_dwordx2 v[60:61], v45, s[4:7], 0 offen sc1\n\t" \
  "s_add_u32 m0, s46, 32768\n\t" \
  "s_add_u32 s89, s45, 16384\n\t" \
  "buffer_load_dwordx4 v32, s[4:7], s89 offen offset:0 sc1 lds\n\t" \
  "buffer_load_dwordx4 v32, s[4:7], s89 offen offset:1024 sc1 lds\n\t" \
  "buffer_load_dwordx4 v32, s[4:7], s89 offen offset:2048 sc1 lds\n\t" \
  "buffer_load_dwordx4 v32, s[4:7], s89 offen offset:3072 sc1 lds\n\t" \
  "s_add_u32 m0, s46, 49152\n\t" \
  "s_add_u32 s89, s45, 81920\n\t" \
  "buffer_load_dwordx4 v32, s[4:7], s89 offen offset:0 sc1 lds\n\t" \
  "buffer_load_dwordx4 v32, s[4:7], s89 offen offset:1024 sc1 lds\n\t" \
  "buffer_load_dwordx4 v32, s[4:7], s89 offen offset:2048 sc1 lds\n\t" \
  "buffer_load_dwordx4 v32, s[4:7], s89 offen offset:3072 sc1 lds\n\t" \
  "s_waitcnt vmcnt(0) lgkmcnt(0)\n\t" \
  "ds_read_b128 v[128:131], v62 offset:32768\n\t" \
  "ds_read_b128 v[132:135], v62 offset:33792\n\t" \
  "ds_read_b128 v[136:139], v62 offset:34816\n\t" \
  "ds_read_b128 v[140:143], v62 offset:35840\n\t" \
  "s_waitcnt lgkmcnt(0)\n\t" \
  "v_max3_u32 v87, v128, v129, v128\n\t" \
  "v_max3_u32 v87, v130, v131, v87\n\t" \
  "v_max3_u32 v87, v132, v133, v87\n\t" \
  "v_max3_u32 v87, v134, v135, v87\n\t" \
  "v_max3_u32 v87, v136, v137, v87\n\t" \
  "v_max3_u32 v87, v138, v139, v87\n\t" \
  "v_max3_u32 v87, v140, v141, v87\n\t" \
  "v_max3_u32 v87, v142, v143, v87\n\t" \
  "ds_read_b128 v[128:131], v62 offset:49152\n\t" \
  "ds_read_b128 v[132:135], v62 offset:50176\n\t" \
  "ds_read_b128 v[136:139], v62 offset:51200\n\t" \
  "ds_read_b128 v[140:143], v62 offset:52224\n\t" \
  "s_waitcnt lgkmcnt(0)\n\t" \
  "v_max3_u32 v87, v128, v129, v87\n\t" \
  "v_max3_u32 v87, v130, v131, v87\n\t" \
  "v_max3_u32 v87, v132, v133, v87\n\t" \
  "v_max3_u32 v87, v134, v135, v87\n\t" \
  "v_max3_u32 v87, v136, v137, v87\n\t" \
  "v_max3_u32 v87, v138, v139, v87\n\t" \
  "v_max3_u32 v87, v140, v141, v87\n\t" \
  "v_max3_u32 v87, v142, v143, v87\n\t" \
  "s_cmp_eq_u64 s[34:35], 0\n\t" \
  "s_cbranch_scc1 Lvbd_%=\n\t" \
  "v_cmp_ne_u64 s[34:35], s[54:55], v[60:61]\n\t" \
  "v_cmp_eq_u32 vcc, -1, v87\n\t" \
  "s_and_b64 vcc, exec, vcc\n\t" \
  "s_cbranch_scc0 Lvbd_%=\n\t" \
  "s_sub_u32 s90, s90, 1\n\t" \
  "s_cbranch_scc0 Lvbc_%=\n\t" \
  "Lvbd_%=:\n\t" \
  "s_mov_b32 m0, s95\n\t" \
  "s_mov_b32 s89, s88\n\t" \
  "s_branch Lvbb_%=\n\t" \
  "Lrfa_%=:\n\t" \
  "s_mov_b32 s95, 131072\n\t" \
  "s_movk_i32 s90, 0x7fff\n\t" \
  "s_waitcnt lgkmcnt(0)\n\t" \
  "s_mov_b64 s[34:35], -1\n\t" \
  "Lrfc_%=:\n\t" \
  "buffer_load_dwordx2 v[60:61], v53, s[4:7], s95 offen sc1\n\t" \
  "s_add_u32 s89, s51, 90112\n\t" \
  "buffer_load_dwordx4 a[92:95], v32, s[4:7], s89 offen offset:0 sc1\n\t" \
  "buffer_load_dwordx4 a[156:159], v32, s[4:7], s89 offen offset:1024 sc1\n\t" \
  "buffer_load_dwordx4 a[32:35], v32, s[4:7], s89 offen offset:2048 sc1\n\t" \
  "buffer_load_dwordx4 a[96:99], v32, s[4:7], s89 offen offset:3072 sc1\n\t" \
  "s_add_u32 s88, s51, 94208\n\t" \
  "buffer_load_dwordx4 a[160:163], v32, s[4:7], s88 offen offset:0 sc1\n\t" \
  "buffer_load_dwordx4 a[36:39], v32, s[4:7], s88 offen offset:1024 sc1\n\t" \
  "buffer_load_dwordx4 a[100:103], v32, s[4:7], s88 offen offset:2048 sc1\n\t" \
  "buffer_load_dwordx4 a[164:167], v32, s[4:7], s88 offen offset:3072 sc1\n\t" \
  "s_add_u32 s89, s51, 98304\n\t" \
  "buffer_load_dwordx4 a[40:43], v32, s[4:7], s89 offen offset:0 sc1\n\t" \
  "buffer_load_dwordx4 a[104:107], v32, s[4:7], s89 offen offset:1024 sc1\n\t" \
  "buffer_load_dwordx4 a[168:171], v54, s[4:7], s89 offen offset:2048 sc1\n\t" \
  "s_waitcnt vmcnt(0)\n\t" \
  "v_mfma_f32_16x16x32_bf16 v[128:131], a[192:195], a[92:95], 0\n\t" \
  "v_mfma_f32_16x16x32_bf16 v[128:131], a[208:211], a[156:159], v[128:131]\n\t" \
  "v_mfma_f32_16x16x32_bf16 v[128:131], a[224:227], a[32:35], v[128:131]\n\t" \
  "v_mfma_f32_16x16x32_bf16 v[128:131], a[240:243], a[96:99], v[128:131]\n\t" \
  "v_mfma_f32_16x16x32_bf16 v[128:131], v[144:147], a[160:163], v[128:131]\n\t" \
  "v_mfma_f32_16x16x32_bf16 v[128:131], v[160:163], a[36:39], v[128:131]\n\t" \
  "v_mfma_f32_16x16x32_bf16 v[128:131], v[192:195], a[100:103], v[128:131]\n\t" \
  "v_mfma_f32_16x16x32_bf16 v[128:131], v[208:211], a[164:167], v[128:131]\n\t" \
  "v_mfma_f32_16x16x32_bf16 v[128:131], v[224:227], a[40:43], v[128:131]\n\t" \
  "v_mfma_f32_16x16x32_bf16 v[128:131], v[240:243], a[104:107], v[128:131]\n\t" \
  "v_mfma_f32_16x16x32_bf16 v[128:131], v[8:11], a[168:171], v[128:131]\n\t" \
  "s_cmp_eq_u64 s[34:35], 0\n\t" \
  "s_cbranch_scc1 Lrfd_%=\n\t" \
  "v_cmp_ne_u64 s[34:35], s[54:55], v[60:61]\n\t" \
  "s_nop 4\n\t" \
  "v_cmp_u_f32 vcc, v128, v128\n\t" \
  "s_and_b64 vcc, exec, vcc\n\t" \
  "s_cbranch_scc0 Lrfd_%=\n\t" \
  "s_sub_u32 s90, s90, 1\n\t" \
  "s_cbranch_scc0 Lrfc_%=\n\t" \
  "Lrfd_%=:\n\t" \
  "s_nop 7\n\t" \
  "v_mfma_f32_16x16x32_bf16 v[128:131], a[192:195], a[92:95], 0\n\t" \
  "v_mfma_f32_16x16x32_bf16 v[132:135], a[196:199], a[92:95], 0\n\t" \
  "v_mfma_f32_16x16x32_bf16 v[136:139], a[200:203], a[92:95], 0\n\t" \
  "v_mfma_f32_16x16x32_bf16 v[140:143], a[204:207], a[92:95], 0\n\t" \
  "v_mfma_f32_16x16x32_bf16 v[128:131], a[208:211], a[156:159], v[128:131]\n\t" \
  "v_mfma_f32_16x16x32_bf16 v[132:135], a[212:215], a[156:159], v[132:135]\n\t" \
  "v_mfma_f32_16x16x32_bf16 v[136:139], a[216:219], a[156:159], v[136:139]\n\t" \
  "v_mfma_f32_16x16x32_bf16 v[140:143], a[220:223], a[156:159], v[140:143]\n\t" \
  "v_mfma_f32_16x16x32_bf16 v[128:131], a[224:227], a[32:35], v[128:131]\n\t" \
  "v_mfma_f32_16x16x32_bf16 v[132:135], a[228:231], a[32:35], v[132:135]\n\t" \
  "v_mfma_f32_16x16x32_bf16 v[136:139], a[232:235], a[32:35], v[136:139]\n\t" \
  "v_mfma_f32_16x16x32_bf16 v[140:143], a[236:239], a[32:35], v[140:143]\n\t" \
  "v_mfma_f32_16x16x32_bf16 v[128:131], a[240:243], a[96:99], v[128:131]\n\t" \
  "v_mfma_f32_16x16x32_bf16 v[132:135], a[244:247], a[96:99], v[132:135]\n\t" \
  "v_mfma_f32_16x16x32_bf16 v[136:139], a[248:251], a[96:99], v[136:139]\n\t" \
  "v_mfma_f32_16x16x32_bf16 v[140:143], a[252:255], a[96:99], v[140:143]\n\t" \
  "v_mfma_f32_16x16x32_bf16 v[128:131], v[144:147], a[160:163], v[128:131]\n\t" \
  "v_mfma_f32_16x16x32_bf16 v[132:135], v[148:151], a[160:163], v[132:135]\n\t" \
  "v_mfma_f32_16x16x32_bf16 v[136:139], v[152:155], a[160:163], v[136:139]\n\t" \
  "v_mfma_f32_16x16x32_bf16 v[140:143], v[156:159], a[160:163], v[140:143]\n\t" \
  "v_mfma_f32_16x16x32_bf16 v[128:131], v[160:163], a[36:39], v[128:131]\n\t" \
  "v_mfma_f32_16x16x32_bf16 v[132:135], v[164:167], a[36:39], v[132:135]\n\t" \
  "v_mfma_f32_16x16x32_bf16 v[136:139], v[0:3], a[36:39], v[136:139]\n\t" \
  "v_mfma_f32_16x16x32_bf16 v[140:143], v[4:7], a[36:39], v[140:143]\n\t" \
  "v_mfma_f32_16x16x32_bf16 v[128:131], v[192:195], a[100:103], v[128:131]\n\t" \
  "v_mfma_f32_16x16x32_bf16 v[132:135], v[196:199], a[100:103], v[132:135]\n\t" \
  "v_mfma_f32_16x16x32_bf16 v[136:139], v[200:203], a[100:103], v[136:139]\n\t" \
  "v_mfma_f32_16x16x32_bf16 v[140:143], v[204:207], a[100:103], v[140:143]\n\t" \
  "v_mfma_f32_16x16x32_bf16 v[128:131], v[208:211], a[164:167], v[128:131]\n\t" \
  "v_mfma_f32_16x16x32_bf16 v[132:135], v[212:215], a[164:167], v[132:135]\n\t" \
  "v_mfma_f32_16x16x32_bf16 v[136:139], v[216:219], a[164:167], v[136:139]\n\t" \
  "v_mfma_f32_16x16x32_bf16 v[140:143], v[220:223], a[164:167], v[140:143]\n\t" \
  "v_mfma_f32_16x16x32_bf16 v[128:131], v[224:227], a[40:43], v[128:131]\n\t" \
  "v_mfma_f32_16x16x32_bf16 v[132:135], v[228:231], a[40:43], v[132:135]\n\t" \
  "v_mfma_f32_16x16x32_bf16 v[136:139], v[232:235], a[40:43], v[136:139]\n\t" \
  "v_mfma_f32_16x16x32_bf16 v[140:143], v[236:239], a[40:43], v[140:143]\n\t" \
  "v_mfma_f32_16x16x32_bf16 v[128:131], v[240:243], a[104:107], v[128:131]\n\t" \
  "v_mfma_f32_16x16x32_bf16 v[132:135], v[244:247], a[104:107], v[132:135]\n\t" \
  "v_mfma_f32_16x16x32_bf16 v[136:139], v[248:251], a[104:107], v[136:139]\n\t" \
  "v_mfma_f32_16x16x32_bf16 v[140:143], v[252:255], a[104:107], v[140:143]\n\t" \
  "v_mfma_f32_16x16x32_bf16 v[128:131], v[8:11], a[168:171], v[128:131]\n\t" \
  "v_mfma_f32_16x16x32_bf16 v[132:135], v[12:15], a[168:171], v[132:135]\n\t" \
  "v_mfma_f32_16x16x32_bf16 v[136:139], v[16:19], a[168:171], v[136:139]\n\t" \
  "v_mfma_f32_16x16x32_bf16 v[140:143], v[20:23], a[168:171], v[140:143]\n\t" \
  "s_nop 7\n\t" \
  "s_branch Lrfb_%=\n\t" \
  "Lrsa_%=:\n\t" \
  "s_mov_b32 s95, 131072\n\t" \
  "s_movk_i32 s90, 0x7fff\n\t" \
  "s_waitcnt lgkmcnt(0)\n\t" \
  "s_mov_b64 s[34:35], -1\n\t" \
  "Lrsc_%=:\n\t" \
  "buffer_load_dwordx2 v[60:61], v53, s[4:7], s95 offen sc1\n\t" \
  "s_add_u32 s89, s51, 135168\n\t" \
  "buffer_load_dwordx4 a[44:47], v32, s[4:7], s89 offen offset:0 sc1\n\t" \
  "buffer_load_dwordx4 a[108:111], v32, s[4:7], s89 offen offset:1024 sc1\n\t" \
  "buffer_load_dwordx4 a[172:175], v32, s[4:7], s89 offen offset:2048 sc1\n\t" \
  "buffer_load_dwordx4 a[48:51], v32, s[4:7], s89 offen offset:3072 sc1\n\t" \
  "s_add_u32 s88, s51, 139264\n\t" \
  "buffer_load_dwordx4 a[112:115], v32, s[4:7], s88 offen offset:0 sc1\n\t" \
  "buffer_load_dwordx4 a[176:179], v32, s[4:7], s88 offen offset:1024 sc1\n\t" \
  "buffer_load_dwordx4 a[52:55], v32, s[4:7], s88 offen offset:2048 sc1\n\t" \
  "buffer_load_dwordx4 a[116:119], v32, s[4:7], s88 offen offset:3072 sc1\n\t" \
  "s_add_u32 s89, s51, 143360\n\t" \
  "buffer_load_dwordx4 a[180:183], v32, s[4:7], s89 offen offset:0 sc1\n\t" \
  "buffer_load_dwordx4 a[56:59], v32, s[4:7], s89 offen offset:1024 sc1\n\t" \
  "buffer_load_dwordx4 a[120:123], v54, s[4:7], s89 offen offset:2048 sc1\n\t" \
  "s_waitcnt vmcnt(0)\n\t" \
  "v_mfma_f32_16x16x32_bf16 v[168:171], a[192:195], a[44:47], 0\n\t" \
  "v_mfma_f32_16x16x32_bf16 v[168:171], a[208:211], a[108:111], v[168:171]\n\t" \
  "v_mfma_f32_16x16x32_bf16 v[168:171], a[224:227], a[172:175], v[168:171]\n\t" \
  "v_mfma_f32_16x16x32_bf16 v[168:171], a[240:243], a[48:51], v[168:171]\n\t" \
  "v_mfma_f32_16x16x32_bf16 v[168:171], v[144:147], a[112:115], v[168:171]\n\t" \
  "v_mfma_f32_16x16x32_bf16 v[168:171], v[160:163], a[176:179], v[168:171]\n\t" \
  "v_mfma_f32_16x16x32_bf16 v[168:171], v[192:195], a[52:55], v[168:171]\n\t" \
  "v_mfma_f32_16x16x32_bf16 v[168:171], v[208:211], a[116:119], v[168:171]\n\t" \
  "v_mfma_f32_16x16x32_bf16 v[168:171], v[224:227], a[180:183], v[168:171]\n\t" \
  "v_mfma_f32_16x16x32_bf16 v[168:171], v[240:243], a[56:59], v[168:171]\n\t" \
  "v_mfma_f32_16x16x32_bf16 v[168:171], v[8:11], a[120:123], v[168:171]\n\t" \
  "s_cmp_eq_u64 s[34:35], 0\n\t" \
  "s_cbranch_scc1 Lrsd_%=\n\t" \
  "v_cmp_ne_u64 s[34:35], s[54:55], v[60:61]\n\t" \
  "s_nop 4\n\t" \
  "v_cmp_u_f32 vcc, v168, v168\n\t" \
  "s_and_b64 vcc, exec, vcc\n\t" \
  "s_cbranch_scc0 Lrsd_%=\n\t" \
  "s_sub_u32 s90, s90, 1\n\t" \
  "s_cbranch_scc0 Lrsc_%=\n\t" \
  "Lrsd_%=:\n\t" \
  "s_nop 7\n\t" \
  "v_mfma_f32_16x16x32_bf16 v[168:171], a[192:195], a[44:47], 0\n\t" \
  "v_mfma_f32_16x16x32_bf16 v[172:175], a[196:199], a[44:47], 0\n\t" \
  "v_mfma_f32_16x16x32_bf16 v[176:179], a[200:203], a[44:47], 0\n\t" \
  "v_mfma_f32_16x16x32_bf16 v[180:183], a[204:207], a[44:47], 0\n\t" \
  "v_mfma_f32_16x16x32_bf16 v[168:171], a[208:211], a[108:111], v[168:171]\n\t" \
  "v_mfma_f32_16x16x32_bf16 v[172:175], a[212:215], a[108:111], v[172:175]\n\t" \
  "v_mfma_f32_16x16x32_bf16 v[176:179], a[216:219], a[108:111], v[176:179]\n\t" \
  "v_mfma_f32_16x16x32_bf16 v[180:183], a[220:223], a[108:111], v[180:183]\n\t" \
  "v_mfma_f32_16x16x32_bf16 v[168:171], a[224:227], a[172:175], v[168:171]\n\t" \
  "v_mfma_f32_16x16x32_bf16 v[172:175], a[228:231], a[172:175], v[172:175]\n\t" \
  "v_mfma_f32_16x16x32_bf16 v[176:179], a[232:235], a[172:175], v[176:179]\n\t" \
  "v_mfma_f32_16x16x32_bf16 v[180:183], a[236:239], a[172:175], v[180:183]\n\t" \
  "v_mfma_f32_16x16x32_bf16 v[168:171], a[240:243], a[48:51], v[168:171]\n\t" \
  "v_mfma_f32_16x16x32_bf16 v[172:175], a[244:247], a[48:51], v[172:175]\n\t" \
  "v_mfma_f32_16x16x32_bf16 v[176:179], a[248:251], a[48:51], v[176:179]\n\t" \
  "v_mfma_f32_16x16x32_bf16 v[180:183], a[252:255], a[48:51], v[180:183]\n\t" \
  "v_mfma_f32_16x16x32_bf16 v[168:171], v[144:147], a[112:115], v[168:171]\n\t" \
  "v_mfma_f32_16x16x32_bf16 v[172:175], v[148:151], a[112:115], v[172:175]\n\t" \
  "v_mfma_f32_16x16x32_bf16 v[176:179], v[152:155], a[112:115], v[176:179]\n\t" \
  "v_mfma_f32_16x16x32_bf16 v[180:183], v[156:159], a[112:115], v[180:183]\n\t" \
  "v_mfma_f32_16x16x32_bf16 v[168:171], v[160:163], a[176:179], v[168:171]\n\t" \
  "v_mfma_f32_16x16x32_bf16 v[172:175], v[164:167], a[176:179], v[172:175]\n\t" \
  "v_mfma_f32_16x16x32_bf16 v[176:179], v[0:3], a[176:179], v[176:179]\n\t" \
  "v_mfma_f32_16x16x32_bf16 v[180:183], v[4:7], a[176:179], v[180:183]\n\t" \
  "v_mfma_f32_16x16x32_bf16 v[168:171], v[192:195], a[52:55], v[168:171]\n\t" \
  "v_mfma_f32_16x16x32_bf16 v[172:175], v[196:199], a[52:55], v[172:175]\n\t" \
  "v_mfma_f32_16x16x32_bf16 v[176:179], v[200:203], a[52:55], v[176:179]\n\t" \
  "v_mfma_f32_16x16x32_bf16 v[180:183], v[204:207], a[52:55], v[180:183]\n\t" \
  "v_mfma_f32_16x16x32_bf16 v[168:171], v[208:211], a[116:119], v[168:171]\n\t" \
  "v_mfma_f32_16x16x32_bf16 v[172:175], v[212:215], a[116:119], v[172:175]\n\t" \
  "v_mfma_f32_16x16x32_bf16 v[176:179], v[216:219], a[116:119], v[176:179]\n\t" \
  "v_mfma_f32_16x16x32_bf16 v[180:183], v[220:223], a[116:119], v[180:183]\n\t" \
  "v_mfma_f32_16x16x32_bf16 v[168:171], v[224:227], a[180:183], v[168:171]\n\t" \
  "v_mfma_f32_16x16x32_bf16 v[172:175], v[228:231], a[180:183], v[172:175]\n\t" \
  "v_mfma_f32_16x16x32_bf16 v[176:179], v[232:235], a[180:183], v[176:179]\n\t" \
  "v_mfma_f32_16x16x32_bf16 v[180:183], v[236:239], a[180:183], v[180:183]\n\t" \
  "v_mfma_f32_16x16x32_bf16 v[168:171], v[240:243], a[56:59], v[168:171]\n\t" \
  "v_mfma_f32_16x16x32_bf16 v[172:175], v[244:247], a[56:59], v[172:175]\n\t" \
  "v_mfma_f32_16x16x32_bf16 v[176:179], v[248:251], a[56:59], v[176:179]\n\t" \
  "v_mfma_f32_16x16x32_bf16 v[180:183], v[252:255], a[56:59], v[180:183]\n\t" \
  "v_mfma_f32_16x16x32_bf16 v[168:171], v[8:11], a[120:123], v[168:171]\n\t" \
  "v_mfma_f32_16x16x32_bf16 v[172:175], v[12:15], a[120:123], v[172:175]\n\t" \
  "v_mfma_f32_16x16x32_bf16 v[176:179], v[16:19], a[120:123], v[176:179]\n\t" \
  "v_mfma_f32_16x16x32_bf16 v[180:183], v[20:23], a[120:123], v[180:183]\n\t" \
  "s_nop 6\n\t" \
  "ds_write_b128 v63, v[176:179] offset:16384\n\t" \
  "ds_write_b128 v63, v[180:183] offset:17408\n\t" \
  "s_nop 7\n\t" \
  "s_branch Lrsb_%=\n\t" \
  "Lend_%=:\n\t"
#define KERNEL_CLOB "s3", "s4", "s5", "s6", "s7", "s8", "s9", "s10", "s11", "s12", "s13", "s14", "s15", "s16", "s17", "s18", "s19", "s20", "s21", "s22", "s23", "s24", "s25", "s26", "s27", "s28", "s29", "s30", "s31", "s32", "s33", "s34", "s35", "s36", "s37", "s38", "s39", "s40", "s41", "s42", "s43", "s44", "s45", "s46", "s47", "s48", "s49", "s50", "s51", "s52", "s53", "s54", "s55", "s56", "s57", "s58", "s59", "s60", "s61", "s62", "s63", "s64", "s65", "s66", "s67", "s68", "s69", "s70", "s71", "s72", "s73", "s74", "s75", "s76", "s77", "s78", "s79", "s80", "s81", "s82", "s83", "s84", "s85", "s86", "s87", "s88", "s89", "s90", "s91", "s94", "s95", "s96", "s97", "s98", "s99", "v1", "v2", "v3", "v4", "v5", "v6", "v7", "v8", "v9", "v10", "v11", "v12", "v13", "v14", "v15", "v16", "v17", "v18", "v19", "v20", "v21", "v22", "v23", "v24", "v25", "v26", "v27", "v28", "v29", "v30", "v31", "v32", "v33", "v34", "v35", "v36", "v37", "v38", "v39", "v40", "v41", "v42", "v43", "v44", "v45", "v46", "v47", "v48", "v49", "v50", "v51", "v52", "v53", "v54", "v55", "v56", "v57", "v59", "v60", "v61", "v62", "v63", "v64", "v65", "v66", "v67", "v68", "v69", "v70", "v71", "v72", "v73", "v74", "v75", "v76", "v77", "v78", "v79", "v80", "v81", "v82", "v83", "v84", "v85", "v86", "v87", "v88", "v89", "v90", "v91", "v92", "v93", "v94", "v95", "v96", "v97", "v98", "v99", "v100", "v101", "v102", "v103", "v104", "v105", "v106", "v107", "v108", "v109", "v110", "v111", "v112", "v113", "v114", "v115", "v116", "v117", "v118", "v119", "v120", "v121", "v122", "v123", "v124", "v125", "v126", "v127", "v128", "v129", "v130", "v131", "v132", "v133", "v134", "v135", "v136", "v137", "v138", "v139", "v140", "v141", "v142", "v143", "v144", "v145", "v146", "v147", "v148", "v149", "v150", "v151", "v152", "v153", "v154", "v155", "v156", "v157", "v158", "v159", "v160", "v161", "v162", "v163", "v164", "v165", "v166", "v167", "v168", "v169", "v170", "v171", "v172", "v173", "v174", "v175", "v176", "v177", "v178", "v179", "v180", "v181", "v182", "v183", "v184", "v185", "v186", "v187", "v188", "v189", "v190", "v191", "v192", "v193", "v194", "v195", "v196", "v197", "v198", "v199", "v200", "v201", "v202", "v203", "v204", "v205", "v206", "v207", "v208", "v209", "v210", "v211", "v212", "v213", "v214", "v215", "v216", "v217", "v218", "v219", "v220", "v221", "v222", "v223", "v224", "v225", "v226", "v227", "v228", "v229", "v230", "v231", "v232", "v233", "v234", "v235", "v236", "v237", "v238", "v239", "v240", "v241", "v242", "v243", "v244", "v245", "v246", "v247", "v248", "v249", "v250", "v251", "v252", "v253", "v254", "v255"


constexpr unsigned LDS_BYTES = 163840;
struct Args { const void* x; const void* wqkv; const void* wo; const void* wg; const void* wu; const void* wd; void* out; void* ws; };

__global__ __launch_bounds__(256, 1) void fused_block(Args) {
  unsigned tid = threadIdx.x;
  asm volatile(KERNEL_ASM : "+{v0}"(tid) : "{s[0:1]}"(__builtin_amdgcn_kernarg_segment_ptr()), "{s2}"(blockIdx.x) : "memory", "scc", "vcc", "a0", "a255", KERNEL_CLOB);
}
}

extern "C" void kernel_launch(void* const* d_in, const int* in_sizes, int n_in, void* d_out, int out_size, void* d_ws, size_t ws_size, hipStream_t stream) {
  Args a;
  a.x = d_in[0]; a.wqkv = d_in[1]; a.wo = d_in[2]; a.wg = d_in[4]; a.wu = d_in[5]; a.wd = d_in[6]; a.out = d_out; a.ws = d_ws;
  (void)hipFuncSetAttribute((const void*)fused_block, hipFuncAttributeMaxDynamicSharedMemorySize, LDS_BYTES);
  fused_block<<<256, 256, LDS_BYTES, stream>>>(a);
}
```
